```python
import torch

def _softmax(x, axis=-1):
    return torch.softmax(x, dim=axis)

def _gelu(x):
    return torch.nn.functional.gelu(x, approximate='tanh')

def _silu(x):
    return torch.nn.functional.silu(x)

def _sigmoid(x):
    return torch.sigmoid(x)

def _layernorm(x, axis=-1, eps=1e-05):
    m = x.mean(dim=axis, keepdim=True)
    v = x.var(dim=axis, keepdim=True, unbiased=False)
    return (x - m) / torch.sqrt(v + eps)

def _scatter_add(tgt, idx, src, axis=0):
    return tgt.clone().index_add_(axis, idx, src)

def _segment_sum(src, seg, n):
    out = torch.zeros((n,) + src.shape[1:], dtype=src.dtype)
    return out.index_add_(0, seg, src)

def _pad(x, pw):
    flat = tuple((v for pair in reversed(pw) for v in pair))
    return torch.nn.functional.pad(x, flat)
kernel_name = 'sequential_preLN__memory_aug_recurrent_mem_tr__dense_activation_mish__prompt'

def setup_inputs():

    _w = lambda a: (lambda t: t.to(torch.bfloat16) if t.is_floating_point() else t)(torch.as_tensor(a))

    def _A_setup():
        B, S, Dm = (1, 2048, 640)
        H, Dh = (32, 192)
        M, seg = (8, 32)
        seg = min(seg, S)
        S = S // seg * seg
        return {'x': _w((lambda *s: torch.randn(*s, dtype=torch.bfloat16) * 0.05)(B, S, Dm)), 'mem0': _w((lambda *s: torch.randn(*s, dtype=torch.bfloat16) * 0.2)(B, M, Dm)), 'Wq': _w((lambda *s: torch.randn(*s, dtype=torch.bfloat16) * 1.6)(Dm, H, Dh) * Dm ** (-0.5)), 'Wk': _w((lambda *s: torch.randn(*s, dtype=torch.bfloat16) * 1.6)(Dm, H, Dh) * Dm ** (-0.5)), 'Wv': _w((lambda *s: torch.randn(*s, dtype=torch.bfloat16) * 0.2)(Dm, H, Dh) * Dm ** (-0.5)), 'Wo': _w((lambda *s: torch.randn(*s, dtype=torch.bfloat16) * 0.2)(H, Dh, Dm) * (H * Dh) ** (-0.5)), 'seg_len': seg}
    _A_in = _A_setup()

    def _B_setup():
        B, S, D, F = (1, 2048, 640, 1536)
        return {'x': _w((lambda *s: torch.randn(*s, dtype=torch.bfloat16) * 0.05)(B, S, D)), 'w1': _w((lambda *s: torch.randn(*s, dtype=torch.bfloat16) * 0.8)(D, F) * D ** (-0.5)), 'b1': _w((lambda *s: torch.randn(*s, dtype=torch.bfloat16) * 0.2)(F)), 'w2': _w((lambda *s: torch.randn(*s, dtype=torch.bfloat16) * 0.1)(F, D) * F ** (-0.5))}
    _B_in = _B_setup()
    return {'A_x': _A_in['x'], 'A_mem0': _A_in['mem0'], 'A_Wq': _A_in['Wq'], 'A_Wk': _A_in['Wk'], 'A_Wv': _A_in['Wv'], 'A_Wo': _A_in['Wo'], 'A_seg_len': _A_in['seg_len'], 'B_x': _B_in['x'], 'B_w1': _B_in['w1'], 'B_b1': _B_in['b1'], 'B_w2': _B_in['w2']}

def reference(A_x, A_mem0, A_Wq, A_Wk, A_Wv, A_Wo, A_seg_len, B_x, B_w1, B_b1, B_w2):

    def _A_ref(x, mem0, Wq, Wk, Wv, Wo, seg_len):
        B, S, Dm = x.shape
        H, Dh = (Wq.shape[1], Wq.shape[2])
        M = mem0.shape[1]
        scale = Dh ** (-0.5)
        n_seg = S // seg_len
        L = M + seg_len
        idx = torch.arange(L)
        mask = (idx[:, None] < M) | (idx[:, None] >= idx[None, :])
        mem = mem0
        outs = []
        for s in range(n_seg):
            xs = x[:, s * seg_len:(s + 1) * seg_len, :].clone()
            h = torch.concatenate([mem, xs], axis=1)
            q = torch.einsum('btd,dhe->bhte', h, Wq)
            k = torch.einsum('btd,dhe->bhte', h, Wk)
            v = torch.einsum('btd,dhe->bhte', h, Wv)
            scores = torch.einsum('bhqe,bhke->bhqk', q, k) * scale
            scores = torch.where(mask[None, None], scores, -1000000000.0)
            p = _softmax(scores, axis=-1)
            o = torch.einsum('bhqk,bhke->bhqe', p, v)
            o = torch.einsum('bhte,hed->btd', o, Wo)
            mem = o[:, :M, :].clone()
            outs.append(o[:, M:, :])
        return torch.concatenate(outs, axis=1)

    def _B_ref(x, w1, b1, w2):
        h = torch.einsum('bsd,df->bsf', x, w1) + b1
        sp = torch.log1p(torch.exp(-torch.abs(h))) + torch.where(h > 0, h, torch.zeros_like(h))
        act = h * torch.tanh(sp)
        return torch.einsum('bsf,fd->bsd', act, w2)
    x = A_x
    h = x + _A_ref(_layernorm(x, axis=-1), A_mem0, A_Wq, A_Wk, A_Wv, A_Wo, A_seg_len)
    h2 = h + _B_ref(_layernorm(h, axis=-1), B_w1, B_b1, B_w2)
    return h2
```

```cpp
#include <hip/hip_runtime.h>
#include <hip/hip_bf16.h>
#include <stdint.h>

typedef __bf16 v2bf __attribute__((ext_vector_type(2)));
typedef float v2f __attribute__((ext_vector_type(2)));
typedef unsigned v4u __attribute__((ext_vector_type(4)));
#define LAS __attribute__((address_space(3)))
#define DEV __device__ __forceinline__

constexpr int NH = 32, DH = 192, DM = 640, NSEG = 64, FF = 1536;
constexpr size_t MB = 1u << 20;
constexpr size_t O_CRIT = 0, O_CTL = 4 * MB, O_XN = 5 * MB, O_K = 8 * MB, O_Q = 32 * MB, O_V = 56 * MB, O_VO = 80 * MB, O_U = 160 * MB, O_PT = 224 * MB, O_H = 230 * MB, O_ACT = 233 * MB, O_DBG = 240 * MB;
constexpr unsigned F_KQ = O_CTL + 4096, F_VO = O_CTL + 12288, F_PT = O_CTL + 13312, F_H = O_CTL + 14336, F_A = O_CTL + 15360;
constexpr unsigned SENT = 0xFFFFFFFFu, P_OFF = 12288;
constexpr int TAB = 147456;
constexpr unsigned L_PM = 59392, L_IMG = 61440, L_PART = 69632;

DEV unsigned crit_map(unsigned o) { return ((o >> 13) << 14) | (o & 8191u); }
DEV unsigned cvt_pk(float lo, float hi) { v2f x = {lo, hi}; v2bf y = __builtin_convertvector(x, v2bf); return __builtin_bit_cast(unsigned, y); }
DEV unsigned pub(unsigned x) { return x < 0xFFFFFFFEu ? x : 0xFFFFFFFEu; }

struct Params { const void *x, *mem0, *wq, *wk, *wv, *wo, *w1, *b1, *w2; void* out; char* ws; };

#define CLOB "v0", "v1", "v2", "v3", "v4", "v5", "v6", "v7", "v8", "v9", "v10", "v11", "v12", "v13", "v14", "v15", "v16", "v17", "v18", "v19", "v20", "v21", "v22", "v23", "v24", "v25", "v26", "v27", "v28", "v29", "v30", "v31", "v32", "v33", "v34", "v35", "v36", "v37", "v38", "v39", "v40", "v41", "v42", "v43", "v44", "v45", "v46", "v47", "v48", "v49", "v50", "v51", "v52", "v53", "v54", "v55", "v56", "v57", "v58", "v59", "v60", "v61", "v62", "v63", "v64", "v65", "v66", "v67", "v68", "v69", "v70", "v71", "v72", "v73", "v74", "v75", "v76", "v77", "v78", "v79", "v80", "v81", "v82", "v83", "v84", "v85", "v86", "v87", "v88", "v89", "v90", "v91", "v92", "v93", "v94", "v95", "v96", "v97", "v98", "v99", "v100", "v101", "v102", "v103", "v104", "v105", "v106", "v107", "v108", "v109", "v110", "v111", "v112", "v113", "v114", "v115", "v116", "v117", "v118", "v119", "v120", "v121", "v122", "v123", "v124", "v125", "v126", "v127", "v128", "v129", "v130", "v131", "v132", "v133", "v134", "v135", "v136", "v137", "v138", "v139", "v140", "v141", "v142", "v143", "v144", "v145", "v146", "v147", "v148", "v149", "v150", "v151", "v152", "v153", "v154", "v155", "v156", "v157", "v158", "v159", "v160", "v161", "v162", "v163", "v164", "v165", "v166", "v167", "v168", "v169", "v170", "v171", "v172", "v173", "v174", "v175", "v176", "v177", "v178", "v179", "v180", "v181", "v182", "v183", "v184", "v185", "v186", "v187", "v188", "v189", "v190", "v191", "v192", "v193", "v194", "v195", "v196", "v197", "v198", "v199", "v200", "v201", "v202", "v203", "v204", "v205", "v206", "v207", "v208", "v209", "v210", "v211", "v212", "v213", "v214", "v215", "v216", "v217", "v218", "v219", "v220", "v221", "v222", "v223", "v224", "v225", "v226", "v227", "v228", "v229", "v230", "v231", "v232", "v233", "v234", "v235", "v236", "v237", "v238", "v239", "v240", "v241", "v242", "v243", "v244", "v245", "v246", "v247", "v248", "v249", "v250", "v251", "v252", "v253", "v254", "v255", "a0", "a1", "a2", "a3", "a4", "a5", "a6", "a7", "a8", "a9", "a10", "a11", "a12", "a13", "a14", "a15", "a16", "a17", "a18", "a19", "a20", "a21", "a22", "a23", "a24", "a25", "a26", "a27", "a28", "a29", "a30", "a31", "a32", "a33", "a34", "a35", "a36", "a37", "a38", "a39", "a40", "a41", "a42", "a43", "a44", "a45", "a46", "a47", "a48", "a49", "a50", "a51", "a52", "a53", "a54", "a55", "a56", "a57", "a58", "a59", "a60", "a61", "a62", "a63", "a64", "a65", "a66", "a67", "a68", "a69", "a70", "a71", "a72", "a73", "a74", "a75", "a76", "a77", "a78", "a79", "a80", "a81", "a82", "a83", "a84", "a85", "a86", "a87", "a88", "a89", "a90", "a91", "a92", "a93", "a94", "a95", "a96", "a97", "a98", "a99", "a100", "a101", "a102", "a103", "a104", "a105", "a106", "a107", "a108", "a109", "a110", "a111", "a112", "a113", "a114", "a115", "a116", "a117", "a118", "a119", "a120", "a121", "a122", "a123", "a124", "a125", "a126", "a127", "a128", "a129", "a130", "a131", "a132", "a133", "a134", "a135", "a136", "a137", "a138", "a139", "a140", "a141", "a142", "a143", "a144", "a145", "a146", "a147", "a148", "a149", "a150", "a151", "a152", "a153", "a154", "a155", "a156", "a157", "a158", "a159", "a160", "a161", "a162", "a163", "a164", "a165", "a166", "a167", "a168", "a169", "a170", "a171", "a172", "a173", "a174", "a175", "a176", "a177", "a178", "a179", "a180", "a181", "a182", "a183", "a184", "a185", "a186", "a187", "a188", "a189", "a190", "a191", "a192", "a193", "a194", "a195", "a196", "a197", "a198", "a199", "a200", "a201", "a202", "a203", "a204", "a205", "a206", "a207", "a208", "a209", "a210", "a211", "a212", "a213", "a214", "a215", "a216", "a217", "a218", "a219", "a220", "a221", "a222", "a223", "a224", "a225", "a226", "a227", "a228", "a229", "a230", "a231", "a232", "a233", "a234", "a235", "a236", "a237", "a238", "a239", "a240", "a241", "a242", "a243", "a244", "a245", "a246", "a247", "a248", "a249", "a250", "a251", "a252", "a253", "a254", "a255", "s20", "s21", "s22", "s23", "s24", "s25", "s26", "s27", "s28", "s29", "s30", "s31", "s36", "s37", "s38", "s39", "s40", "s41", "s42", "s43", "s44", "s45", "s46", "s47", "s48", "s49", "s50", "s51", "s52", "s53", "s54", "s55", "s56", "s57", "s58", "s59", "s60", "s61", "s62", "s63", "s64", "s65", "s66", "s67", "s68", "s69", "s70", "s71", "s72", "s73", "s74", "s75", "s76", "s77", "s78", "s79", "s80", "s81", "s82", "s83", "s84", "s85", "s86", "s87", "s88", "s89", "s90", "s91", "s92", "s93", "s94", "s95", "s96", "s97", "s98", "s99", "vcc", "scc", "memory"

__global__ void __launch_bounds__(256, 1) k_main(Params p) {
    __shared__ __attribute__((aligned(16))) char smem[163840];
    LAS unsigned* tab = (LAS unsigned*)((LAS char*)smem + TAB) + threadIdx.x;
    const int b = blockIdx.x, wave = __builtin_amdgcn_readfirstlane(threadIdx.x >> 6), lane = threadIdx.x & 63;
    const unsigned par = __builtin_amdgcn_readfirstlane(*(const unsigned*)(p.ws + O_CTL)) << 13;
    const int x = b & 7, r = b >> 3;
    const int kg = lane >> 4, tq = (lane >> 2) & 3, tp = lane & 3, n = lane & 15, g = kg & 1, up = lane >> 5, dup = (lane >> 3) & 1;
    const unsigned idx = (lane & 1) ? 0xEEEEu : 0x4444u;
    const int ch = n ^ (2 * (kg + 4 * ((wave >> 1) & 1))), f = 2 * (tq + 4 * g);
    const unsigned trA = (8 * kg + tq) * 256 + (((4 * wave) ^ f) + (tp >> 1)) * 16 + (tp & 1) * 8;
    const unsigned row = 4 * wave + kg;
    const int rho = lane & 31, rq = rho >> 3, col = 8 * (2 * (rq >> 1) + ((rho >> 2) & 1)) + 4 * (rq & 1) + (rho & 3);
    const int oc = 16 * (wave >> 1) + 8 * up + 4 * (wave & 1);
    const int ch2 = n ^ (4 * kg);
    const unsigned trP = (8 * up + tq) * 256 + ((4 * wave + 2 * g + (tp & 1)) ^ (4 * tq)) * 16 + (tp >> 1) * 8;
    int role = -1, id = 0; const char *p0 = p.ws, *p1 = p.ws;
    if (x < 4) {
        if (r < 8) {
            const int h = x * 8 + r;
            const int fi = 4 * g + 2 * up + dup, fg = (lane >> 1) & 3, fd = lane & 1;
            const unsigned c[15] = {lane * 16u, lane * 8u, idx, L_IMG + n * 16u + kg * 4, L_IMG + (8u * (n >> 2) + (n & 3)) * 16 + kg * 4,
                L_IMG + (8u * kg + tq) * 16 + (tp & 1) * 8, L_PART + (lane & 7) * 72u + kg * 16 + dup * 8, L_PART + fi * 72u + fg * 16 + fd * 8,
                crit_map(P_OFF + (h >> 1) * 1024 + (2 * fi + fd + 16 * (2 * (h & 1) + (fg >> 1))) * 16 + (fg & 1) * 8),
                fd ? 0x40000000u : crit_map(P_OFF + 16384 + h * 128 + fi * 16 + fg * 4),
                ((kg >> 1) * 16u + n) * 16 + g * 8, row * 12288 + ch * 16, row * 12288 + 256 + (ch & 7) * 16 + (ch >> 3), trA, trA ^ 32};
#pragma unroll
            for (int k = 0; k < 15; ++k) tab[k * 256] = c[k];
            role = 0; id = h; p0 = (const char*)p.wq + h * 384; p1 = (const char*)p.wk + h * 384;
        } else if (r < 16) {
            const int h = x * 8 + r - 8;
            const unsigned c[12] = {lane * 16u, lane * 8u, idx, ((n >> 2) * 2u + (n & 1)) * 32 + kg * 8 + (n & 2), (g + 8u * up) * 16384 + h * 256 + n * 16,
                kg * 256u + n * 8, h * 256u + n * 16 + kg * 4,
                row * 12288 + ch * 16, row * 12288 + 256 + (ch & 7) * 16 + (ch >> 3) * (64 * 12288), row * 1280 + ch * 16, trA, trA ^ 32};
#pragma unroll
            for (int k = 0; k < 12; ++k) tab[k * 256] = c[k];
            role = 1; id = h; p0 = (const char*)p.wv + h * 384; p1 = (const char*)p.wo + (size_t)h * DH * DM * 2;
        } else if (r < 26) {
            const int u = x * 10 + r - 16;
            tab[0] = lane * 16u; tab[256] = idx;
            tab[512] = crit_map(u * 256 + (2 * kg + (n & 1)) * 32 + ((n >> 1) & 1) * 16 + (n >> 2) * 4);
            tab[768] = up * 512 + n * 16 + g * 8; tab[1024] = (2 * kg + (lane & 1)) * 128 + (n >> 1) * 16;
            tab[1280] = F_VO + ((u >> 1) & 3) * 128 + (lane & 31) * 4;
            role = wave < 2 ? 2 : -1; id = u;
        } else if (r < 31) {
            const int j = x * 5 + r - 26, u = 2 * j + (col >> 4);
            const unsigned c[8] = {lane * 16u, (unsigned)O_VO + u * 32768 + wave * 8192 + ((col & 15) + 16 * up) * 16, u * 16384u + (8 * wave + up) * 256 + (col & 15) * 16,
                rho * 1280u + j * 64 + oc * 2, (unsigned)O_H + (4 * j + (oc >> 3)) * 512 + rho * 16 + (wave & 1) * 8,
                F_VO + (j & 3) * 128 + (8 * wave + (lane & 7)) * 4, F_PT + wave * 128 + rho * 4, F_H + (j * 4 + wave) * 4};
#pragma unroll
            for (int k = 0; k < 8; ++k) tab[k * 256] = c[k];
            role = 5; id = j; p0 = (const char*)p.x;
        }
    }
    else if (r < 24) {
        const int h = (x - 4) * 8 + r / 3, part = r % 3;
        const unsigned trN = (8 * up + tq) * 256 + ((4 * wave + 2 * g + (tp >> 1)) ^ (4 * tq)) * 16 + (tp & 1) * 8;
        const char *wq = (const char*)p.wq + h * 384 + (size_t)row * 12288, *wk = (const char*)p.wk + h * 384 + (size_t)row * 12288, *wv = (const char*)p.wv + h * 384 + (size_t)row * 12288;
        id = h;
        if (part < 2) {
            const uintptr_t pa = (uintptr_t)(part == 0 ? wq + ch2 * 16 : wk + 128 + ch2 * 16);
            const uintptr_t pb = (uintptr_t)(part == 0 ? (ch2 < 8 ? wq + 256 + ch2 * 16 : wk + (ch2 - 8) * 16) : wv + ch2 * 16);
            unsigned st[4];
#pragma unroll
            for (int t = 0; t < 2; ++t) {
                const int gt = part * 8 + t * 4 + wave, mat = gt / 6, tau = gt % 6;
                const unsigned base = mat == 0 ? O_Q : mat == 1 ? O_K : O_V;
                const unsigned o = mat == 2 ? tau * 2048 + lane * 16 : tau < 4 ? tau * 2048 + g * 1024 + n * 16 + up * 256 : 8192 + (tau - 4) * 2048 + g * 512 + up * 256 + n * 16;
                st[2 * t] = base + o; st[2 * t + 1] = base + o + (mat != 2 && tau < 4 ? 512 : 1024);
            }
            const unsigned c[11] = {lane * 16u, trP, st[0], st[1], st[2], st[3], F_KQ + h * 128 + (part * 4 + wave) * 4, (unsigned)pa, (unsigned)(pa >> 32), (unsigned)pb, (unsigned)(pb >> 32)};
#pragma unroll
            for (int k = 0; k < 11; ++k) tab[k * 256] = c[k];
            role = 3;
        } else {
            const uintptr_t pv = (uintptr_t)(wv + 256 + (ch & 7) * 16 + (size_t)(ch >> 3) * 64 * 12288);
            const uintptr_t po = (uintptr_t)((const char*)p.wo + ((size_t)h * DH + row) * 1280 + ch2 * 16);
            const unsigned trB = (8 * kg + tq) * 256 + (((2 * wave) ^ f) + (tp >> 1)) * 16 + (tp & 1) * 8;
            const int tok = 16 * (rq & 1) + 4 * (2 * (rq >> 1) + ((rho >> 2) & 1)) + (rho & 3);
            const unsigned c[14] = {lane * 16u, trB, trB ^ 128, trN, kg * 512u + n * 16, 81920u + 8192 + wave * 1024 + up * 512 + n * 16 + g * 8, up * 512u + tok * 16,
                (unsigned)O_VO + (2 * wave + g) * 32768 + h * 1024 + up * 256 + n * 16, F_VO + wave * 128 + h * 4, F_KQ + h * 128 + 16 + (lane & 3) * 4,
                (unsigned)pv, (unsigned)(pv >> 32), (unsigned)po, (unsigned)(po >> 32)};
#pragma unroll
            for (int k = 0; k < 14; ++k) tab[k * 256] = c[k];
            role = 4;
        }
    } else if (r < 27) {
        const int fb = (x - 4) * 3 + r - 24;
        const uintptr_t pa = (uintptr_t)((const char*)p.w1 + (size_t)row * 3072 + fb * 256 + ch2 * 16);
        const unsigned c[8] = {lane * 16u, trP, (unsigned)O_ACT + (4 * fb + wave) * 2048 + lane * 16, F_A + (fb * 4 + wave) * 4, F_H + (lane < 40 ? lane : 39) * 8,
            (128u * fb + 32 * wave + 8 * up) * 2, (unsigned)pa, (unsigned)(pa >> 32)};
#pragma unroll
        for (int k = 0; k < 8; ++k) tab[k * 256] = c[k];
        role = 6; id = fb; p1 = (const char*)p.b1;
    } else {
        const int j = (x - 4) * 5 + r - 27;
        const uintptr_t pa = (uintptr_t)((const char*)p.w2 + (size_t)(384 * (ch2 >> 2) + row) * 1280 + j * 64 + (ch2 & 3) * 16);
        const unsigned c[7] = {lane * 16u, trP, (unsigned)O_H + (4 * j + (oc >> 3)) * 512 + rho * 16 + (wave & 1) * 8, rho * 1280u + j * 64 + oc * 2,
            F_A + (12 * wave + (lane < 12 ? lane : 11)) * 4, (unsigned)pa, (unsigned)(pa >> 32)};
#pragma unroll
        for (int k = 0; k < 7; ++k) tab[k * 256] = c[k];
        role = 7; id = j; p0 = (const char*)p.out;
    }
    if (role < 0) return;
    asm volatile(R"ASM(
s_cmp_eq_u32 %6, 1
s_cbranch_scc1 ROLE_SV
s_cmp_eq_u32 %6, 2
s_cbranch_scc1 ROLE_S2
s_cmp_eq_u32 %6, 3
s_cbranch_scc1 ROLE_PA
s_cmp_eq_u32 %6, 4
s_cbranch_scc1 ROLE_PC
s_cmp_eq_u32 %6, 5
s_cbranch_scc1 ROLE_S3
s_cmp_eq_u32 %6, 6
s_cbranch_scc1 ROLE_F1
s_cmp_eq_u32 %6, 7
s_cbranch_scc1 ROLE_F2
ROLE_S1:
s_mov_b64 s[48:49], %0
s_mov_b64 s[50:51], %1
s_mov_b64 s[52:53], %2
s_mov_b32 s54, %3
s_mov_b32 s40, %4
s_mov_b32 s39, %5
s_mov_b32 s41, 0x200000
s_mov_b32 s36, 0
s_lshl_b32 s55, s40, 10
v_mbcnt_lo_u32_b32 v31, -1, 0
v_mbcnt_hi_u32_b32 v31, -1, v31
s_lshl_b32 s42, s40, 8
s_add_u32 s42, s42, 147456
v_lshl_add_u32 v31, v31, 2, s42
ds_read_b32 v116, v31 offset:0
ds_read_b32 v118, v31 offset:1024
ds_read_b32 v119, v31 offset:2048
ds_read_b32 v120, v31 offset:3072
ds_read_b32 v121, v31 offset:4096
ds_read_b32 v122, v31 offset:5120
ds_read_b32 v123, v31 offset:6144
ds_read_b32 v124, v31 offset:7168
ds_read_b32 v125, v31 offset:8192
ds_read_b32 v126, v31 offset:9216
ds_read_b32 v127, v31 offset:10240
ds_read_b32 v0, v31 offset:11264
ds_read_b32 v1, v31 offset:12288
ds_read_b32 v3, v31 offset:13312
ds_read_b32 v4, v31 offset:14336
s_waitcnt lgkmcnt(0)
s_memrealtime s[94:95]
s_waitcnt lgkmcnt(0)
s_mov_b32 s92, s94
s_add_u32 s20, s48, 0x0
s_addc_u32 s21, s49, 0
s_and_b32 s21, s21, 0xffff
s_mov_b32 s22, 0x400000
s_mov_b32 s23, 0x20000
s_add_u32 s96, s48, 0x0
s_addc_u32 s97, s49, 0
s_and_b32 s97, s97, 0xffff
s_mov_b32 s98, 0xfe00000
s_mov_b32 s99, 0x20000
v_add_u32 v117, 98304, v116
v_add_u32 v7, s55, v116
v_add_u32 v7, s55, v7
v_add_u32 v7, s55, v7
v_add_u32 v7, s55, v7
v_add_co_u32 v8, vcc, s50, v0
v_mov_b32 v9, s51
s_nop 0
v_addc_co_u32 v9, vcc, 0, v9, vcc
s_nop 1
v_add_co_u32 v12, vcc, s52, v0
v_mov_b32 v13, s53
s_nop 0
v_addc_co_u32 v13, vcc, 0, v13, vcc
v_and_b32 v2, 1, v1
v_and_b32 v1, -2, v1
v_cmp_eq_u32 s[76:77], 1, v2
v_add_co_u32 v10, vcc, s50, v1
v_mov_b32 v11, s51
s_nop 0
v_addc_co_u32 v11, vcc, 0, v11, vcc
s_nop 1
v_add_co_u32 v14, vcc, s52, v1
v_mov_b32 v15, s53
s_nop 0
v_addc_co_u32 v15, vcc, 0, v15, vcc
v_cndmask_b32 v10, v10, v14, s[76:77]
v_cndmask_b32 v11, v11, v15, s[76:77]
s_lshl_b32 s42, s40, 6
s_lshl_b32 s43, s40, 5
s_xor_b32 s42, s42, s43
v_xor_b32 v5, s42, v3
v_xor_b32 v6, 128, v5
s_mov_b32 s44, 196608
s_mov_b32 s45, 0
s_add_u32 m0, s55, 0
s_nop 0
global_load_lds_dwordx4 v[8:9], off
v_lshl_add_u64 v[8:9], v[8:9], 0, s[44:45]
s_add_u32 m0, s55, 4096
s_nop 0
global_load_lds_dwordx4 v[8:9], off
v_lshl_add_u64 v[8:9], v[8:9], 0, s[44:45]
s_add_u32 m0, s55, 8192
s_nop 0
global_load_lds_dwordx4 v[8:9], off
v_lshl_add_u64 v[8:9], v[8:9], 0, s[44:45]
s_add_u32 m0, s55, 12288
s_nop 0
global_load_lds_dwordx4 v[8:9], off
v_lshl_add_u64 v[8:9], v[8:9], 0, s[44:45]
s_add_u32 m0, s55, 16384
s_nop 0
global_load_lds_dwordx4 v[10:11], off
v_lshl_add_u64 v[10:11], v[10:11], 0, s[44:45]
s_add_u32 m0, s55, 20480
s_nop 0
global_load_lds_dwordx4 v[10:11], off
v_lshl_add_u64 v[10:11], v[10:11], 0, s[44:45]
s_add_u32 m0, s55, 24576
s_nop 0
global_load_lds_dwordx4 v[10:11], off
v_lshl_add_u64 v[10:11], v[10:11], 0, s[44:45]
s_add_u32 m0, s55, 28672
s_nop 0
global_load_lds_dwordx4 v[10:11], off
v_lshl_add_u64 v[10:11], v[10:11], 0, s[44:45]
s_add_u32 m0, s55, 32768
s_nop 0
global_load_lds_dwordx4 v[12:13], off
v_lshl_add_u64 v[12:13], v[12:13], 0, s[44:45]
s_add_u32 m0, s55, 36864
s_nop 0
global_load_lds_dwordx4 v[12:13], off
v_lshl_add_u64 v[12:13], v[12:13], 0, s[44:45]
s_add_u32 m0, s55, 40960
s_nop 0
global_load_lds_dwordx4 v[12:13], off
v_lshl_add_u64 v[12:13], v[12:13], 0, s[44:45]
s_add_u32 m0, s55, 45056
s_nop 0
global_load_lds_dwordx4 v[12:13], off
v_lshl_add_u64 v[12:13], v[12:13], 0, s[44:45]
s_add_u32 m0, s55, 49152
s_nop 0
global_load_lds_dwordx4 v[8:9], off
v_lshl_add_u64 v[8:9], v[8:9], 0, s[44:45]
s_add_u32 m0, s55, 53248
s_nop 0
global_load_lds_dwordx4 v[8:9], off
v_lshl_add_u64 v[8:9], v[8:9], 0, s[44:45]
s_add_u32 m0, s55, 57344
s_nop 0
global_load_lds_dwordx4 v[8:9], off
v_lshl_add_u64 v[8:9], v[8:9], 0, s[44:45]
s_add_u32 m0, s55, 61440
s_nop 0
global_load_lds_dwordx4 v[8:9], off
v_lshl_add_u64 v[8:9], v[8:9], 0, s[44:45]
s_add_u32 m0, s55, 65536
s_nop 0
global_load_lds_dwordx4 v[10:11], off
v_lshl_add_u64 v[10:11], v[10:11], 0, s[44:45]
s_add_u32 m0, s55, 69632
s_nop 0
global_load_lds_dwordx4 v[10:11], off
v_lshl_add_u64 v[10:11], v[10:11], 0, s[44:45]
s_add_u32 m0, s55, 73728
s_nop 0
global_load_lds_dwordx4 v[10:11], off
v_lshl_add_u64 v[10:11], v[10:11], 0, s[44:45]
s_add_u32 m0, s55, 77824
s_nop 0
global_load_lds_dwordx4 v[10:11], off
v_lshl_add_u64 v[10:11], v[10:11], 0, s[44:45]
s_add_u32 m0, s55, 81920
s_nop 0
global_load_lds_dwordx4 v[12:13], off
v_lshl_add_u64 v[12:13], v[12:13], 0, s[44:45]
s_add_u32 m0, s55, 86016
s_nop 0
global_load_lds_dwordx4 v[12:13], off
v_lshl_add_u64 v[12:13], v[12:13], 0, s[44:45]
s_add_u32 m0, s55, 90112
s_nop 0
global_load_lds_dwordx4 v[12:13], off
v_lshl_add_u64 v[12:13], v[12:13], 0, s[44:45]
s_add_u32 m0, s55, 94208
s_nop 0
global_load_lds_dwordx4 v[12:13], off
v_lshl_add_u64 v[12:13], v[12:13], 0, s[44:45]
s_add_u32 m0, s55, 98304
s_nop 0
global_load_lds_dwordx4 v[8:9], off
v_lshl_add_u64 v[8:9], v[8:9], 0, s[44:45]
s_add_u32 m0, s55, 102400
s_nop 0
global_load_lds_dwordx4 v[8:9], off
v_lshl_add_u64 v[8:9], v[8:9], 0, s[44:45]
s_add_u32 m0, s55, 106496
s_nop 0
global_load_lds_dwordx4 v[8:9], off
v_lshl_add_u64 v[8:9], v[8:9], 0, s[44:45]
s_add_u32 m0, s55, 110592
s_nop 0
global_load_lds_dwordx4 v[8:9], off
v_lshl_add_u64 v[8:9], v[8:9], 0, s[44:45]
s_add_u32 m0, s55, 114688
s_nop 0
global_load_lds_dwordx4 v[10:11], off
v_lshl_add_u64 v[10:11], v[10:11], 0, s[44:45]
s_add_u32 m0, s55, 118784
s_nop 0
global_load_lds_dwordx4 v[10:11], off
v_lshl_add_u64 v[10:11], v[10:11], 0, s[44:45]
s_add_u32 m0, s55, 122880
s_nop 0
global_load_lds_dwordx4 v[10:11], off
v_lshl_add_u64 v[10:11], v[10:11], 0, s[44:45]
s_add_u32 m0, s55, 126976
s_nop 0
global_load_lds_dwordx4 v[10:11], off
v_lshl_add_u64 v[10:11], v[10:11], 0, s[44:45]
s_waitcnt vmcnt(28)
s_waitcnt lgkmcnt(0)
s_barrier
s_add_u32 m0, s55, 131072
s_nop 0
global_load_lds_dwordx4 v[12:13], off
v_lshl_add_u64 v[12:13], v[12:13], 0, s[44:45]
s_add_u32 m0, s55, 135168
s_nop 0
global_load_lds_dwordx4 v[12:13], off
v_lshl_add_u64 v[12:13], v[12:13], 0, s[44:45]
s_add_u32 m0, s55, 139264
s_nop 0
global_load_lds_dwordx4 v[12:13], off
v_lshl_add_u64 v[12:13], v[12:13], 0, s[44:45]
s_add_u32 m0, s55, 143360
s_nop 0
global_load_lds_dwordx4 v[12:13], off
v_lshl_add_u64 v[12:13], v[12:13], 0, s[44:45]
v_add_u32 v16, 0, v3
ds_read_b64_tr_b16 a[0:1], v16 offset:0
ds_read_b64_tr_b16 a[2:3], v16 offset:1024
ds_read_b64_tr_b16 a[4:5], v16 offset:8192
ds_read_b64_tr_b16 a[6:7], v16 offset:9216
v_add_u32 v17, 0, v4
ds_read_b64_tr_b16 a[8:9], v17 offset:0
ds_read_b64_tr_b16 a[10:11], v17 offset:1024
ds_read_b64_tr_b16 a[12:13], v17 offset:8192
ds_read_b64_tr_b16 a[14:15], v17 offset:9216
s_waitcnt vmcnt(28)
s_waitcnt lgkmcnt(0)
s_barrier
s_add_u32 m0, s55, 0
s_nop 0
global_load_lds_dwordx4 v[8:9], off
v_lshl_add_u64 v[8:9], v[8:9], 0, s[44:45]
s_add_u32 m0, s55, 4096
s_nop 0
global_load_lds_dwordx4 v[8:9], off
v_lshl_add_u64 v[8:9], v[8:9], 0, s[44:45]
s_add_u32 m0, s55, 8192
s_nop 0
global_load_lds_dwordx4 v[8:9], off
v_lshl_add_u64 v[8:9], v[8:9], 0, s[44:45]
s_add_u32 m0, s55, 12288
s_nop 0
global_load_lds_dwordx4 v[8:9], off
v_lshl_add_u64 v[8:9], v[8:9], 0, s[44:45]
v_add_u32 v16, 16384, v5
ds_read_b64_tr_b16 a[16:17], v16 offset:0
ds_read_b64_tr_b16 a[18:19], v16 offset:1024
ds_read_b64_tr_b16 a[20:21], v16 offset:8192
ds_read_b64_tr_b16 a[22:23], v16 offset:9216
v_add_u32 v17, 16384, v6
ds_read_b64_tr_b16 a[24:25], v17 offset:0
ds_read_b64_tr_b16 a[26:27], v17 offset:1024
ds_read_b64_tr_b16 a[28:29], v17 offset:8192
ds_read_b64_tr_b16 a[30:31], v17 offset:9216
s_waitcnt vmcnt(28)
s_waitcnt lgkmcnt(0)
s_barrier
s_add_u32 m0, s55, 16384
s_nop 0
global_load_lds_dwordx4 v[10:11], off
v_lshl_add_u64 v[10:11], v[10:11], 0, s[44:45]
s_add_u32 m0, s55, 20480
s_nop 0
global_load_lds_dwordx4 v[10:11], off
v_lshl_add_u64 v[10:11], v[10:11], 0, s[44:45]
s_add_u32 m0, s55, 24576
s_nop 0
global_load_lds_dwordx4 v[10:11], off
v_lshl_add_u64 v[10:11], v[10:11], 0, s[44:45]
s_add_u32 m0, s55, 28672
s_nop 0
global_load_lds_dwordx4 v[10:11], off
v_lshl_add_u64 v[10:11], v[10:11], 0, s[44:45]
v_add_u32 v16, 32768, v3
ds_read_b64_tr_b16 a[32:33], v16 offset:0
ds_read_b64_tr_b16 a[34:35], v16 offset:1024
ds_read_b64_tr_b16 a[36:37], v16 offset:8192
ds_read_b64_tr_b16 a[38:39], v16 offset:9216
v_add_u32 v17, 32768, v4
ds_read_b64_tr_b16 a[40:41], v17 offset:0
ds_read_b64_tr_b16 a[42:43], v17 offset:1024
ds_read_b64_tr_b16 a[44:45], v17 offset:8192
ds_read_b64_tr_b16 a[46:47], v17 offset:9216
s_waitcnt vmcnt(28)
s_waitcnt lgkmcnt(0)
s_barrier
s_add_u32 m0, s55, 32768
s_nop 0
global_load_lds_dwordx4 v[12:13], off
v_lshl_add_u64 v[12:13], v[12:13], 0, s[44:45]
s_add_u32 m0, s55, 36864
s_nop 0
global_load_lds_dwordx4 v[12:13], off
v_lshl_add_u64 v[12:13], v[12:13], 0, s[44:45]
s_add_u32 m0, s55, 40960
s_nop 0
global_load_lds_dwordx4 v[12:13], off
v_lshl_add_u64 v[12:13], v[12:13], 0, s[44:45]
s_add_u32 m0, s55, 45056
s_nop 0
global_load_lds_dwordx4 v[12:13], off
v_lshl_add_u64 v[12:13], v[12:13], 0, s[44:45]
v_add_u32 v16, 49152, v3
ds_read_b64_tr_b16 a[48:49], v16 offset:0
ds_read_b64_tr_b16 a[50:51], v16 offset:1024
ds_read_b64_tr_b16 a[52:53], v16 offset:8192
ds_read_b64_tr_b16 a[54:55], v16 offset:9216
v_add_u32 v17, 49152, v4
ds_read_b64_tr_b16 a[56:57], v17 offset:0
ds_read_b64_tr_b16 a[58:59], v17 offset:1024
ds_read_b64_tr_b16 a[60:61], v17 offset:8192
ds_read_b64_tr_b16 a[62:63], v17 offset:9216
s_waitcnt vmcnt(28)
s_waitcnt lgkmcnt(0)
s_barrier
s_add_u32 m0, s55, 49152
s_nop 0
global_load_lds_dwordx4 v[8:9], off
v_lshl_add_u64 v[8:9], v[8:9], 0, s[44:45]
s_add_u32 m0, s55, 53248
s_nop 0
global_load_lds_dwordx4 v[8:9], off
v_lshl_add_u64 v[8:9], v[8:9], 0, s[44:45]
s_add_u32 m0, s55, 57344
s_nop 0
global_load_lds_dwordx4 v[8:9], off
v_lshl_add_u64 v[8:9], v[8:9], 0, s[44:45]
s_add_u32 m0, s55, 61440
s_nop 0
global_load_lds_dwordx4 v[8:9], off
v_lshl_add_u64 v[8:9], v[8:9], 0, s[44:45]
v_add_u32 v16, 65536, v5
ds_read_b64_tr_b16 a[64:65], v16 offset:0
ds_read_b64_tr_b16 a[66:67], v16 offset:1024
ds_read_b64_tr_b16 a[68:69], v16 offset:8192
ds_read_b64_tr_b16 a[70:71], v16 offset:9216
v_add_u32 v17, 65536, v6
ds_read_b64_tr_b16 a[72:73], v17 offset:0
ds_read_b64_tr_b16 a[74:75], v17 offset:1024
ds_read_b64_tr_b16 a[76:77], v17 offset:8192
ds_read_b64_tr_b16 a[78:79], v17 offset:9216
s_waitcnt vmcnt(28)
s_waitcnt lgkmcnt(0)
s_barrier
s_add_u32 m0, s55, 65536
s_nop 0
global_load_lds_dwordx4 v[10:11], off
v_lshl_add_u64 v[10:11], v[10:11], 0, s[44:45]
s_add_u32 m0, s55, 69632
s_nop 0
global_load_lds_dwordx4 v[10:11], off
v_lshl_add_u64 v[10:11], v[10:11], 0, s[44:45]
s_add_u32 m0, s55, 73728
s_nop 0
global_load_lds_dwordx4 v[10:11], off
v_lshl_add_u64 v[10:11], v[10:11], 0, s[44:45]
s_add_u32 m0, s55, 77824
s_nop 0
global_load_lds_dwordx4 v[10:11], off
v_lshl_add_u64 v[10:11], v[10:11], 0, s[44:45]
v_add_u32 v16, 81920, v3
ds_read_b64_tr_b16 a[80:81], v16 offset:0
ds_read_b64_tr_b16 a[82:83], v16 offset:1024
ds_read_b64_tr_b16 a[84:85], v16 offset:8192
ds_read_b64_tr_b16 a[86:87], v16 offset:9216
v_add_u32 v17, 81920, v4
ds_read_b64_tr_b16 a[88:89], v17 offset:0
ds_read_b64_tr_b16 a[90:91], v17 offset:1024
ds_read_b64_tr_b16 a[92:93], v17 offset:8192
ds_read_b64_tr_b16 a[94:95], v17 offset:9216
s_waitcnt vmcnt(28)
s_waitcnt lgkmcnt(0)
s_barrier
s_add_u32 m0, s55, 81920
s_nop 0
global_load_lds_dwordx4 v[12:13], off
v_lshl_add_u64 v[12:13], v[12:13], 0, s[44:45]
s_add_u32 m0, s55, 86016
s_nop 0
global_load_lds_dwordx4 v[12:13], off
v_lshl_add_u64 v[12:13], v[12:13], 0, s[44:45]
s_add_u32 m0, s55, 90112
s_nop 0
global_load_lds_dwordx4 v[12:13], off
v_lshl_add_u64 v[12:13], v[12:13], 0, s[44:45]
s_add_u32 m0, s55, 94208
s_nop 0
global_load_lds_dwordx4 v[12:13], off
v_lshl_add_u64 v[12:13], v[12:13], 0, s[44:45]
v_add_u32 v16, 98304, v3
ds_read_b64_tr_b16 a[96:97], v16 offset:0
ds_read_b64_tr_b16 a[98:99], v16 offset:1024
ds_read_b64_tr_b16 a[100:101], v16 offset:8192
ds_read_b64_tr_b16 a[102:103], v16 offset:9216
v_add_u32 v17, 98304, v4
ds_read_b64_tr_b16 a[104:105], v17 offset:0
ds_read_b64_tr_b16 a[106:107], v17 offset:1024
ds_read_b64_tr_b16 a[108:109], v17 offset:8192
ds_read_b64_tr_b16 a[110:111], v17 offset:9216
s_waitcnt vmcnt(28)
s_waitcnt lgkmcnt(0)
s_barrier
s_add_u32 m0, s55, 98304
s_nop 0
global_load_lds_dwordx4 v[8:9], off
v_lshl_add_u64 v[8:9], v[8:9], 0, s[44:45]
s_add_u32 m0, s55, 102400
s_nop 0
global_load_lds_dwordx4 v[8:9], off
v_lshl_add_u64 v[8:9], v[8:9], 0, s[44:45]
s_add_u32 m0, s55, 106496
s_nop 0
global_load_lds_dwordx4 v[8:9], off
v_lshl_add_u64 v[8:9], v[8:9], 0, s[44:45]
s_add_u32 m0, s55, 110592
s_nop 0
global_load_lds_dwordx4 v[8:9], off
v_lshl_add_u64 v[8:9], v[8:9], 0, s[44:45]
v_add_u32 v16, 114688, v5
ds_read_b64_tr_b16 a[112:113], v16 offset:0
ds_read_b64_tr_b16 a[114:115], v16 offset:1024
ds_read_b64_tr_b16 a[116:117], v16 offset:8192
ds_read_b64_tr_b16 a[118:119], v16 offset:9216
v_add_u32 v17, 114688, v6
ds_read_b64_tr_b16 a[120:121], v17 offset:0
ds_read_b64_tr_b16 a[122:123], v17 offset:1024
ds_read_b64_tr_b16 a[124:125], v17 offset:8192
ds_read_b64_tr_b16 a[126:127], v17 offset:9216
s_waitcnt vmcnt(28)
s_waitcnt lgkmcnt(0)
s_barrier
s_add_u32 m0, s55, 114688
s_nop 0
global_load_lds_dwordx4 v[10:11], off
v_lshl_add_u64 v[10:11], v[10:11], 0, s[44:45]
s_add_u32 m0, s55, 118784
s_nop 0
global_load_lds_dwordx4 v[10:11], off
v_lshl_add_u64 v[10:11], v[10:11], 0, s[44:45]
s_add_u32 m0, s55, 122880
s_nop 0
global_load_lds_dwordx4 v[10:11], off
v_lshl_add_u64 v[10:11], v[10:11], 0, s[44:45]
s_add_u32 m0, s55, 126976
s_nop 0
global_load_lds_dwordx4 v[10:11], off
v_lshl_add_u64 v[10:11], v[10:11], 0, s[44:45]
v_add_u32 v16, 131072, v3
ds_read_b64_tr_b16 a[128:129], v16 offset:0
ds_read_b64_tr_b16 a[130:131], v16 offset:1024
ds_read_b64_tr_b16 a[132:133], v16 offset:8192
ds_read_b64_tr_b16 a[134:135], v16 offset:9216
v_add_u32 v17, 131072, v4
ds_read_b64_tr_b16 a[136:137], v17 offset:0
ds_read_b64_tr_b16 a[138:139], v17 offset:1024
ds_read_b64_tr_b16 a[140:141], v17 offset:8192
ds_read_b64_tr_b16 a[142:143], v17 offset:9216
s_waitcnt vmcnt(28)
s_waitcnt lgkmcnt(0)
s_barrier
s_add_u32 m0, s55, 131072
s_nop 0
global_load_lds_dwordx4 v[12:13], off
v_lshl_add_u64 v[12:13], v[12:13], 0, s[44:45]
s_add_u32 m0, s55, 135168
s_nop 0
global_load_lds_dwordx4 v[12:13], off
v_lshl_add_u64 v[12:13], v[12:13], 0, s[44:45]
s_add_u32 m0, s55, 139264
s_nop 0
global_load_lds_dwordx4 v[12:13], off
v_lshl_add_u64 v[12:13], v[12:13], 0, s[44:45]
s_add_u32 m0, s55, 143360
s_nop 0
global_load_lds_dwordx4 v[12:13], off
v_lshl_add_u64 v[12:13], v[12:13], 0, s[44:45]
v_add_u32 v16, 0, v3
ds_read_b64_tr_b16 a[144:145], v16 offset:0
ds_read_b64_tr_b16 a[146:147], v16 offset:1024
ds_read_b64_tr_b16 a[148:149], v16 offset:8192
ds_read_b64_tr_b16 a[150:151], v16 offset:9216
v_add_u32 v17, 0, v4
ds_read_b64_tr_b16 a[152:153], v17 offset:0
ds_read_b64_tr_b16 a[154:155], v17 offset:1024
ds_read_b64_tr_b16 a[156:157], v17 offset:8192
ds_read_b64_tr_b16 a[158:159], v17 offset:9216
s_waitcnt vmcnt(28)
s_waitcnt lgkmcnt(0)
s_barrier
s_add_u32 m0, s55, 0
s_nop 0
global_load_lds_dwordx4 v[8:9], off
v_lshl_add_u64 v[8:9], v[8:9], 0, s[44:45]
s_add_u32 m0, s55, 4096
s_nop 0
global_load_lds_dwordx4 v[8:9], off
v_lshl_add_u64 v[8:9], v[8:9], 0, s[44:45]
s_add_u32 m0, s55, 8192
s_nop 0
global_load_lds_dwordx4 v[8:9], off
v_lshl_add_u64 v[8:9], v[8:9], 0, s[44:45]
s_add_u32 m0, s55, 12288
s_nop 0
global_load_lds_dwordx4 v[8:9], off
v_lshl_add_u64 v[8:9], v[8:9], 0, s[44:45]
v_add_u32 v16, 16384, v5
ds_read_b64_tr_b16 a[160:161], v16 offset:0
ds_read_b64_tr_b16 a[162:163], v16 offset:1024
ds_read_b64_tr_b16 a[164:165], v16 offset:8192
ds_read_b64_tr_b16 a[166:167], v16 offset:9216
v_add_u32 v17, 16384, v6
ds_read_b64_tr_b16 a[168:169], v17 offset:0
ds_read_b64_tr_b16 a[170:171], v17 offset:1024
ds_read_b64_tr_b16 a[172:173], v17 offset:8192
ds_read_b64_tr_b16 a[174:175], v17 offset:9216
s_waitcnt vmcnt(28)
s_waitcnt lgkmcnt(0)
s_barrier
s_add_u32 m0, s55, 16384
s_nop 0
global_load_lds_dwordx4 v[10:11], off
v_lshl_add_u64 v[10:11], v[10:11], 0, s[44:45]
s_add_u32 m0, s55, 20480
s_nop 0
global_load_lds_dwordx4 v[10:11], off
v_lshl_add_u64 v[10:11], v[10:11], 0, s[44:45]
s_add_u32 m0, s55, 24576
s_nop 0
global_load_lds_dwordx4 v[10:11], off
v_lshl_add_u64 v[10:11], v[10:11], 0, s[44:45]
s_add_u32 m0, s55, 28672
s_nop 0
global_load_lds_dwordx4 v[10:11], off
v_lshl_add_u64 v[10:11], v[10:11], 0, s[44:45]
v_add_u32 v16, 32768, v3
ds_read_b64_tr_b16 a[176:177], v16 offset:0
ds_read_b64_tr_b16 a[178:179], v16 offset:1024
ds_read_b64_tr_b16 a[180:181], v16 offset:8192
ds_read_b64_tr_b16 a[182:183], v16 offset:9216
v_add_u32 v17, 32768, v4
ds_read_b64_tr_b16 a[184:185], v17 offset:0
ds_read_b64_tr_b16 a[186:187], v17 offset:1024
ds_read_b64_tr_b16 a[188:189], v17 offset:8192
ds_read_b64_tr_b16 a[190:191], v17 offset:9216
s_waitcnt vmcnt(28)
s_waitcnt lgkmcnt(0)
s_barrier
s_add_u32 m0, s55, 32768
s_nop 0
global_load_lds_dwordx4 v[12:13], off
v_lshl_add_u64 v[12:13], v[12:13], 0, s[44:45]
s_add_u32 m0, s55, 36864
s_nop 0
global_load_lds_dwordx4 v[12:13], off
v_lshl_add_u64 v[12:13], v[12:13], 0, s[44:45]
s_add_u32 m0, s55, 40960
s_nop 0
global_load_lds_dwordx4 v[12:13], off
v_lshl_add_u64 v[12:13], v[12:13], 0, s[44:45]
s_add_u32 m0, s55, 45056
s_nop 0
global_load_lds_dwordx4 v[12:13], off
v_lshl_add_u64 v[12:13], v[12:13], 0, s[44:45]
v_add_u32 v16, 49152, v3
ds_read_b64_tr_b16 a[192:193], v16 offset:0
ds_read_b64_tr_b16 a[194:195], v16 offset:1024
ds_read_b64_tr_b16 a[196:197], v16 offset:8192
ds_read_b64_tr_b16 a[198:199], v16 offset:9216
v_add_u32 v17, 49152, v4
ds_read_b64_tr_b16 a[200:201], v17 offset:0
ds_read_b64_tr_b16 a[202:203], v17 offset:1024
ds_read_b64_tr_b16 a[204:205], v17 offset:8192
ds_read_b64_tr_b16 a[206:207], v17 offset:9216
s_waitcnt vmcnt(28)
s_waitcnt lgkmcnt(0)
s_barrier
s_add_u32 m0, s55, 49152
s_nop 0
global_load_lds_dwordx4 v[8:9], off
v_lshl_add_u64 v[8:9], v[8:9], 0, s[44:45]
s_add_u32 m0, s55, 53248
s_nop 0
global_load_lds_dwordx4 v[8:9], off
v_lshl_add_u64 v[8:9], v[8:9], 0, s[44:45]
s_add_u32 m0, s55, 57344
s_nop 0
global_load_lds_dwordx4 v[8:9], off
v_lshl_add_u64 v[8:9], v[8:9], 0, s[44:45]
s_add_u32 m0, s55, 61440
s_nop 0
global_load_lds_dwordx4 v[8:9], off
v_lshl_add_u64 v[8:9], v[8:9], 0, s[44:45]
v_add_u32 v16, 65536, v5
ds_read_b64_tr_b16 a[208:209], v16 offset:0
ds_read_b64_tr_b16 a[210:211], v16 offset:1024
ds_read_b64_tr_b16 a[212:213], v16 offset:8192
ds_read_b64_tr_b16 a[214:215], v16 offset:9216
v_add_u32 v17, 65536, v6
ds_read_b64_tr_b16 a[216:217], v17 offset:0
ds_read_b64_tr_b16 a[218:219], v17 offset:1024
ds_read_b64_tr_b16 a[220:221], v17 offset:8192
ds_read_b64_tr_b16 a[222:223], v17 offset:9216
s_waitcnt vmcnt(28)
s_waitcnt lgkmcnt(0)
s_barrier
s_add_u32 m0, s55, 65536
s_nop 0
global_load_lds_dwordx4 v[10:11], off
v_lshl_add_u64 v[10:11], v[10:11], 0, s[44:45]
s_add_u32 m0, s55, 69632
s_nop 0
global_load_lds_dwordx4 v[10:11], off
v_lshl_add_u64 v[10:11], v[10:11], 0, s[44:45]
s_add_u32 m0, s55, 73728
s_nop 0
global_load_lds_dwordx4 v[10:11], off
v_lshl_add_u64 v[10:11], v[10:11], 0, s[44:45]
s_add_u32 m0, s55, 77824
s_nop 0
global_load_lds_dwordx4 v[10:11], off
v_lshl_add_u64 v[10:11], v[10:11], 0, s[44:45]
v_add_u32 v16, 81920, v3
ds_read_b64_tr_b16 a[224:225], v16 offset:0
ds_read_b64_tr_b16 a[226:227], v16 offset:1024
ds_read_b64_tr_b16 a[228:229], v16 offset:8192
ds_read_b64_tr_b16 a[230:231], v16 offset:9216
v_add_u32 v17, 81920, v4
ds_read_b64_tr_b16 a[232:233], v17 offset:0
ds_read_b64_tr_b16 a[234:235], v17 offset:1024
ds_read_b64_tr_b16 a[236:237], v17 offset:8192
ds_read_b64_tr_b16 a[238:239], v17 offset:9216
s_waitcnt vmcnt(28)
s_waitcnt lgkmcnt(0)
s_barrier
s_add_u32 m0, s55, 81920
s_nop 0
global_load_lds_dwordx4 v[12:13], off
v_lshl_add_u64 v[12:13], v[12:13], 0, s[44:45]
s_add_u32 m0, s55, 86016
s_nop 0
global_load_lds_dwordx4 v[12:13], off
v_lshl_add_u64 v[12:13], v[12:13], 0, s[44:45]
s_add_u32 m0, s55, 90112
s_nop 0
global_load_lds_dwordx4 v[12:13], off
v_lshl_add_u64 v[12:13], v[12:13], 0, s[44:45]
s_add_u32 m0, s55, 94208
s_nop 0
global_load_lds_dwordx4 v[12:13], off
v_lshl_add_u64 v[12:13], v[12:13], 0, s[44:45]
v_add_u32 v16, 98304, v3
ds_read_b64_tr_b16 a[240:241], v16 offset:0
ds_read_b64_tr_b16 a[242:243], v16 offset:1024
ds_read_b64_tr_b16 a[244:245], v16 offset:8192
ds_read_b64_tr_b16 a[246:247], v16 offset:9216
v_add_u32 v17, 98304, v4
ds_read_b64_tr_b16 a[248:249], v17 offset:0
ds_read_b64_tr_b16 a[250:251], v17 offset:1024
ds_read_b64_tr_b16 a[252:253], v17 offset:8192
ds_read_b64_tr_b16 a[254:255], v17 offset:9216
s_waitcnt vmcnt(28)
s_waitcnt lgkmcnt(0)
s_barrier
s_add_u32 m0, s55, 98304
s_nop 0
global_load_lds_dwordx4 v[8:9], off
v_lshl_add_u64 v[8:9], v[8:9], 0, s[44:45]
s_add_u32 m0, s55, 102400
s_nop 0
global_load_lds_dwordx4 v[8:9], off
v_lshl_add_u64 v[8:9], v[8:9], 0, s[44:45]
s_add_u32 m0, s55, 106496
s_nop 0
global_load_lds_dwordx4 v[8:9], off
v_lshl_add_u64 v[8:9], v[8:9], 0, s[44:45]
s_add_u32 m0, s55, 110592
s_nop 0
global_load_lds_dwordx4 v[8:9], off
v_lshl_add_u64 v[8:9], v[8:9], 0, s[44:45]
v_add_u32 v16, 114688, v5
ds_read_b64_tr_b16 v[128:129], v16 offset:0
ds_read_b64_tr_b16 v[130:131], v16 offset:1024
ds_read_b64_tr_b16 v[132:133], v16 offset:8192
ds_read_b64_tr_b16 v[134:135], v16 offset:9216
v_add_u32 v17, 114688, v6
ds_read_b64_tr_b16 v[136:137], v17 offset:0
ds_read_b64_tr_b16 v[138:139], v17 offset:1024
ds_read_b64_tr_b16 v[140:141], v17 offset:8192
ds_read_b64_tr_b16 v[142:143], v17 offset:9216
s_waitcnt vmcnt(28)
s_waitcnt lgkmcnt(0)
s_barrier
s_add_u32 m0, s55, 114688
s_nop 0
global_load_lds_dwordx4 v[10:11], off
v_lshl_add_u64 v[10:11], v[10:11], 0, s[44:45]
s_add_u32 m0, s55, 118784
s_nop 0
global_load_lds_dwordx4 v[10:11], off
v_lshl_add_u64 v[10:11], v[10:11], 0, s[44:45]
s_add_u32 m0, s55, 122880
s_nop 0
global_load_lds_dwordx4 v[10:11], off
v_lshl_add_u64 v[10:11], v[10:11], 0, s[44:45]
s_add_u32 m0, s55, 126976
s_nop 0
global_load_lds_dwordx4 v[10:11], off
v_lshl_add_u64 v[10:11], v[10:11], 0, s[44:45]
v_add_u32 v16, 131072, v3
ds_read_b64_tr_b16 v[144:145], v16 offset:0
ds_read_b64_tr_b16 v[146:147], v16 offset:1024
ds_read_b64_tr_b16 v[148:149], v16 offset:8192
ds_read_b64_tr_b16 v[150:151], v16 offset:9216
v_add_u32 v17, 131072, v4
ds_read_b64_tr_b16 v[152:153], v17 offset:0
ds_read_b64_tr_b16 v[154:155], v17 offset:1024
ds_read_b64_tr_b16 v[156:157], v17 offset:8192
ds_read_b64_tr_b16 v[158:159], v17 offset:9216
s_waitcnt vmcnt(28)
s_waitcnt lgkmcnt(0)
s_barrier
s_add_u32 m0, s55, 131072
s_nop 0
global_load_lds_dwordx4 v[12:13], off
v_lshl_add_u64 v[12:13], v[12:13], 0, s[44:45]
s_add_u32 m0, s55, 135168
s_nop 0
global_load_lds_dwordx4 v[12:13], off
v_lshl_add_u64 v[12:13], v[12:13], 0, s[44:45]
s_add_u32 m0, s55, 139264
s_nop 0
global_load_lds_dwordx4 v[12:13], off
v_lshl_add_u64 v[12:13], v[12:13], 0, s[44:45]
s_add_u32 m0, s55, 143360
s_nop 0
global_load_lds_dwordx4 v[12:13], off
v_lshl_add_u64 v[12:13], v[12:13], 0, s[44:45]
v_add_u32 v16, 0, v3
ds_read_b64_tr_b16 v[160:161], v16 offset:0
ds_read_b64_tr_b16 v[162:163], v16 offset:1024
ds_read_b64_tr_b16 v[164:165], v16 offset:8192
ds_read_b64_tr_b16 v[166:167], v16 offset:9216
v_add_u32 v17, 0, v4
ds_read_b64_tr_b16 v[168:169], v17 offset:0
ds_read_b64_tr_b16 v[170:171], v17 offset:1024
ds_read_b64_tr_b16 v[172:173], v17 offset:8192
ds_read_b64_tr_b16 v[174:175], v17 offset:9216
s_waitcnt vmcnt(28)
s_waitcnt lgkmcnt(0)
s_barrier
s_add_u32 m0, s55, 0
s_nop 0
global_load_lds_dwordx4 v[8:9], off
v_lshl_add_u64 v[8:9], v[8:9], 0, s[44:45]
s_add_u32 m0, s55, 4096
s_nop 0
global_load_lds_dwordx4 v[8:9], off
v_lshl_add_u64 v[8:9], v[8:9], 0, s[44:45]
s_add_u32 m0, s55, 8192
s_nop 0
global_load_lds_dwordx4 v[8:9], off
v_lshl_add_u64 v[8:9], v[8:9], 0, s[44:45]
s_add_u32 m0, s55, 12288
s_nop 0
global_load_lds_dwordx4 v[8:9], off
v_lshl_add_u64 v[8:9], v[8:9], 0, s[44:45]
v_add_u32 v16, 16384, v5
ds_read_b64_tr_b16 v[176:177], v16 offset:0
ds_read_b64_tr_b16 v[178:179], v16 offset:1024
ds_read_b64_tr_b16 v[180:181], v16 offset:8192
ds_read_b64_tr_b16 v[182:183], v16 offset:9216
v_add_u32 v17, 16384, v6
ds_read_b64_tr_b16 v[184:185], v17 offset:0
ds_read_b64_tr_b16 v[186:187], v17 offset:1024
ds_read_b64_tr_b16 v[188:189], v17 offset:8192
ds_read_b64_tr_b16 v[190:191], v17 offset:9216
s_waitcnt vmcnt(28)
s_waitcnt lgkmcnt(0)
s_barrier
s_add_u32 m0, s55, 16384
s_nop 0
global_load_lds_dwordx4 v[10:11], off
v_lshl_add_u64 v[10:11], v[10:11], 0, s[44:45]
s_add_u32 m0, s55, 20480
s_nop 0
global_load_lds_dwordx4 v[10:11], off
v_lshl_add_u64 v[10:11], v[10:11], 0, s[44:45]
s_add_u32 m0, s55, 24576
s_nop 0
global_load_lds_dwordx4 v[10:11], off
v_lshl_add_u64 v[10:11], v[10:11], 0, s[44:45]
s_add_u32 m0, s55, 28672
s_nop 0
global_load_lds_dwordx4 v[10:11], off
v_lshl_add_u64 v[10:11], v[10:11], 0, s[44:45]
v_add_u32 v16, 32768, v3
ds_read_b64_tr_b16 v[192:193], v16 offset:0
ds_read_b64_tr_b16 v[194:195], v16 offset:1024
ds_read_b64_tr_b16 v[196:197], v16 offset:8192
ds_read_b64_tr_b16 v[198:199], v16 offset:9216
v_add_u32 v17, 32768, v4
ds_read_b64_tr_b16 v[200:201], v17 offset:0
ds_read_b64_tr_b16 v[202:203], v17 offset:1024
ds_read_b64_tr_b16 v[204:205], v17 offset:8192
ds_read_b64_tr_b16 v[206:207], v17 offset:9216
s_waitcnt vmcnt(28)
s_waitcnt lgkmcnt(0)
s_barrier
s_add_u32 m0, s55, 32768
s_nop 0
global_load_lds_dwordx4 v[12:13], off
v_lshl_add_u64 v[12:13], v[12:13], 0, s[44:45]
s_add_u32 m0, s55, 36864
s_nop 0
global_load_lds_dwordx4 v[12:13], off
v_lshl_add_u64 v[12:13], v[12:13], 0, s[44:45]
s_add_u32 m0, s55, 40960
s_nop 0
global_load_lds_dwordx4 v[12:13], off
v_lshl_add_u64 v[12:13], v[12:13], 0, s[44:45]
s_add_u32 m0, s55, 45056
s_nop 0
global_load_lds_dwordx4 v[12:13], off
v_lshl_add_u64 v[12:13], v[12:13], 0, s[44:45]
v_add_u32 v16, 49152, v3
ds_read_b64_tr_b16 v[208:209], v16 offset:0
ds_read_b64_tr_b16 v[210:211], v16 offset:1024
ds_read_b64_tr_b16 v[212:213], v16 offset:8192
ds_read_b64_tr_b16 v[214:215], v16 offset:9216
v_add_u32 v17, 49152, v4
ds_read_b64_tr_b16 v[216:217], v17 offset:0
ds_read_b64_tr_b16 v[218:219], v17 offset:1024
ds_read_b64_tr_b16 v[220:221], v17 offset:8192
ds_read_b64_tr_b16 v[222:223], v17 offset:9216
s_waitcnt vmcnt(28)
s_waitcnt lgkmcnt(0)
s_barrier
v_add_u32 v16, 65536, v5
ds_read_b64_tr_b16 v[224:225], v16 offset:0
ds_read_b64_tr_b16 v[226:227], v16 offset:1024
ds_read_b64_tr_b16 v[228:229], v16 offset:8192
ds_read_b64_tr_b16 v[230:231], v16 offset:9216
v_add_u32 v17, 65536, v6
ds_read_b64_tr_b16 v[232:233], v17 offset:0
ds_read_b64_tr_b16 v[234:235], v17 offset:1024
ds_read_b64_tr_b16 v[236:237], v17 offset:8192
ds_read_b64_tr_b16 v[238:239], v17 offset:9216
s_waitcnt vmcnt(24)
s_waitcnt lgkmcnt(0)
s_barrier
v_add_u32 v16, 81920, v3
ds_read_b64_tr_b16 v[240:241], v16 offset:0
ds_read_b64_tr_b16 v[242:243], v16 offset:1024
ds_read_b64_tr_b16 v[244:245], v16 offset:8192
ds_read_b64_tr_b16 v[246:247], v16 offset:9216
v_add_u32 v17, 81920, v4
ds_read_b64_tr_b16 v[248:249], v17 offset:0
ds_read_b64_tr_b16 v[250:251], v17 offset:1024
ds_read_b64_tr_b16 v[252:253], v17 offset:8192
ds_read_b64_tr_b16 v[254:255], v17 offset:9216
s_waitcnt vmcnt(20)
s_waitcnt lgkmcnt(0)
s_barrier
v_add_u32 v16, 98304, v3
ds_read_b64_tr_b16 v[32:33], v16 offset:0
ds_read_b64_tr_b16 v[34:35], v16 offset:1024
ds_read_b64_tr_b16 v[36:37], v16 offset:8192
ds_read_b64_tr_b16 v[38:39], v16 offset:9216
v_add_u32 v17, 98304, v4
ds_read_b64_tr_b16 v[40:41], v17 offset:0
ds_read_b64_tr_b16 v[42:43], v17 offset:1024
ds_read_b64_tr_b16 v[44:45], v17 offset:8192
ds_read_b64_tr_b16 v[46:47], v17 offset:9216
s_waitcnt vmcnt(16)
s_waitcnt lgkmcnt(0)
s_barrier
v_add_u32 v16, 98304, v7
ds_write_b128 v16, v[32:35] offset:0
ds_write_b128 v16, v[36:39] offset:1024
ds_write_b128 v16, v[40:43] offset:2048
ds_write_b128 v16, v[44:47] offset:3072
v_add_u32 v16, 114688, v5
ds_read_b64_tr_b16 v[32:33], v16 offset:0
ds_read_b64_tr_b16 v[34:35], v16 offset:1024
ds_read_b64_tr_b16 v[36:37], v16 offset:8192
ds_read_b64_tr_b16 v[38:39], v16 offset:9216
v_add_u32 v17, 114688, v6
ds_read_b64_tr_b16 v[40:41], v17 offset:0
ds_read_b64_tr_b16 v[42:43], v17 offset:1024
ds_read_b64_tr_b16 v[44:45], v17 offset:8192
ds_read_b64_tr_b16 v[46:47], v17 offset:9216
s_waitcnt vmcnt(12)
s_waitcnt lgkmcnt(0)
s_barrier
v_add_u32 v16, 114688, v7
ds_write_b128 v16, v[32:35] offset:0
ds_write_b128 v16, v[36:39] offset:1024
ds_write_b128 v16, v[40:43] offset:2048
ds_write_b128 v16, v[44:47] offset:3072
v_add_u32 v16, 131072, v3
ds_read_b64_tr_b16 v[32:33], v16 offset:0
ds_read_b64_tr_b16 v[34:35], v16 offset:1024
ds_read_b64_tr_b16 v[36:37], v16 offset:8192
ds_read_b64_tr_b16 v[38:39], v16 offset:9216
v_add_u32 v17, 131072, v4
ds_read_b64_tr_b16 v[40:41], v17 offset:0
ds_read_b64_tr_b16 v[42:43], v17 offset:1024
ds_read_b64_tr_b16 v[44:45], v17 offset:8192
ds_read_b64_tr_b16 v[46:47], v17 offset:9216
s_waitcnt vmcnt(8)
s_waitcnt lgkmcnt(0)
s_barrier
v_add_u32 v16, 131072, v7
ds_write_b128 v16, v[32:35] offset:0
ds_write_b128 v16, v[36:39] offset:1024
ds_write_b128 v16, v[40:43] offset:2048
ds_write_b128 v16, v[44:47] offset:3072
v_add_u32 v16, 0, v3
ds_read_b64_tr_b16 v[32:33], v16 offset:0
ds_read_b64_tr_b16 v[34:35], v16 offset:1024
ds_read_b64_tr_b16 v[36:37], v16 offset:8192
ds_read_b64_tr_b16 v[38:39], v16 offset:9216
v_add_u32 v17, 0, v4
ds_read_b64_tr_b16 v[40:41], v17 offset:0
ds_read_b64_tr_b16 v[42:43], v17 offset:1024
ds_read_b64_tr_b16 v[44:45], v17 offset:8192
ds_read_b64_tr_b16 v[46:47], v17 offset:9216
s_waitcnt vmcnt(4)
s_waitcnt lgkmcnt(0)
s_barrier
v_add_u32 v16, 0, v7
ds_write_b128 v16, v[32:35] offset:0
ds_write_b128 v16, v[36:39] offset:1024
ds_write_b128 v16, v[40:43] offset:2048
ds_write_b128 v16, v[44:47] offset:3072
v_add_u32 v16, 16384, v5
ds_read_b64_tr_b16 v[32:33], v16 offset:0
ds_read_b64_tr_b16 v[34:35], v16 offset:1024
ds_read_b64_tr_b16 v[36:37], v16 offset:8192
ds_read_b64_tr_b16 v[38:39], v16 offset:9216
v_add_u32 v17, 16384, v6
ds_read_b64_tr_b16 v[40:41], v17 offset:0
ds_read_b64_tr_b16 v[42:43], v17 offset:1024
ds_read_b64_tr_b16 v[44:45], v17 offset:8192
ds_read_b64_tr_b16 v[46:47], v17 offset:9216
s_waitcnt vmcnt(0)
s_waitcnt lgkmcnt(0)
s_barrier
v_add_u32 v16, 16384, v7
ds_write_b128 v16, v[32:35] offset:0
ds_write_b128 v16, v[36:39] offset:1024
ds_write_b128 v16, v[40:43] offset:2048
ds_write_b128 v16, v[44:47] offset:3072
v_add_u32 v16, 32768, v3
ds_read_b64_tr_b16 v[32:33], v16 offset:0
ds_read_b64_tr_b16 v[34:35], v16 offset:1024
ds_read_b64_tr_b16 v[36:37], v16 offset:8192
ds_read_b64_tr_b16 v[38:39], v16 offset:9216
v_add_u32 v17, 32768, v4
ds_read_b64_tr_b16 v[40:41], v17 offset:0
ds_read_b64_tr_b16 v[42:43], v17 offset:1024
ds_read_b64_tr_b16 v[44:45], v17 offset:8192
ds_read_b64_tr_b16 v[46:47], v17 offset:9216
s_waitcnt lgkmcnt(0)
s_barrier
v_add_u32 v16, 32768, v7
ds_write_b128 v16, v[32:35] offset:0
ds_write_b128 v16, v[36:39] offset:1024
ds_write_b128 v16, v[40:43] offset:2048
ds_write_b128 v16, v[44:47] offset:3072
s_waitcnt lgkmcnt(0)
s_barrier
v_add_u32 v43, 74752, v116
s_lshl_b32 s59, s54, 2
s_add_u32 s59, s59, s40
s_lshl_b32 s59, s59, 2
s_add_u32 s59, s59, 4207616
s_mov_b32 s56, 0x3dd53b94
v_mov_b32 v114, 0
v_mov_b32 v115, 0
v_and_b32 v0, 128, v116
v_cmp_ne_u32 s[78:79], 0, v0
s_mov_b32 s76, 0
s_mov_b32 s77, -1
s_nop 2
s_cmp_eq_u32 s40, 1
s_cbranch_scc1 S1_w1
s_cmp_eq_u32 s40, 2
s_cbranch_scc1 S1_w2
s_cmp_eq_u32 s40, 3
s_cbranch_scc1 S1_w3
S1_w0:
v_bfe_u32 v1, v116, 4, 3
s_lshl_b32 s42, s54, 7
s_add_u32 s42, s42, 4198400
v_lshl_add_u32 v1, v1, 2, s42
s_nop 1
S1_w0_K0:
buffer_load_dword v0, v1, s[96:99], 0 offen offset:0 sc1
s_waitcnt vmcnt(0)
v_cmp_gt_u32 vcc, 1, v0
s_cbranch_vccz S1_w0_K0K
s_sleep 3
s_sub_u32 s41, s41, 1
s_cbranch_scc0 S1_w0_K0
s_mov_b32 s41, 0
S1_w0_K0K:
s_add_u32 s42, s36, 0
s_min_u32 s42, s42, 63
s_lshl_b32 s42, s42, 5
s_add_u32 s42, s42, s54
s_mul_i32 s42, s42, 12288
s_add_u32 s43, s42, 8396800
s_add_u32 s42, s42, 8388608
buffer_load_dwordx4 v[104:107], v116, s[96:99], s42 offen offset:0 sc1
buffer_load_dwordx4 v[108:111], v116, s[96:99], s42 offen offset:1024 sc1
buffer_load_dwordx2 v[100:101], v127, s[96:99], s43 offen offset:0 sc1
buffer_load_dwordx2 v[102:103], v127, s[96:99], s43 offen offset:512 sc1
s_add_u32 s42, s42, 25165824
s_add_u32 s43, s43, 25165824
buffer_load_dwordx4 v[76:79], v116, s[96:99], s42 offen offset:0 sc1
buffer_load_dwordx4 v[80:83], v116, s[96:99], s42 offen offset:1024 sc1
buffer_load_dwordx2 v[84:85], v127, s[96:99], s43 offen offset:0 sc1
buffer_load_dwordx2 v[86:87], v127, s[96:99], s43 offen offset:512 sc1
s_waitcnt vmcnt(3)
ds_write_b128 v117, v[76:79] offset:49152
s_waitcnt vmcnt(2)
ds_write_b128 v117, v[80:83] offset:50176
s_waitcnt vmcnt(1)
ds_write_b64 v117, v[84:85] offset:51200
s_waitcnt vmcnt(0)
ds_write_b64 v117, v[86:87] offset:51208
S1_w0_T:
s_add_u32 s42, s36, 0
s_lshl_b32 s37, s42, 16
s_lshr_b32 s43, s37, 8
s_xor_b32 s43, s43, s39
s_and_b32 s43, s43, 8192
s_or_b32 s37, s37, s43
s_add_u32 s57, s37, 0
s_add_u32 s58, s37, 16384
v_mov_b32 v52, 0
v_mov_b32 v53, 0
v_mov_b32 v54, 0
v_mov_b32 v55, 0
v_mov_b32 v56, 0
v_mov_b32 v57, 0
v_mov_b32 v58, 0
v_mov_b32 v59, 0
v_mov_b32 v60, 0
v_mov_b32 v61, 0
v_mov_b32 v62, 0
v_mov_b32 v63, 0
v_mov_b32 v64, 0
v_mov_b32 v65, 0
v_mov_b32 v66, 0
v_mov_b32 v67, 0
v_mov_b32 v68, 0
v_mov_b32 v69, 0
v_mov_b32 v70, 0
v_mov_b32 v71, 0
v_mov_b32 v72, 0
v_mov_b32 v73, 0
v_mov_b32 v74, 0
v_mov_b32 v75, 0
ds_read_b128 v[76:79], v117 offset:0
ds_read_b128 v[80:83], v117 offset:1024
ds_read_b128 v[84:87], v117 offset:2048
ds_read_b128 v[88:91], v117 offset:3072
ds_read_b128 v[92:95], v117 offset:16384
ds_read_b128 v[96:99], v117 offset:17408
s_waitcnt lgkmcnt(0)
S1_w0_W:
s_memrealtime s[94:95]
s_waitcnt lgkmcnt(0)
s_sub_u32 s94, s92, s94
s_lshl_b32 s94, s94, 1
s_cmp_gt_i32 s94, 20000
s_cbranch_scc1 S1_w0_WG
s_cmp_gt_i32 s94, 16
s_cbranch_scc1 S1_w0_W
s_cmp_lt_i32 s94, 6
s_cbranch_scc1 S1_w0_WG
s_nop 3
s_cmp_lt_i32 s94, 8
s_cbranch_scc1 S1_w0_WG
s_nop 3
s_cmp_lt_i32 s94, 10
s_cbranch_scc1 S1_w0_WG
s_nop 3
s_cmp_lt_i32 s94, 12
s_cbranch_scc1 S1_w0_WG
s_nop 3
s_cmp_lt_i32 s94, 14
s_cbranch_scc1 S1_w0_WG
s_nop 3
s_cmp_lt_i32 s94, 16
s_cbranch_scc1 S1_w0_WG
s_nop 3
S1_w0_WG:
S1_w0_L:
buffer_load_dwordx4 v[32:35], v116, s[20:23], s57 offen offset:0 sc1
buffer_load_dwordx4 v[36:39], v116, s[20:23], s57 offen offset:1024 sc1
buffer_load_dwordx2 v[40:41], v118, s[20:23], s58 offen offset:0 sc1
s_waitcnt vmcnt(2)
v_max3_u32 v0, v32, v33, v34
s_waitcnt vmcnt(1)
v_max3_u32 v0, v0, v35, v36
v_max3_u32 v0, v0, v37, v38
s_waitcnt vmcnt(0)
v_max3_u32 v0, v0, v39, v40
v_max_u32 v0, v0, v41
v_cmp_eq_u32 vcc, -1, v0
s_cbranch_vccnz S1_w0_FA
S1_w0_HA:
ds_write_b128 v116, v[32:35] offset:49152
ds_write_b128 v116, v[36:39] offset:50176
ds_write_b64 v118, v[40:41] offset:57344
v_smfmac_f32_16x16x64_bf16 v[52:55], v[32:35], a[0:7], v119
v_smfmac_f32_16x16x64_bf16 v[56:59], v[32:35], a[8:15], v119
v_smfmac_f32_16x16x64_bf16 v[60:63], v[32:35], a[16:23], v119
v_smfmac_f32_16x16x64_bf16 v[52:55], v[36:39], a[48:55], v119
s_waitcnt lgkmcnt(0)
s_barrier
ds_read_b128 v[0:3], v116 offset:57344
v_smfmac_f32_16x16x64_bf16 v[56:59], v[36:39], a[56:63], v119
ds_read_b128 v[4:7], v116 offset:51200
v_smfmac_f32_16x16x64_bf16 v[60:63], v[36:39], a[64:71], v119
ds_read_b128 v[8:11], v116 offset:52224
v_smfmac_f32_16x16x64_bf16 v[64:67], v[32:35], a[32:39], v119
ds_read_b128 v[12:15], v116 offset:53248
v_smfmac_f32_16x16x64_bf16 v[68:71], v[32:35], a[40:47], v119
ds_read_b128 v[16:19], v116 offset:54272
v_smfmac_f32_16x16x64_bf16 v[72:75], v[32:35], a[24:31], v119
ds_read_b128 v[20:23], v116 offset:55296
v_smfmac_f32_16x16x64_bf16 v[64:67], v[36:39], a[80:87], v119
ds_read_b128 v[24:27], v116 offset:56320
v_smfmac_f32_16x16x64_bf16 v[68:71], v[36:39], a[88:95], v119
ds_read_b128 v[28:31], v116 offset:58368
v_smfmac_f32_16x16x64_bf16 v[72:75], v[36:39], a[72:79], v119
S1_w0_C:
s_waitcnt lgkmcnt(7)
v_smfmac_f32_16x16x64_bf16 v[52:55], v[0:3], v[76:83], v119
ds_read_b128 v[76:79], v116 offset:0
v_smfmac_f32_16x16x64_bf16 v[56:59], v[0:3], v[84:91], v119
s_waitcnt lgkmcnt(7)
ds_read_b128 v[80:83], v116 offset:1024
v_smfmac_f32_16x16x64_bf16 v[52:55], v[4:7], a[96:103], v119
ds_read_b128 v[84:87], v116 offset:2048
v_smfmac_f32_16x16x64_bf16 v[56:59], v[4:7], a[104:111], v119
s_waitcnt lgkmcnt(8)
ds_read_b128 v[88:91], v116 offset:3072
v_smfmac_f32_16x16x64_bf16 v[52:55], v[8:11], a[144:151], v119
v_smfmac_f32_16x16x64_bf16 v[56:59], v[8:11], a[152:159], v119
s_waitcnt lgkmcnt(8)
v_smfmac_f32_16x16x64_bf16 v[52:55], v[12:15], a[192:199], v119
v_smfmac_f32_16x16x64_bf16 v[56:59], v[12:15], a[200:207], v119
s_waitcnt lgkmcnt(7)
v_smfmac_f32_16x16x64_bf16 v[52:55], v[16:19], a[240:247], v119
v_smfmac_f32_16x16x64_bf16 v[56:59], v[16:19], a[248:255], v119
s_waitcnt lgkmcnt(6)
v_smfmac_f32_16x16x64_bf16 v[52:55], v[20:23], v[160:167], v119
v_smfmac_f32_16x16x64_bf16 v[56:59], v[20:23], v[168:175], v119
s_waitcnt lgkmcnt(5)
v_smfmac_f32_16x16x64_bf16 v[52:55], v[24:27], v[208:215], v119
v_smfmac_f32_16x16x64_bf16 v[56:59], v[24:27], v[216:223], v119
s_waitcnt lgkmcnt(2)
v_smfmac_f32_16x16x64_bf16 v[52:55], v[28:31], v[76:83], v119
s_waitcnt lgkmcnt(0)
ds_read_b128 v[76:79], v116 offset:16384
v_smfmac_f32_16x16x64_bf16 v[56:59], v[28:31], v[84:91], v119
ds_read_b128 v[80:83], v116 offset:17408
v_smfmac_f32_16x16x64_bf16 v[60:63], v[0:3], v[92:99], v119
ds_read_b128 v[84:87], v117 offset:32768
v_smfmac_f32_16x16x64_bf16 v[60:63], v[4:7], a[112:119], v119
ds_read_b128 v[88:91], v117 offset:33792
v_add_f32 v52, v52, v53
v_smfmac_f32_16x16x64_bf16 v[60:63], v[8:11], a[160:167], v119
ds_read_b128 v[92:95], v117 offset:34816
v_add_f32 v54, v54, v55
v_smfmac_f32_16x16x64_bf16 v[60:63], v[12:15], a[208:215], v119
ds_read_b128 v[96:99], v117 offset:35840
v_cvt_pk_bf16_f32 v52, v52, v54
v_smfmac_f32_16x16x64_bf16 v[60:63], v[16:19], v[128:135], v119
ds_write_b32 v120, v52 offset:0
v_add_f32 v56, v56, v57
v_smfmac_f32_16x16x64_bf16 v[60:63], v[20:23], v[176:183], v119
v_add_f32 v58, v58, v59
v_smfmac_f32_16x16x64_bf16 v[60:63], v[24:27], v[224:231], v119
s_waitcnt lgkmcnt(5)
v_cvt_pk_bf16_f32 v56, v56, v58
v_smfmac_f32_16x16x64_bf16 v[60:63], v[28:31], v[76:83], v119
s_waitcnt lgkmcnt(3)
ds_read_b128 v[76:79], v116 offset:32768
ds_write_b32 v120, v56 offset:256
v_smfmac_f32_16x16x64_bf16 v[64:67], v[0:3], v[84:91], v119
s_waitcnt lgkmcnt(3)
ds_read_b128 v[80:83], v116 offset:33792
v_smfmac_f32_16x16x64_bf16 v[68:71], v[0:3], v[92:99], v119
ds_read_b128 v[84:87], v116 offset:34816
v_smfmac_f32_16x16x64_bf16 v[64:67], v[4:7], a[128:135], v119
ds_read_b128 v[88:91], v116 offset:35840
v_add_f32 v60, v60, v61
v_smfmac_f32_16x16x64_bf16 v[68:71], v[4:7], a[136:143], v119
ds_read_b128 v[92:95], v117 offset:18432
v_add_f32 v62, v62, v63
v_smfmac_f32_16x16x64_bf16 v[64:67], v[8:11], a[176:183], v119
ds_read_b128 v[96:99], v117 offset:19456
v_cvt_pk_bf16_f32 v60, v60, v62
v_smfmac_f32_16x16x64_bf16 v[68:71], v[8:11], a[184:191], v119
ds_write_b32 v121, v60 offset:4096
v_mov_b32 v42, s36
v_smfmac_f32_16x16x64_bf16 v[64:67], v[12:15], a[224:231], v119
buffer_store_dword v42, v114, s[96:99], s59 offen offset:0 sc1
ds_read_b64_tr_b16 v[32:33], v122 offset:0
ds_read_b64_tr_b16 v[34:35], v122 offset:64
v_smfmac_f32_16x16x64_bf16 v[68:71], v[12:15], a[232:239], v119
ds_read_b64_tr_b16 v[112:113], v122 offset:4096
v_smfmac_f32_16x16x64_bf16 v[64:67], v[16:19], v[144:151], v119
v_smfmac_f32_16x16x64_bf16 v[68:71], v[16:19], v[152:159], v119
v_smfmac_f32_16x16x64_bf16 v[64:67], v[20:23], v[192:199], v119
v_smfmac_f32_16x16x64_bf16 v[68:71], v[20:23], v[200:207], v119
v_smfmac_f32_16x16x64_bf16 v[64:67], v[24:27], v[240:247], v119
v_smfmac_f32_16x16x64_bf16 v[68:71], v[24:27], v[248:255], v119
s_waitcnt lgkmcnt(8)
v_smfmac_f32_16x16x64_bf16 v[64:67], v[28:31], v[76:83], v119
s_waitcnt lgkmcnt(6)
ds_read_b128 v[76:79], v116 offset:18432
v_smfmac_f32_16x16x64_bf16 v[68:71], v[28:31], v[84:91], v119
s_waitcnt lgkmcnt(5)
ds_read_b128 v[80:83], v116 offset:19456
v_smfmac_f32_16x16x64_bf16 v[72:75], v[0:3], v[92:99], v119
s_waitcnt lgkmcnt(3)
v_mfma_f32_16x16x32_bf16 v[52:55], v[104:107], v[32:35], 0
v_add_f32 v64, v64, v65
v_add_f32 v66, v66, v67
v_mfma_f32_16x16x32_bf16 v[56:59], v[108:111], v[32:35], 0
s_waitcnt lgkmcnt(2)
v_cvt_pk_bf16_f32 v64, v64, v66
v_mfma_f32_16x16x32_bf16 v[52:55], v[100:103], v[112:115], v[52:55]
ds_write_b32 v120, v64 offset:2048
v_add_f32 v68, v68, v69
v_add_f32 v70, v70, v71
v_mfma_f32_16x16x32_bf16 v[56:59], v[102:105], v[112:115], v[56:59]
v_cvt_pk_bf16_f32 v68, v68, v70
v_smfmac_f32_16x16x64_bf16 v[72:75], v[4:7], a[120:127], v119
ds_write_b32 v120, v68 offset:2304
ds_read_b64_tr_b16 v[48:49], v122 offset:2048
ds_read_b64_tr_b16 v[50:51], v122 offset:2112
v_cndmask_b32 v52, v52, v54, s[78:79]
v_cndmask_b32 v53, v53, v55, s[78:79]
v_smfmac_f32_16x16x64_bf16 v[72:75], v[8:11], a[168:175], v119
ds_write_b64 v123, v[52:53] offset:0
v_cndmask_b32 v56, v56, v58, s[78:79]
v_cndmask_b32 v57, v57, v59, s[78:79]
v_smfmac_f32_16x16x64_bf16 v[72:75], v[12:15], a[216:223], v119
ds_write_b64 v123, v[56:57] offset:640
v_smfmac_f32_16x16x64_bf16 v[72:75], v[16:19], v[136:143], v119
v_smfmac_f32_16x16x64_bf16 v[72:75], v[20:23], v[184:191], v119
v_smfmac_f32_16x16x64_bf16 v[72:75], v[24:27], v[232:239], v119
s_waitcnt lgkmcnt(6)
s_waitcnt lgkmcnt(0)
s_barrier
ds_read_b64 v[0:1], v124 offset:0
v_smfmac_f32_16x16x64_bf16 v[72:75], v[28:31], v[76:83], v119
ds_read_b64 v[2:3], v124 offset:1280
ds_read_b64 v[4:5], v124 offset:2560
ds_read_b64 v[6:7], v124 offset:3840
v_mfma_f32_16x16x32_bf16 v[60:63], v[32:35], v[48:51], 0
ds_read_b64 v[8:9], v124 offset:640
ds_read_b64 v[10:11], v124 offset:1920
ds_read_b64 v[12:13], v124 offset:3200
ds_read_b64 v[14:15], v124 offset:4480
v_add_f32 v72, v72, v73
v_add_f32 v74, v74, v75
v_cvt_pk_bf16_f32 v72, v72, v74
ds_write_b32 v121, v72 offset:5120
v_cndmask_b32 v60, v60, v61, s[78:79]
v_cndmask_b32 v62, v62, v63, s[78:79]
v_cndmask_b32 v60, v60, v62, s[76:77]
ds_write_b32 v118, v60 offset:59392
ds_read_b64_tr_b16 v[36:37], v122 offset:4096
ds_read_b64_tr_b16 v[38:39], v122 offset:4160
ds_read_b64_tr_b16 v[44:45], v122 offset:4608
ds_read_b64_tr_b16 v[46:47], v122 offset:4672
s_waitcnt lgkmcnt(12)
v_add_f32 v0, v0, v2
s_waitcnt lgkmcnt(10)
v_add_f32 v4, v4, v6
v_add_f32 v0, v0, v4
v_add_f32 v1, v1, v3
v_add_f32 v5, v5, v7
v_add_f32 v1, v1, v5
s_waitcnt lgkmcnt(8)
v_add_f32 v8, v8, v10
s_waitcnt lgkmcnt(6)
v_add_f32 v12, v12, v14
v_add_f32 v8, v8, v12
v_add_f32 v9, v9, v11
v_add_f32 v13, v13, v15
v_add_f32 v9, v9, v13
s_waitcnt lgkmcnt(4)
s_barrier
ds_read_b64_tr_b16 v[64:65], v122 offset:5120
ds_read_b64_tr_b16 v[66:67], v122 offset:5184
ds_read_b64_tr_b16 v[68:69], v122 offset:5632
ds_read_b64_tr_b16 v[70:71], v122 offset:5696
ds_read_b64 v[20:21], v118 offset:59392
ds_read_b64 v[22:23], v118 offset:59904
v_max3_f32 v24, v0, v1, v8
v_max_f32 v24, v24, v9
v_mul_f32 v24, s56, v24
v_fma_f32 v16, v0, s56, -v24
v_fma_f32 v17, v1, s56, -v24
v_fma_f32 v18, v8, s56, -v24
v_fma_f32 v19, v9, s56, -v24
v_exp_f32 v16, v16
v_exp_f32 v17, v17
v_exp_f32 v18, v18
v_exp_f32 v19, v19
s_waitcnt lgkmcnt(4)
v_mfma_f32_16x16x32_bf16 v[72:75], v[36:39], v[64:67], 0
s_waitcnt lgkmcnt(2)
v_mfma_f32_16x16x32_bf16 v[72:75], v[44:47], v[68:71], v[72:75]
s_waitcnt lgkmcnt(1)
v_add_f32 v20, v20, v21
s_waitcnt lgkmcnt(0)
v_add_f32 v22, v22, v23
v_add_f32 v20, v20, v22
v_mul_f32 v20, s56, v20
v_add_f32 v25, v16, v17
v_add_f32 v30, v18, v19
v_add_f32 v25, v25, v30
v_cndmask_b32 v72, v72, v73, s[78:79]
v_cndmask_b32 v74, v74, v75, s[78:79]
v_cndmask_b32 v28, v72, v74, s[76:77]
v_fma_f32 v28, v28, s56, v20
v_max_f32 v27, v28, v24
s_nop 1
v_max_f32_dpp v27, v27, v27 quad_perm:[1,0,3,2] row_mask:0xf bank_mask:0xf
s_nop 1
v_max_f32_dpp v27, v27, v27 quad_perm:[2,3,0,1] row_mask:0xf bank_mask:0xf
s_nop 1
v_max_f32_dpp v27, v27, v27 row_half_mirror row_mask:0xf bank_mask:0xf
v_sub_f32 v28, v28, v27
v_sub_f32 v26, v24, v27
v_exp_f32 v28, v28
v_exp_f32 v26, v26
s_nop 0
v_fma_f32 v25, v25, v26, v28
s_nop 1
v_add_f32_dpp v25, v25, v25 quad_perm:[1,0,3,2] row_mask:0xf bank_mask:0xf
s_nop 1
v_add_f32_dpp v25, v25, v25 quad_perm:[2,3,0,1] row_mask:0xf bank_mask:0xf
s_nop 1
v_add_f32_dpp v25, v25, v25 row_half_mirror row_mask:0xf bank_mask:0xf
v_rcp_f32 v25, v25
s_nop 0
v_mul_f32 v26, v26, v25
v_mul_f32 v16, v16, v26
v_mul_f32 v17, v17, v26
v_mul_f32 v18, v18, v26
v_mul_f32 v19, v19, v26
v_cvt_pk_bf16_f32 v16, v16, v17
v_cvt_pk_bf16_f32 v17, v18, v19
buffer_store_dwordx2 v[16:17], v125, s[20:23], s37 offen offset:0 sc1
v_mul_f32_dpp v29, v28, v25 quad_perm:[1,0,3,2] row_mask:0xf bank_mask:0xf
v_mul_f32 v28, v28, v25
v_cvt_pk_bf16_f32 v28, v28, v29
buffer_store_dword v28, v126, s[20:23], s37 offen offset:0 sc1
s_add_u32 s42, s37, 16384
buffer_load_dword v40, v114, s[20:23], s42 offen offset:3076 sc1
v_bfe_u32 v42, v116, 4, 3
s_lshl_b32 s42, s54, 7
s_add_u32 s42, s42, 4198400
v_lshl_add_u32 v42, v42, 2, s42
buffer_load_dword v41, v42, s[96:99], 0 offen offset:0 sc1
ds_read_b128 v[0:3], v117 offset:49152
ds_read_b128 v[4:7], v117 offset:50176
ds_read_b64 v[8:9], v117 offset:51200
ds_read_b64 v[12:13], v117 offset:51208
ds_read_b64_tr_b16 v[16:17], v122 offset:5120
v_mov_b32 v10, 0
v_mov_b32 v11, 0
v_mov_b32 v14, 0
v_mov_b32 v15, 0
v_mov_b32 v18, 0
v_mov_b32 v19, 0
s_waitcnt lgkmcnt(4)
v_mfma_f32_16x16x32_bf16 v[52:55], v[48:51], v[0:3], 0
s_waitcnt lgkmcnt(3)
v_mfma_f32_16x16x32_bf16 v[56:59], v[48:51], v[4:7], 0
v_mfma_f32_16x16x32_bf16 v[60:63], v[104:107], v[0:3], 0
v_mfma_f32_16x16x32_bf16 v[64:67], v[104:107], v[4:7], 0
v_mfma_f32_16x16x32_bf16 v[68:71], v[108:111], v[4:7], 0
s_waitcnt lgkmcnt(0)
v_mfma_f32_16x16x32_bf16 v[52:55], v[16:19], v[8:11], v[52:55]
v_mfma_f32_16x16x32_bf16 v[56:59], v[16:19], v[12:15], v[56:59]
v_mfma_f32_16x16x32_bf16 v[60:63], v[100:103], v[8:11], v[60:63]
v_mfma_f32_16x16x32_bf16 v[64:67], v[100:103], v[12:15], v[64:67]
v_mfma_f32_16x16x32_bf16 v[68:71], v[102:105], v[12:15], v[68:71]
v_lshrrev_b32 v22, 7, v116
v_bfe_u32 v23, v116, 5, 2
v_bfe_u32 v24, v116, 4, 1
v_lshlrev_b32 v20, 4, v22
v_lshl_add_u32 v20, v23, 8, v20
v_add_u32 v20, 76800, v20
v_lshlrev_b32 v21, 4, v22
v_bfe_u32 v25, v116, 6, 1
v_lshl_add_u32 v21, v25, 8, v21
v_bfe_u32 v25, v116, 4, 2
v_lshl_add_u32 v21, v25, 2, v21
v_add_u32 v21, 74752, v21
s_add_u32 s58, s36, 2
s_min_u32 s58, s58, 64
ds_write_b128 v43, v[52:55] offset:0
ds_write_b128 v43, v[56:59] offset:1024
ds_write_b128 v43, v[60:63] offset:2048
ds_write_b128 v43, v[64:67] offset:3072
ds_write_b128 v43, v[68:71] offset:4096
s_waitcnt vmcnt(0)
v_cmp_gt_u32 vcc, s58, v41
s_cbranch_vccz S1_w0_KK
s_nop 3
S1_w0_K:
buffer_load_dword v41, v42, s[96:99], 0 offen offset:0 sc1
s_waitcnt vmcnt(0)
v_cmp_gt_u32 vcc, s58, v41
s_cbranch_vccz S1_w0_KK
s_sleep 3
s_sub_u32 s41, s41, 1
s_cbranch_scc0 S1_w0_K
s_mov_b32 s41, 0
S1_w0_KK:
s_add_u32 s42, s36, 1
s_min_u32 s42, s42, 63
s_lshl_b32 s42, s42, 5
s_add_u32 s42, s42, s54
s_mul_i32 s42, s42, 12288
s_add_u32 s43, s42, 8396800
s_add_u32 s42, s42, 8388608
buffer_load_dwordx4 v[104:107], v116, s[96:99], s42 offen offset:0 sc1
buffer_load_dwordx4 v[108:111], v116, s[96:99], s42 offen offset:1024 sc1
buffer_load_dwordx2 v[100:101], v127, s[96:99], s43 offen offset:0 sc1
buffer_load_dwordx2 v[102:103], v127, s[96:99], s43 offen offset:512 sc1
s_add_u32 s42, s42, 25165824
s_add_u32 s43, s43, 25165824
buffer_load_dwordx4 v[76:79], v116, s[96:99], s42 offen offset:0 sc1
buffer_load_dwordx4 v[80:83], v116, s[96:99], s42 offen offset:1024 sc1
buffer_load_dwordx2 v[84:85], v127, s[96:99], s43 offen offset:0 sc1
buffer_load_dwordx2 v[86:87], v127, s[96:99], s43 offen offset:512 sc1
v_lshlrev_b32 v26, 4, v22
v_and_b32 v25, 1, v23
v_lshl_add_u32 v27, v25, 9, v26
v_lshrrev_b32 v25, 1, v23
v_lshl_add_u32 v27, v25, 10, v27
v_lshl_add_u32 v27, v24, 3, v27
v_lshl_add_u32 v26, v23, 2, v26
v_lshl_add_u32 v26, v24, 30, v26
v_lshlrev_b32 v25, 2, v23
v_sub_u32 v22, v22, v25
v_lshlrev_b32 v25, 4, v24
v_sub_u32 v22, v22, v25
v_add_u32 v22, 0, v22
s_waitcnt lgkmcnt(0)
s_barrier
ds_read_b128 v[52:55], v20 offset:0
ds_read_b128 v[56:59], v20 offset:5120
ds_read_b128 v[60:63], v20 offset:10240
ds_read_b128 v[64:67], v20 offset:15360
ds_read_b32 v68, v21 offset:0
ds_read_b32 v69, v21 offset:5120
ds_read_b32 v70, v21 offset:10240
ds_read_b32 v71, v21 offset:15360
s_mul_i32 s42, s36, 81920
s_add_u32 s42, s42, 234881024
s_lshl_b32 s43, s54, 11
s_add_u32 s57, s42, s43
s_lshl_b32 s43, s54, 9
s_add_u32 s58, s42, s43
s_add_u32 s58, s58, 65536
v_mov_b32 v28, 0xff800000
s_waitcnt lgkmcnt(6)
v_add_f32 v52, v52, v56
s_waitcnt lgkmcnt(4)
v_add_f32 v60, v60, v64
v_add_f32 v52, v52, v60
v_cmp_le_i32 vcc, 0, v22
s_nop 1
v_cndmask_b32 v52, v28, v52, vcc
v_add_f32 v53, v53, v57
v_add_f32 v61, v61, v65
v_add_f32 v53, v53, v61
v_cmp_le_i32 vcc, 1, v22
s_nop 1
v_cndmask_b32 v53, v28, v53, vcc
v_add_f32 v54, v54, v58
v_add_f32 v62, v62, v66
v_add_f32 v54, v54, v62
v_cmp_le_i32 vcc, 2, v22
s_nop 1
v_cndmask_b32 v54, v28, v54, vcc
v_add_f32 v55, v55, v59
v_add_f32 v63, v63, v67
v_add_f32 v55, v55, v63
v_cmp_le_i32 vcc, 3, v22
s_nop 1
v_cndmask_b32 v55, v28, v55, vcc
s_waitcnt lgkmcnt(2)
v_add_f32 v68, v68, v69
s_waitcnt lgkmcnt(0)
v_add_f32 v70, v70, v71
v_add_f32 v68, v68, v70
v_max3_f32 v29, v52, v53, v54
v_max3_f32 v29, v29, v55, v68
s_nop 1
v_max_f32_dpp v29, v29, v29 quad_perm:[1,0,3,2] row_mask:0xf bank_mask:0xf
s_nop 1
v_max_f32_dpp v29, v29, v29 quad_perm:[2,3,0,1] row_mask:0xf bank_mask:0xf
s_nop 1
v_max_f32_dpp v29, v29, v29 row_half_mirror row_mask:0xf bank_mask:0xf
v_mul_f32 v29, s56, v29
v_fma_f32 v52, v52, s56, -v29
v_fma_f32 v53, v53, s56, -v29
v_fma_f32 v54, v54, s56, -v29
v_fma_f32 v55, v55, s56, -v29
v_fma_f32 v68, v68, s56, -v29
v_exp_f32 v52, v52
v_exp_f32 v53, v53
v_exp_f32 v54, v54
v_exp_f32 v55, v55
v_exp_f32 v68, v68
v_add_f32 v29, v52, v53
v_add_f32 v30, v54, v55
v_add_f32 v29, v29, v68
v_add_f32 v29, v29, v30
s_nop 1
v_add_f32_dpp v29, v29, v29 quad_perm:[1,0,3,2] row_mask:0xf bank_mask:0xf
s_nop 1
v_add_f32_dpp v29, v29, v29 quad_perm:[2,3,0,1] row_mask:0xf bank_mask:0xf
s_nop 1
v_add_f32_dpp v29, v29, v29 row_half_mirror row_mask:0xf bank_mask:0xf
v_rcp_f32 v29, v29
s_nop 1
v_mul_f32_dpp v30, v68, v29 quad_perm:[1,0,3,2] row_mask:0xf bank_mask:0xf
v_mul_f32 v52, v52, v29
v_mul_f32 v53, v53, v29
v_mul_f32 v54, v54, v29
v_mul_f32 v55, v55, v29
v_mul_f32 v68, v68, v29
v_cvt_pk_bf16_f32 v52, v52, v53
v_cvt_pk_bf16_f32 v53, v54, v55
v_cvt_pk_bf16_f32 v68, v68, v30
buffer_store_dwordx2 v[52:53], v27, s[96:99], s57 offen offset:0 sc1
buffer_store_dword v68, v26, s[96:99], s58 offen offset:0 sc1
s_waitcnt vmcnt(5)
ds_write_b128 v117, v[76:79] offset:49152
s_waitcnt vmcnt(4)
ds_write_b128 v117, v[80:83] offset:50176
s_waitcnt vmcnt(3)
ds_write_b64 v117, v[84:85] offset:51200
s_waitcnt vmcnt(2)
ds_write_b64 v117, v[86:87] offset:51208
s_cmp_eq_u32 s36, 0
s_cbranch_scc1 S1_w0_Z
v_readfirstlane_b32 s92, v40
s_nop 0
s_cmp_lg_u32 s92, -1
s_cbranch_scc1 S1_w0_Z
s_nop 1
v_mov_b32 v1, 0
s_add_u32 s42, s37, 16384
S1_w0_S:
buffer_load_dword v0, v1, s[20:23], s42 offen offset:3076 sc1
s_waitcnt vmcnt(0)
v_readfirstlane_b32 s92, v0
s_nop 0
s_cmp_lg_u32 s92, -1
s_cbranch_scc1 S1_w0_SK
s_sleep 2
s_sub_u32 s41, s41, 1
s_cbranch_scc0 S1_w0_S
s_mov_b32 s41, 0
S1_w0_SK:
S1_w0_Z:
s_add_u32 s36, s36, 1
s_cmp_lt_u32 s36, 64
s_cbranch_scc1 S1_w0_T
s_waitcnt vmcnt(0)
v_mov_b32 v42, s36
buffer_store_dword v42, v114, s[96:99], s59 offen offset:0 sc1
s_branch S1_END
S1_w0_FA:
s_sub_u32 s41, s41, 1
s_cbranch_scc0 S1_w0_L
s_mov_b32 s41, 0
s_branch S1_w0_HA
S1_w1:
v_bfe_u32 v1, v116, 4, 3
s_lshl_b32 s42, s54, 7
s_add_u32 s42, s42, 4198400
v_lshl_add_u32 v1, v1, 2, s42
s_nop 1
S1_w1_K0:
buffer_load_dword v0, v1, s[96:99], 0 offen offset:0 sc1
s_waitcnt vmcnt(0)
v_cmp_gt_u32 vcc, 1, v0
s_cbranch_vccz S1_w1_K0K
s_sleep 3
s_sub_u32 s41, s41, 1
s_cbranch_scc0 S1_w1_K0
s_mov_b32 s41, 0
S1_w1_K0K:
s_add_u32 s42, s36, 0
s_min_u32 s42, s42, 63
s_lshl_b32 s42, s42, 5
s_add_u32 s42, s42, s54
s_mul_i32 s42, s42, 12288
s_add_u32 s43, s42, 8397824
s_add_u32 s42, s42, 8390656
buffer_load_dwordx4 v[104:107], v116, s[96:99], s42 offen offset:0 sc1
buffer_load_dwordx4 v[108:111], v116, s[96:99], s42 offen offset:1024 sc1
buffer_load_dwordx2 v[100:101], v127, s[96:99], s43 offen offset:0 sc1
buffer_load_dwordx2 v[102:103], v127, s[96:99], s43 offen offset:512 sc1
s_add_u32 s42, s42, 25165824
s_add_u32 s43, s43, 25165824
buffer_load_dwordx4 v[76:79], v116, s[96:99], s42 offen offset:0 sc1
buffer_load_dwordx4 v[80:83], v116, s[96:99], s42 offen offset:1024 sc1
buffer_load_dwordx2 v[84:85], v127, s[96:99], s43 offen offset:0 sc1
buffer_load_dwordx2 v[86:87], v127, s[96:99], s43 offen offset:512 sc1
s_waitcnt vmcnt(3)
ds_write_b128 v117, v[76:79] offset:53248
s_waitcnt vmcnt(2)
ds_write_b128 v117, v[80:83] offset:54272
s_waitcnt vmcnt(1)
ds_write_b64 v117, v[84:85] offset:55296
s_waitcnt vmcnt(0)
ds_write_b64 v117, v[86:87] offset:55304
S1_w1_T:
s_add_u32 s42, s36, 0
s_lshl_b32 s37, s42, 16
s_lshr_b32 s43, s37, 8
s_xor_b32 s43, s43, s39
s_and_b32 s43, s43, 8192
s_or_b32 s37, s37, s43
s_add_u32 s57, s37, 0
s_add_u32 s58, s37, 16384
v_mov_b32 v52, 0
v_mov_b32 v53, 0
v_mov_b32 v54, 0
v_mov_b32 v55, 0
v_mov_b32 v56, 0
v_mov_b32 v57, 0
v_mov_b32 v58, 0
v_mov_b32 v59, 0
v_mov_b32 v60, 0
v_mov_b32 v61, 0
v_mov_b32 v62, 0
v_mov_b32 v63, 0
v_mov_b32 v64, 0
v_mov_b32 v65, 0
v_mov_b32 v66, 0
v_mov_b32 v67, 0
v_mov_b32 v68, 0
v_mov_b32 v69, 0
v_mov_b32 v70, 0
v_mov_b32 v71, 0
v_mov_b32 v72, 0
v_mov_b32 v73, 0
v_mov_b32 v74, 0
v_mov_b32 v75, 0
ds_read_b128 v[76:79], v117 offset:4096
ds_read_b128 v[80:83], v117 offset:5120
ds_read_b128 v[84:87], v117 offset:6144
ds_read_b128 v[88:91], v117 offset:7168
ds_read_b128 v[92:95], v117 offset:20480
ds_read_b128 v[96:99], v117 offset:21504
s_waitcnt lgkmcnt(0)
S1_w1_W:
s_memrealtime s[94:95]
s_waitcnt lgkmcnt(0)
s_sub_u32 s94, s92, s94
s_lshl_b32 s94, s94, 1
s_cmp_gt_i32 s94, 20000
s_cbranch_scc1 S1_w1_WG
s_cmp_gt_i32 s94, 16
s_cbranch_scc1 S1_w1_W
s_cmp_lt_i32 s94, 6
s_cbranch_scc1 S1_w1_WG
s_nop 3
s_cmp_lt_i32 s94, 8
s_cbranch_scc1 S1_w1_WG
s_nop 3
s_cmp_lt_i32 s94, 10
s_cbranch_scc1 S1_w1_WG
s_nop 3
s_cmp_lt_i32 s94, 12
s_cbranch_scc1 S1_w1_WG
s_nop 3
s_cmp_lt_i32 s94, 14
s_cbranch_scc1 S1_w1_WG
s_nop 3
s_cmp_lt_i32 s94, 16
s_cbranch_scc1 S1_w1_WG
s_nop 3
S1_w1_WG:
S1_w1_L:
buffer_load_dwordx4 v[32:35], v116, s[20:23], s57 offen offset:2048 sc1
buffer_load_dwordx4 v[36:39], v116, s[20:23], s57 offen offset:3072 sc1
buffer_load_dwordx2 v[40:41], v118, s[20:23], s58 offen offset:512 sc1
s_waitcnt vmcnt(2)
v_max3_u32 v0, v32, v33, v34
s_waitcnt vmcnt(1)
v_max3_u32 v0, v0, v35, v36
v_max3_u32 v0, v0, v37, v38
s_waitcnt vmcnt(0)
v_max3_u32 v0, v0, v39, v40
v_max_u32 v0, v0, v41
v_cmp_eq_u32 vcc, -1, v0
s_cbranch_vccnz S1_w1_FA
S1_w1_HA:
ds_write_b128 v116, v[32:35] offset:51200
ds_write_b128 v116, v[36:39] offset:52224
ds_write_b64 v118, v[40:41] offset:57856
v_smfmac_f32_16x16x64_bf16 v[52:55], v[32:35], a[96:103], v119
v_smfmac_f32_16x16x64_bf16 v[56:59], v[32:35], a[104:111], v119
v_smfmac_f32_16x16x64_bf16 v[60:63], v[32:35], a[112:119], v119
v_smfmac_f32_16x16x64_bf16 v[52:55], v[36:39], a[144:151], v119
s_waitcnt lgkmcnt(0)
s_barrier
ds_read_b128 v[0:3], v116 offset:57344
v_smfmac_f32_16x16x64_bf16 v[56:59], v[36:39], a[152:159], v119
ds_read_b128 v[4:7], v116 offset:53248
v_smfmac_f32_16x16x64_bf16 v[60:63], v[36:39], a[160:167], v119
ds_read_b128 v[8:11], v116 offset:54272
v_smfmac_f32_16x16x64_bf16 v[64:67], v[32:35], a[128:135], v119
ds_read_b128 v[12:15], v116 offset:55296
v_smfmac_f32_16x16x64_bf16 v[68:71], v[32:35], a[136:143], v119
ds_read_b128 v[16:19], v116 offset:56320
v_smfmac_f32_16x16x64_bf16 v[72:75], v[32:35], a[120:127], v119
ds_read_b128 v[20:23], v116 offset:49152
v_smfmac_f32_16x16x64_bf16 v[64:67], v[36:39], a[176:183], v119
ds_read_b128 v[24:27], v116 offset:50176
v_smfmac_f32_16x16x64_bf16 v[68:71], v[36:39], a[184:191], v119
ds_read_b128 v[28:31], v116 offset:58368
v_smfmac_f32_16x16x64_bf16 v[72:75], v[36:39], a[168:175], v119
S1_w1_C:
s_waitcnt lgkmcnt(7)
v_smfmac_f32_16x16x64_bf16 v[52:55], v[0:3], v[76:83], v119
ds_read_b128 v[76:79], v116 offset:4096
v_smfmac_f32_16x16x64_bf16 v[56:59], v[0:3], v[84:91], v119
s_waitcnt lgkmcnt(7)
ds_read_b128 v[80:83], v116 offset:5120
v_smfmac_f32_16x16x64_bf16 v[52:55], v[4:7], a[192:199], v119
ds_read_b128 v[84:87], v116 offset:6144
v_smfmac_f32_16x16x64_bf16 v[56:59], v[4:7], a[200:207], v119
s_waitcnt lgkmcnt(8)
ds_read_b128 v[88:91], v116 offset:7168
v_smfmac_f32_16x16x64_bf16 v[52:55], v[8:11], a[240:247], v119
v_smfmac_f32_16x16x64_bf16 v[56:59], v[8:11], a[248:255], v119
s_waitcnt lgkmcnt(8)
v_smfmac_f32_16x16x64_bf16 v[52:55], v[12:15], v[160:167], v119
v_smfmac_f32_16x16x64_bf16 v[56:59], v[12:15], v[168:175], v119
s_waitcnt lgkmcnt(7)
v_smfmac_f32_16x16x64_bf16 v[52:55], v[16:19], v[208:215], v119
v_smfmac_f32_16x16x64_bf16 v[56:59], v[16:19], v[216:223], v119
s_waitcnt lgkmcnt(6)
v_smfmac_f32_16x16x64_bf16 v[52:55], v[20:23], a[0:7], v119
v_smfmac_f32_16x16x64_bf16 v[56:59], v[20:23], a[8:15], v119
s_waitcnt lgkmcnt(5)
v_smfmac_f32_16x16x64_bf16 v[52:55], v[24:27], a[48:55], v119
v_smfmac_f32_16x16x64_bf16 v[56:59], v[24:27], a[56:63], v119
s_waitcnt lgkmcnt(2)
v_smfmac_f32_16x16x64_bf16 v[52:55], v[28:31], v[76:83], v119
s_waitcnt lgkmcnt(0)
ds_read_b128 v[76:79], v116 offset:20480
v_smfmac_f32_16x16x64_bf16 v[56:59], v[28:31], v[84:91], v119
ds_read_b128 v[80:83], v116 offset:21504
v_smfmac_f32_16x16x64_bf16 v[60:63], v[0:3], v[92:99], v119
ds_read_b128 v[84:87], v117 offset:36864
v_smfmac_f32_16x16x64_bf16 v[60:63], v[4:7], a[208:215], v119
ds_read_b128 v[88:91], v117 offset:37888
v_add_f32 v52, v52, v53
v_smfmac_f32_16x16x64_bf16 v[60:63], v[8:11], v[128:135], v119
ds_read_b128 v[92:95], v117 offset:38912
v_add_f32 v54, v54, v55
v_smfmac_f32_16x16x64_bf16 v[60:63], v[12:15], v[176:183], v119
ds_read_b128 v[96:99], v117 offset:39936
v_cvt_pk_bf16_f32 v52, v52, v54
v_smfmac_f32_16x16x64_bf16 v[60:63], v[16:19], v[224:231], v119
ds_write_b32 v120, v52 offset:512
v_add_f32 v56, v56, v57
v_smfmac_f32_16x16x64_bf16 v[60:63], v[20:23], a[16:23], v119
v_add_f32 v58, v58, v59
v_smfmac_f32_16x16x64_bf16 v[60:63], v[24:27], a[64:71], v119
s_waitcnt lgkmcnt(5)
v_cvt_pk_bf16_f32 v56, v56, v58
v_smfmac_f32_16x16x64_bf16 v[60:63], v[28:31], v[76:83], v119
s_waitcnt lgkmcnt(3)
ds_read_b128 v[76:79], v116 offset:36864
ds_write_b32 v120, v56 offset:768
v_smfmac_f32_16x16x64_bf16 v[64:67], v[0:3], v[84:91], v119
s_waitcnt lgkmcnt(3)
ds_read_b128 v[80:83], v116 offset:37888
v_smfmac_f32_16x16x64_bf16 v[68:71], v[0:3], v[92:99], v119
ds_read_b128 v[84:87], v116 offset:38912
v_smfmac_f32_16x16x64_bf16 v[64:67], v[4:7], a[224:231], v119
ds_read_b128 v[88:91], v116 offset:39936
v_add_f32 v60, v60, v61
v_smfmac_f32_16x16x64_bf16 v[68:71], v[4:7], a[232:239], v119
ds_read_b128 v[92:95], v117 offset:22528
v_add_f32 v62, v62, v63
v_smfmac_f32_16x16x64_bf16 v[64:67], v[8:11], v[144:151], v119
ds_read_b128 v[96:99], v117 offset:23552
v_cvt_pk_bf16_f32 v60, v60, v62
v_smfmac_f32_16x16x64_bf16 v[68:71], v[8:11], v[152:159], v119
ds_write_b32 v121, v60 offset:4160
v_mov_b32 v42, s36
v_smfmac_f32_16x16x64_bf16 v[64:67], v[12:15], v[192:199], v119
buffer_store_dword v42, v114, s[96:99], s59 offen offset:0 sc1
ds_read_b64_tr_b16 v[32:33], v122 offset:512
ds_read_b64_tr_b16 v[34:35], v122 offset:576
v_smfmac_f32_16x16x64_bf16 v[68:71], v[12:15], v[200:207], v119
ds_read_b64_tr_b16 v[112:113], v122 offset:4160
v_smfmac_f32_16x16x64_bf16 v[64:67], v[16:19], v[240:247], v119
v_smfmac_f32_16x16x64_bf16 v[68:71], v[16:19], v[248:255], v119
v_smfmac_f32_16x16x64_bf16 v[64:67], v[20:23], a[32:39], v119
v_smfmac_f32_16x16x64_bf16 v[68:71], v[20:23], a[40:47], v119
v_smfmac_f32_16x16x64_bf16 v[64:67], v[24:27], a[80:87], v119
v_smfmac_f32_16x16x64_bf16 v[68:71], v[24:27], a[88:95], v119
s_waitcnt lgkmcnt(8)
v_smfmac_f32_16x16x64_bf16 v[64:67], v[28:31], v[76:83], v119
s_waitcnt lgkmcnt(6)
ds_read_b128 v[76:79], v116 offset:22528
v_smfmac_f32_16x16x64_bf16 v[68:71], v[28:31], v[84:91], v119
s_waitcnt lgkmcnt(5)
ds_read_b128 v[80:83], v116 offset:23552
v_smfmac_f32_16x16x64_bf16 v[72:75], v[0:3], v[92:99], v119
s_waitcnt lgkmcnt(3)
v_mfma_f32_16x16x32_bf16 v[52:55], v[104:107], v[32:35], 0
v_add_f32 v64, v64, v65
v_add_f32 v66, v66, v67
v_mfma_f32_16x16x32_bf16 v[56:59], v[108:111], v[32:35], 0
s_waitcnt lgkmcnt(2)
v_cvt_pk_bf16_f32 v64, v64, v66
v_mfma_f32_16x16x32_bf16 v[52:55], v[100:103], v[112:115], v[52:55]
ds_write_b32 v120, v64 offset:2560
v_add_f32 v68, v68, v69
v_add_f32 v70, v70, v71
v_mfma_f32_16x16x32_bf16 v[56:59], v[102:105], v[112:115], v[56:59]
v_cvt_pk_bf16_f32 v68, v68, v70
v_smfmac_f32_16x16x64_bf16 v[72:75], v[4:7], a[216:223], v119
ds_write_b32 v120, v68 offset:2816
ds_read_b64_tr_b16 v[48:49], v122 offset:2560
ds_read_b64_tr_b16 v[50:51], v122 offset:2624
v_cndmask_b32 v52, v52, v54, s[78:79]
v_cndmask_b32 v53, v53, v55, s[78:79]
v_smfmac_f32_16x16x64_bf16 v[72:75], v[8:11], v[136:143], v119
ds_write_b64 v123, v[52:53] offset:1280
v_cndmask_b32 v56, v56, v58, s[78:79]
v_cndmask_b32 v57, v57, v59, s[78:79]
v_smfmac_f32_16x16x64_bf16 v[72:75], v[12:15], v[184:191], v119
ds_write_b64 v123, v[56:57] offset:1920
v_smfmac_f32_16x16x64_bf16 v[72:75], v[16:19], v[232:239], v119
v_smfmac_f32_16x16x64_bf16 v[72:75], v[20:23], a[24:31], v119
v_smfmac_f32_16x16x64_bf16 v[72:75], v[24:27], a[72:79], v119
s_waitcnt lgkmcnt(6)
s_waitcnt lgkmcnt(0)
s_barrier
v_smfmac_f32_16x16x64_bf16 v[72:75], v[28:31], v[76:83], v119
v_mfma_f32_16x16x32_bf16 v[60:63], v[32:35], v[48:51], 0
s_nop 6
v_add_f32 v72, v72, v73
v_add_f32 v74, v74, v75
v_cvt_pk_bf16_f32 v72, v72, v74
ds_write_b32 v121, v72 offset:5184
v_cndmask_b32 v60, v60, v61, s[78:79]
v_cndmask_b32 v62, v62, v63, s[78:79]
v_cndmask_b32 v60, v60, v62, s[76:77]
ds_write_b32 v118, v60 offset:59396
s_waitcnt lgkmcnt(0)
s_barrier
s_add_u32 s42, s37, 16384
buffer_load_dword v40, v114, s[20:23], s42 offen offset:3076 sc1
v_bfe_u32 v42, v116, 4, 3
s_lshl_b32 s42, s54, 7
s_add_u32 s42, s42, 4198400
v_lshl_add_u32 v42, v42, 2, s42
buffer_load_dword v41, v42, s[96:99], 0 offen offset:0 sc1
ds_read_b128 v[0:3], v117 offset:53248
ds_read_b128 v[4:7], v117 offset:54272
ds_read_b64 v[8:9], v117 offset:55296
ds_read_b64 v[12:13], v117 offset:55304
ds_read_b64_tr_b16 v[16:17], v122 offset:5184
v_mov_b32 v10, 0
v_mov_b32 v11, 0
v_mov_b32 v14, 0
v_mov_b32 v15, 0
v_mov_b32 v18, 0
v_mov_b32 v19, 0
s_waitcnt lgkmcnt(4)
v_mfma_f32_16x16x32_bf16 v[52:55], v[48:51], v[0:3], 0
s_waitcnt lgkmcnt(3)
v_mfma_f32_16x16x32_bf16 v[56:59], v[48:51], v[4:7], 0
v_mfma_f32_16x16x32_bf16 v[60:63], v[104:107], v[0:3], 0
v_mfma_f32_16x16x32_bf16 v[64:67], v[104:107], v[4:7], 0
v_mfma_f32_16x16x32_bf16 v[68:71], v[108:111], v[4:7], 0
s_waitcnt lgkmcnt(0)
v_mfma_f32_16x16x32_bf16 v[52:55], v[16:19], v[8:11], v[52:55]
v_mfma_f32_16x16x32_bf16 v[56:59], v[16:19], v[12:15], v[56:59]
v_mfma_f32_16x16x32_bf16 v[60:63], v[100:103], v[8:11], v[60:63]
v_mfma_f32_16x16x32_bf16 v[64:67], v[100:103], v[12:15], v[64:67]
v_mfma_f32_16x16x32_bf16 v[68:71], v[102:105], v[12:15], v[68:71]
v_lshrrev_b32 v22, 7, v116
v_bfe_u32 v23, v116, 5, 2
v_bfe_u32 v24, v116, 4, 1
v_lshlrev_b32 v20, 4, v22
v_lshl_add_u32 v20, v23, 8, v20
v_add_u32 v20, 76928, v20
v_lshlrev_b32 v21, 4, v22
v_bfe_u32 v25, v116, 6, 1
v_lshl_add_u32 v21, v25, 8, v21
v_bfe_u32 v25, v116, 4, 2
v_lshl_add_u32 v21, v25, 2, v21
v_add_u32 v21, 74880, v21
s_add_u32 s58, s36, 2
s_min_u32 s58, s58, 64
ds_write_b128 v43, v[52:55] offset:5120
ds_write_b128 v43, v[56:59] offset:6144
ds_write_b128 v43, v[60:63] offset:7168
ds_write_b128 v43, v[64:67] offset:8192
ds_write_b128 v43, v[68:71] offset:9216
s_waitcnt vmcnt(0)
v_cmp_gt_u32 vcc, s58, v41
s_cbranch_vccz S1_w1_KK
s_nop 3
S1_w1_K:
buffer_load_dword v41, v42, s[96:99], 0 offen offset:0 sc1
s_waitcnt vmcnt(0)
v_cmp_gt_u32 vcc, s58, v41
s_cbranch_vccz S1_w1_KK
s_sleep 3
s_sub_u32 s41, s41, 1
s_cbranch_scc0 S1_w1_K
s_mov_b32 s41, 0
S1_w1_KK:
s_add_u32 s42, s36, 1
s_min_u32 s42, s42, 63
s_lshl_b32 s42, s42, 5
s_add_u32 s42, s42, s54
s_mul_i32 s42, s42, 12288
s_add_u32 s43, s42, 8397824
s_add_u32 s42, s42, 8390656
buffer_load_dwordx4 v[104:107], v116, s[96:99], s42 offen offset:0 sc1
buffer_load_dwordx4 v[108:111], v116, s[96:99], s42 offen offset:1024 sc1
buffer_load_dwordx2 v[100:101], v127, s[96:99], s43 offen offset:0 sc1
buffer_load_dwordx2 v[102:103], v127, s[96:99], s43 offen offset:512 sc1
s_add_u32 s42, s42, 25165824
s_add_u32 s43, s43, 25165824
buffer_load_dwordx4 v[76:79], v116, s[96:99], s42 offen offset:0 sc1
buffer_load_dwordx4 v[80:83], v116, s[96:99], s42 offen offset:1024 sc1
buffer_load_dwordx2 v[84:85], v127, s[96:99], s43 offen offset:0 sc1
buffer_load_dwordx2 v[86:87], v127, s[96:99], s43 offen offset:512 sc1
v_lshlrev_b32 v26, 4, v22
v_and_b32 v25, 1, v23
v_lshl_add_u32 v27, v25, 9, v26
v_lshrrev_b32 v25, 1, v23
v_lshl_add_u32 v27, v25, 10, v27
v_lshl_add_u32 v27, v24, 3, v27
v_lshl_add_u32 v26, v23, 2, v26
v_lshl_add_u32 v26, v24, 30, v26
v_lshlrev_b32 v25, 2, v23
v_sub_u32 v22, v22, v25
v_lshlrev_b32 v25, 4, v24
v_sub_u32 v22, v22, v25
v_add_u32 v22, 8, v22
s_waitcnt lgkmcnt(0)
s_barrier
ds_read_b128 v[52:55], v20 offset:0
ds_read_b128 v[56:59], v20 offset:5120
ds_read_b128 v[60:63], v20 offset:10240
ds_read_b128 v[64:67], v20 offset:15360
ds_read_b32 v68, v21 offset:0
ds_read_b32 v69, v21 offset:5120
ds_read_b32 v70, v21 offset:10240
ds_read_b32 v71, v21 offset:15360
s_mul_i32 s42, s36, 81920
s_add_u32 s42, s42, 234881152
s_lshl_b32 s43, s54, 11
s_add_u32 s57, s42, s43
s_lshl_b32 s43, s54, 9
s_add_u32 s58, s42, s43
s_add_u32 s58, s58, 65536
v_mov_b32 v28, 0xff800000
s_waitcnt lgkmcnt(6)
v_add_f32 v52, v52, v56
s_waitcnt lgkmcnt(4)
v_add_f32 v60, v60, v64
v_add_f32 v52, v52, v60
v_cmp_le_i32 vcc, 0, v22
s_nop 1
v_cndmask_b32 v52, v28, v52, vcc
v_add_f32 v53, v53, v57
v_add_f32 v61, v61, v65
v_add_f32 v53, v53, v61
v_cmp_le_i32 vcc, 1, v22
s_nop 1
v_cndmask_b32 v53, v28, v53, vcc
v_add_f32 v54, v54, v58
v_add_f32 v62, v62, v66
v_add_f32 v54, v54, v62
v_cmp_le_i32 vcc, 2, v22
s_nop 1
v_cndmask_b32 v54, v28, v54, vcc
v_add_f32 v55, v55, v59
v_add_f32 v63, v63, v67
v_add_f32 v55, v55, v63
v_cmp_le_i32 vcc, 3, v22
s_nop 1
v_cndmask_b32 v55, v28, v55, vcc
s_waitcnt lgkmcnt(2)
v_add_f32 v68, v68, v69
s_waitcnt lgkmcnt(0)
v_add_f32 v70, v70, v71
v_add_f32 v68, v68, v70
v_max3_f32 v29, v52, v53, v54
v_max3_f32 v29, v29, v55, v68
s_nop 1
v_max_f32_dpp v29, v29, v29 quad_perm:[1,0,3,2] row_mask:0xf bank_mask:0xf
s_nop 1
v_max_f32_dpp v29, v29, v29 quad_perm:[2,3,0,1] row_mask:0xf bank_mask:0xf
s_nop 1
v_max_f32_dpp v29, v29, v29 row_half_mirror row_mask:0xf bank_mask:0xf
v_mul_f32 v29, s56, v29
v_fma_f32 v52, v52, s56, -v29
v_fma_f32 v53, v53, s56, -v29
v_fma_f32 v54, v54, s56, -v29
v_fma_f32 v55, v55, s56, -v29
v_fma_f32 v68, v68, s56, -v29
v_exp_f32 v52, v52
v_exp_f32 v53, v53
v_exp_f32 v54, v54
v_exp_f32 v55, v55
v_exp_f32 v68, v68
v_add_f32 v29, v52, v53
v_add_f32 v30, v54, v55
v_add_f32 v29, v29, v68
v_add_f32 v29, v29, v30
s_nop 1
v_add_f32_dpp v29, v29, v29 quad_perm:[1,0,3,2] row_mask:0xf bank_mask:0xf
s_nop 1
v_add_f32_dpp v29, v29, v29 quad_perm:[2,3,0,1] row_mask:0xf bank_mask:0xf
s_nop 1
v_add_f32_dpp v29, v29, v29 row_half_mirror row_mask:0xf bank_mask:0xf
v_rcp_f32 v29, v29
s_nop 1
v_mul_f32_dpp v30, v68, v29 quad_perm:[1,0,3,2] row_mask:0xf bank_mask:0xf
v_mul_f32 v52, v52, v29
v_mul_f32 v53, v53, v29
v_mul_f32 v54, v54, v29
v_mul_f32 v55, v55, v29
v_mul_f32 v68, v68, v29
v_cvt_pk_bf16_f32 v52, v52, v53
v_cvt_pk_bf16_f32 v53, v54, v55
v_cvt_pk_bf16_f32 v68, v68, v30
buffer_store_dwordx2 v[52:53], v27, s[96:99], s57 offen offset:0 sc1
buffer_store_dword v68, v26, s[96:99], s58 offen offset:0 sc1
s_waitcnt vmcnt(5)
ds_write_b128 v117, v[76:79] offset:53248
s_waitcnt vmcnt(4)
ds_write_b128 v117, v[80:83] offset:54272
s_waitcnt vmcnt(3)
ds_write_b64 v117, v[84:85] offset:55296
s_waitcnt vmcnt(2)
ds_write_b64 v117, v[86:87] offset:55304
s_cmp_eq_u32 s36, 0
s_cbranch_scc1 S1_w1_Z
v_readfirstlane_b32 s92, v40
s_nop 0
s_cmp_lg_u32 s92, -1
s_cbranch_scc1 S1_w1_Z
s_nop 1
v_mov_b32 v1, 0
s_add_u32 s42, s37, 16384
S1_w1_S:
buffer_load_dword v0, v1, s[20:23], s42 offen offset:3076 sc1
s_waitcnt vmcnt(0)
v_readfirstlane_b32 s92, v0
s_nop 0
s_cmp_lg_u32 s92, -1
s_cbranch_scc1 S1_w1_SK
s_sleep 2
s_sub_u32 s41, s41, 1
s_cbranch_scc0 S1_w1_S
s_mov_b32 s41, 0
S1_w1_SK:
S1_w1_Z:
s_add_u32 s36, s36, 1
s_cmp_lt_u32 s36, 64
s_cbranch_scc1 S1_w1_T
s_waitcnt vmcnt(0)
v_mov_b32 v42, s36
buffer_store_dword v42, v114, s[96:99], s59 offen offset:0 sc1
s_branch S1_END
S1_w1_FA:
s_sub_u32 s41, s41, 1
s_cbranch_scc0 S1_w1_L
s_mov_b32 s41, 0
s_branch S1_w1_HA
S1_w2:
v_bfe_u32 v1, v116, 4, 3
s_lshl_b32 s42, s54, 7
s_add_u32 s42, s42, 4198400
v_lshl_add_u32 v1, v1, 2, s42
s_nop 1
S1_w2_K0:
buffer_load_dword v0, v1, s[96:99], 0 offen offset:0 sc1
s_waitcnt vmcnt(0)
v_cmp_gt_u32 vcc, 1, v0
s_cbranch_vccz S1_w2_K0K
s_sleep 3
s_sub_u32 s41, s41, 1
s_cbranch_scc0 S1_w2_K0
s_mov_b32 s41, 0
S1_w2_K0K:
s_add_u32 s42, s36, 0
s_min_u32 s42, s42, 63
s_lshl_b32 s42, s42, 5
s_add_u32 s42, s42, s54
s_mul_i32 s42, s42, 12288
s_add_u32 s43, s42, 8398848
s_add_u32 s42, s42, 8392704
buffer_load_dwordx4 v[104:107], v116, s[96:99], s42 offen offset:0 sc1
buffer_load_dwordx4 v[108:111], v116, s[96:99], s42 offen offset:1024 sc1
buffer_load_dwordx2 v[100:101], v127, s[96:99], s43 offen offset:0 sc1
buffer_load_dwordx2 v[102:103], v127, s[96:99], s43 offen offset:512 sc1
s_add_u32 s42, s42, 25165824
s_add_u32 s43, s43, 25165824
buffer_load_dwordx4 v[76:79], v116, s[96:99], s42 offen offset:0 sc1
buffer_load_dwordx4 v[80:83], v116, s[96:99], s42 offen offset:1024 sc1
buffer_load_dwordx2 v[84:85], v127, s[96:99], s43 offen offset:0 sc1
buffer_load_dwordx2 v[86:87], v127, s[96:99], s43 offen offset:512 sc1
s_waitcnt vmcnt(3)
ds_write_b128 v117, v[76:79] offset:57344
s_waitcnt vmcnt(2)
ds_write_b128 v117, v[80:83] offset:58368
s_waitcnt vmcnt(1)
ds_write_b64 v117, v[84:85] offset:59392
s_waitcnt vmcnt(0)
ds_write_b64 v117, v[86:87] offset:59400
S1_w2_T:
s_add_u32 s42, s36, 0
s_lshl_b32 s37, s42, 16
s_lshr_b32 s43, s37, 8
s_xor_b32 s43, s43, s39
s_and_b32 s43, s43, 8192
s_or_b32 s37, s37, s43
s_add_u32 s57, s37, 4096
s_add_u32 s58, s37, 16384
v_mov_b32 v52, 0
v_mov_b32 v53, 0
v_mov_b32 v54, 0
v_mov_b32 v55, 0
v_mov_b32 v56, 0
v_mov_b32 v57, 0
v_mov_b32 v58, 0
v_mov_b32 v59, 0
v_mov_b32 v60, 0
v_mov_b32 v61, 0
v_mov_b32 v62, 0
v_mov_b32 v63, 0
v_mov_b32 v64, 0
v_mov_b32 v65, 0
v_mov_b32 v66, 0
v_mov_b32 v67, 0
v_mov_b32 v68, 0
v_mov_b32 v69, 0
v_mov_b32 v70, 0
v_mov_b32 v71, 0
v_mov_b32 v72, 0
v_mov_b32 v73, 0
v_mov_b32 v74, 0
v_mov_b32 v75, 0
ds_read_b128 v[76:79], v117 offset:8192
ds_read_b128 v[80:83], v117 offset:9216
ds_read_b128 v[84:87], v117 offset:10240
ds_read_b128 v[88:91], v117 offset:11264
ds_read_b128 v[92:95], v117 offset:24576
ds_read_b128 v[96:99], v117 offset:25600
s_waitcnt lgkmcnt(0)
S1_w2_W:
s_memrealtime s[94:95]
s_waitcnt lgkmcnt(0)
s_sub_u32 s94, s92, s94
s_lshl_b32 s94, s94, 1
s_cmp_gt_i32 s94, 20000
s_cbranch_scc1 S1_w2_WG
s_cmp_gt_i32 s94, 16
s_cbranch_scc1 S1_w2_W
s_cmp_lt_i32 s94, 6
s_cbranch_scc1 S1_w2_WG
s_nop 3
s_cmp_lt_i32 s94, 8
s_cbranch_scc1 S1_w2_WG
s_nop 3
s_cmp_lt_i32 s94, 10
s_cbranch_scc1 S1_w2_WG
s_nop 3
s_cmp_lt_i32 s94, 12
s_cbranch_scc1 S1_w2_WG
s_nop 3
s_cmp_lt_i32 s94, 14
s_cbranch_scc1 S1_w2_WG
s_nop 3
s_cmp_lt_i32 s94, 16
s_cbranch_scc1 S1_w2_WG
s_nop 3
S1_w2_WG:
S1_w2_L:
buffer_load_dwordx4 v[32:35], v116, s[20:23], s57 offen offset:0 sc1
buffer_load_dwordx4 v[36:39], v116, s[20:23], s57 offen offset:1024 sc1
buffer_load_dwordx2 v[40:41], v118, s[20:23], s58 offen offset:1024 sc1
s_waitcnt vmcnt(2)
v_max3_u32 v0, v32, v33, v34
s_waitcnt vmcnt(1)
v_max3_u32 v0, v0, v35, v36
v_max3_u32 v0, v0, v37, v38
s_waitcnt vmcnt(0)
v_max3_u32 v0, v0, v39, v40
v_max_u32 v0, v0, v41
v_cmp_eq_u32 vcc, -1, v0
s_cbranch_vccnz S1_w2_FA
S1_w2_HA:
ds_write_b128 v116, v[32:35] offset:53248
ds_write_b128 v116, v[36:39] offset:54272
ds_write_b64 v118, v[40:41] offset:58368
v_smfmac_f32_16x16x64_bf16 v[52:55], v[32:35], a[192:199], v119
v_smfmac_f32_16x16x64_bf16 v[56:59], v[32:35], a[200:207], v119
v_smfmac_f32_16x16x64_bf16 v[60:63], v[32:35], a[208:215], v119
v_smfmac_f32_16x16x64_bf16 v[52:55], v[36:39], a[240:247], v119
s_waitcnt lgkmcnt(0)
s_barrier
ds_read_b128 v[0:3], v116 offset:57344
v_smfmac_f32_16x16x64_bf16 v[56:59], v[36:39], a[248:255], v119
ds_read_b128 v[4:7], v116 offset:55296
v_smfmac_f32_16x16x64_bf16 v[60:63], v[36:39], v[128:135], v119
ds_read_b128 v[8:11], v116 offset:56320
v_smfmac_f32_16x16x64_bf16 v[64:67], v[32:35], a[224:231], v119
ds_read_b128 v[12:15], v116 offset:49152
v_smfmac_f32_16x16x64_bf16 v[68:71], v[32:35], a[232:239], v119
ds_read_b128 v[16:19], v116 offset:50176
v_smfmac_f32_16x16x64_bf16 v[72:75], v[32:35], a[216:223], v119
ds_read_b128 v[20:23], v116 offset:51200
v_smfmac_f32_16x16x64_bf16 v[64:67], v[36:39], v[144:151], v119
ds_read_b128 v[24:27], v116 offset:52224
v_smfmac_f32_16x16x64_bf16 v[68:71], v[36:39], v[152:159], v119
ds_read_b128 v[28:31], v116 offset:58368
v_smfmac_f32_16x16x64_bf16 v[72:75], v[36:39], v[136:143], v119
S1_w2_C:
s_waitcnt lgkmcnt(7)
v_smfmac_f32_16x16x64_bf16 v[52:55], v[0:3], v[76:83], v119
ds_read_b128 v[76:79], v116 offset:8192
v_smfmac_f32_16x16x64_bf16 v[56:59], v[0:3], v[84:91], v119
s_waitcnt lgkmcnt(7)
ds_read_b128 v[80:83], v116 offset:9216
v_smfmac_f32_16x16x64_bf16 v[52:55], v[4:7], v[160:167], v119
ds_read_b128 v[84:87], v116 offset:10240
v_smfmac_f32_16x16x64_bf16 v[56:59], v[4:7], v[168:175], v119
s_waitcnt lgkmcnt(8)
ds_read_b128 v[88:91], v116 offset:11264
v_smfmac_f32_16x16x64_bf16 v[52:55], v[8:11], v[208:215], v119
v_smfmac_f32_16x16x64_bf16 v[56:59], v[8:11], v[216:223], v119
s_waitcnt lgkmcnt(8)
v_smfmac_f32_16x16x64_bf16 v[52:55], v[12:15], a[0:7], v119
v_smfmac_f32_16x16x64_bf16 v[56:59], v[12:15], a[8:15], v119
s_waitcnt lgkmcnt(7)
v_smfmac_f32_16x16x64_bf16 v[52:55], v[16:19], a[48:55], v119
v_smfmac_f32_16x16x64_bf16 v[56:59], v[16:19], a[56:63], v119
s_waitcnt lgkmcnt(6)
v_smfmac_f32_16x16x64_bf16 v[52:55], v[20:23], a[96:103], v119
v_smfmac_f32_16x16x64_bf16 v[56:59], v[20:23], a[104:111], v119
s_waitcnt lgkmcnt(5)
v_smfmac_f32_16x16x64_bf16 v[52:55], v[24:27], a[144:151], v119
v_smfmac_f32_16x16x64_bf16 v[56:59], v[24:27], a[152:159], v119
s_waitcnt lgkmcnt(2)
v_smfmac_f32_16x16x64_bf16 v[52:55], v[28:31], v[76:83], v119
s_waitcnt lgkmcnt(0)
ds_read_b128 v[76:79], v116 offset:24576
v_smfmac_f32_16x16x64_bf16 v[56:59], v[28:31], v[84:91], v119
ds_read_b128 v[80:83], v116 offset:25600
v_smfmac_f32_16x16x64_bf16 v[60:63], v[0:3], v[92:99], v119
ds_read_b128 v[84:87], v117 offset:40960
v_smfmac_f32_16x16x64_bf16 v[60:63], v[4:7], v[176:183], v119
ds_read_b128 v[88:91], v117 offset:41984
v_add_f32 v52, v52, v53
v_smfmac_f32_16x16x64_bf16 v[60:63], v[8:11], v[224:231], v119
ds_read_b128 v[92:95], v117 offset:43008
v_add_f32 v54, v54, v55
v_smfmac_f32_16x16x64_bf16 v[60:63], v[12:15], a[16:23], v119
ds_read_b128 v[96:99], v117 offset:44032
v_cvt_pk_bf16_f32 v52, v52, v54
v_smfmac_f32_16x16x64_bf16 v[60:63], v[16:19], a[64:71], v119
ds_write_b32 v120, v52 offset:1024
v_add_f32 v56, v56, v57
v_smfmac_f32_16x16x64_bf16 v[60:63], v[20:23], a[112:119], v119
v_add_f32 v58, v58, v59
v_smfmac_f32_16x16x64_bf16 v[60:63], v[24:27], a[160:167], v119
s_waitcnt lgkmcnt(5)
v_cvt_pk_bf16_f32 v56, v56, v58
v_smfmac_f32_16x16x64_bf16 v[60:63], v[28:31], v[76:83], v119
s_waitcnt lgkmcnt(3)
ds_read_b128 v[76:79], v116 offset:40960
ds_write_b32 v120, v56 offset:1280
v_smfmac_f32_16x16x64_bf16 v[64:67], v[0:3], v[84:91], v119
s_waitcnt lgkmcnt(3)
ds_read_b128 v[80:83], v116 offset:41984
v_smfmac_f32_16x16x64_bf16 v[68:71], v[0:3], v[92:99], v119
ds_read_b128 v[84:87], v116 offset:43008
v_smfmac_f32_16x16x64_bf16 v[64:67], v[4:7], v[192:199], v119
ds_read_b128 v[88:91], v116 offset:44032
v_add_f32 v60, v60, v61
v_smfmac_f32_16x16x64_bf16 v[68:71], v[4:7], v[200:207], v119
ds_read_b128 v[92:95], v117 offset:26624
v_add_f32 v62, v62, v63
v_smfmac_f32_16x16x64_bf16 v[64:67], v[8:11], v[240:247], v119
ds_read_b128 v[96:99], v117 offset:27648
v_cvt_pk_bf16_f32 v60, v60, v62
v_smfmac_f32_16x16x64_bf16 v[68:71], v[8:11], v[248:255], v119
ds_write_b32 v121, v60 offset:4608
v_mov_b32 v42, s36
v_smfmac_f32_16x16x64_bf16 v[64:67], v[12:15], a[32:39], v119
buffer_store_dword v42, v114, s[96:99], s59 offen offset:0 sc1
ds_read_b64_tr_b16 v[32:33], v122 offset:1024
ds_read_b64_tr_b16 v[34:35], v122 offset:1088
v_smfmac_f32_16x16x64_bf16 v[68:71], v[12:15], a[40:47], v119
ds_read_b64_tr_b16 v[112:113], v122 offset:4608
v_smfmac_f32_16x16x64_bf16 v[64:67], v[16:19], a[80:87], v119
v_smfmac_f32_16x16x64_bf16 v[68:71], v[16:19], a[88:95], v119
v_smfmac_f32_16x16x64_bf16 v[64:67], v[20:23], a[128:135], v119
v_smfmac_f32_16x16x64_bf16 v[68:71], v[20:23], a[136:143], v119
v_smfmac_f32_16x16x64_bf16 v[64:67], v[24:27], a[176:183], v119
v_smfmac_f32_16x16x64_bf16 v[68:71], v[24:27], a[184:191], v119
s_waitcnt lgkmcnt(8)
v_smfmac_f32_16x16x64_bf16 v[64:67], v[28:31], v[76:83], v119
s_waitcnt lgkmcnt(6)
ds_read_b128 v[76:79], v116 offset:26624
v_smfmac_f32_16x16x64_bf16 v[68:71], v[28:31], v[84:91], v119
s_waitcnt lgkmcnt(5)
ds_read_b128 v[80:83], v116 offset:27648
v_smfmac_f32_16x16x64_bf16 v[72:75], v[0:3], v[92:99], v119
s_waitcnt lgkmcnt(3)
v_mfma_f32_16x16x32_bf16 v[52:55], v[104:107], v[32:35], 0
v_add_f32 v64, v64, v65
v_add_f32 v66, v66, v67
v_mfma_f32_16x16x32_bf16 v[56:59], v[108:111], v[32:35], 0
s_waitcnt lgkmcnt(2)
v_cvt_pk_bf16_f32 v64, v64, v66
v_mfma_f32_16x16x32_bf16 v[52:55], v[100:103], v[112:115], v[52:55]
ds_write_b32 v120, v64 offset:3072
v_add_f32 v68, v68, v69
v_add_f32 v70, v70, v71
v_mfma_f32_16x16x32_bf16 v[56:59], v[102:105], v[112:115], v[56:59]
v_cvt_pk_bf16_f32 v68, v68, v70
v_smfmac_f32_16x16x64_bf16 v[72:75], v[4:7], v[184:191], v119
ds_write_b32 v120, v68 offset:3328
ds_read_b64_tr_b16 v[48:49], v122 offset:3072
ds_read_b64_tr_b16 v[50:51], v122 offset:3136
v_cndmask_b32 v52, v52, v54, s[78:79]
v_cndmask_b32 v53, v53, v55, s[78:79]
v_smfmac_f32_16x16x64_bf16 v[72:75], v[8:11], v[232:239], v119
ds_write_b64 v123, v[52:53] offset:2560
v_cndmask_b32 v56, v56, v58, s[78:79]
v_cndmask_b32 v57, v57, v59, s[78:79]
v_smfmac_f32_16x16x64_bf16 v[72:75], v[12:15], a[24:31], v119
ds_write_b64 v123, v[56:57] offset:3200
v_smfmac_f32_16x16x64_bf16 v[72:75], v[16:19], a[72:79], v119
v_smfmac_f32_16x16x64_bf16 v[72:75], v[20:23], a[120:127], v119
v_smfmac_f32_16x16x64_bf16 v[72:75], v[24:27], a[168:175], v119
s_waitcnt lgkmcnt(6)
s_waitcnt lgkmcnt(0)
s_barrier
v_smfmac_f32_16x16x64_bf16 v[72:75], v[28:31], v[76:83], v119
v_mfma_f32_16x16x32_bf16 v[60:63], v[32:35], v[48:51], 0
s_nop 6
v_add_f32 v72, v72, v73
v_add_f32 v74, v74, v75
v_cvt_pk_bf16_f32 v72, v72, v74
ds_write_b32 v121, v72 offset:5632
v_cndmask_b32 v60, v60, v61, s[78:79]
v_cndmask_b32 v62, v62, v63, s[78:79]
v_cndmask_b32 v60, v60, v62, s[76:77]
ds_write_b32 v118, v60 offset:59904
s_waitcnt lgkmcnt(0)
s_barrier
s_add_u32 s42, s37, 16384
buffer_load_dword v40, v114, s[20:23], s42 offen offset:3076 sc1
v_bfe_u32 v42, v116, 4, 3
s_lshl_b32 s42, s54, 7
s_add_u32 s42, s42, 4198400
v_lshl_add_u32 v42, v42, 2, s42
buffer_load_dword v41, v42, s[96:99], 0 offen offset:0 sc1
ds_read_b128 v[0:3], v117 offset:57344
ds_read_b128 v[4:7], v117 offset:58368
ds_read_b64 v[8:9], v117 offset:59392
ds_read_b64 v[12:13], v117 offset:59400
ds_read_b64_tr_b16 v[16:17], v122 offset:5632
v_mov_b32 v10, 0
v_mov_b32 v11, 0
v_mov_b32 v14, 0
v_mov_b32 v15, 0
v_mov_b32 v18, 0
v_mov_b32 v19, 0
s_waitcnt lgkmcnt(4)
v_mfma_f32_16x16x32_bf16 v[52:55], v[48:51], v[0:3], 0
s_waitcnt lgkmcnt(3)
v_mfma_f32_16x16x32_bf16 v[56:59], v[48:51], v[4:7], 0
v_mfma_f32_16x16x32_bf16 v[60:63], v[104:107], v[0:3], 0
v_mfma_f32_16x16x32_bf16 v[64:67], v[104:107], v[4:7], 0
v_mfma_f32_16x16x32_bf16 v[68:71], v[108:111], v[4:7], 0
s_waitcnt lgkmcnt(0)
v_mfma_f32_16x16x32_bf16 v[52:55], v[16:19], v[8:11], v[52:55]
v_mfma_f32_16x16x32_bf16 v[56:59], v[16:19], v[12:15], v[56:59]
v_mfma_f32_16x16x32_bf16 v[60:63], v[100:103], v[8:11], v[60:63]
v_mfma_f32_16x16x32_bf16 v[64:67], v[100:103], v[12:15], v[64:67]
v_mfma_f32_16x16x32_bf16 v[68:71], v[102:105], v[12:15], v[68:71]
v_lshrrev_b32 v22, 7, v116
v_bfe_u32 v23, v116, 5, 2
v_bfe_u32 v24, v116, 4, 1
v_lshlrev_b32 v20, 4, v22
v_lshl_add_u32 v20, v23, 8, v20
v_lshl_add_u32 v20, v24, 10, v20
v_add_u32 v20, 77824, v20
v_lshlrev_b32 v21, 4, v22
v_bfe_u32 v25, v116, 6, 1
v_lshl_add_u32 v21, v25, 8, v21
v_bfe_u32 v25, v116, 4, 2
v_lshl_add_u32 v21, v25, 2, v21
v_add_u32 v21, 75776, v21
s_add_u32 s58, s36, 2
s_min_u32 s58, s58, 64
ds_write_b128 v43, v[52:55] offset:10240
ds_write_b128 v43, v[56:59] offset:11264
ds_write_b128 v43, v[60:63] offset:12288
ds_write_b128 v43, v[64:67] offset:13312
ds_write_b128 v43, v[68:71] offset:14336
s_waitcnt vmcnt(0)
v_cmp_gt_u32 vcc, s58, v41
s_cbranch_vccz S1_w2_KK
s_nop 3
S1_w2_K:
buffer_load_dword v41, v42, s[96:99], 0 offen offset:0 sc1
s_waitcnt vmcnt(0)
v_cmp_gt_u32 vcc, s58, v41
s_cbranch_vccz S1_w2_KK
s_sleep 3
s_sub_u32 s41, s41, 1
s_cbranch_scc0 S1_w2_K
s_mov_b32 s41, 0
S1_w2_KK:
s_add_u32 s42, s36, 1
s_min_u32 s42, s42, 63
s_lshl_b32 s42, s42, 5
s_add_u32 s42, s42, s54
s_mul_i32 s42, s42, 12288
s_add_u32 s43, s42, 8398848
s_add_u32 s42, s42, 8392704
buffer_load_dwordx4 v[104:107], v116, s[96:99], s42 offen offset:0 sc1
buffer_load_dwordx4 v[108:111], v116, s[96:99], s42 offen offset:1024 sc1
buffer_load_dwordx2 v[100:101], v127, s[96:99], s43 offen offset:0 sc1
buffer_load_dwordx2 v[102:103], v127, s[96:99], s43 offen offset:512 sc1
s_add_u32 s42, s42, 25165824
s_add_u32 s43, s43, 25165824
buffer_load_dwordx4 v[76:79], v116, s[96:99], s42 offen offset:0 sc1
buffer_load_dwordx4 v[80:83], v116, s[96:99], s42 offen offset:1024 sc1
buffer_load_dwordx2 v[84:85], v127, s[96:99], s43 offen offset:0 sc1
buffer_load_dwordx2 v[86:87], v127, s[96:99], s43 offen offset:512 sc1
v_lshlrev_b32 v26, 4, v22
v_and_b32 v25, 1, v23
v_lshl_add_u32 v27, v25, 9, v26
v_lshrrev_b32 v25, 1, v23
v_lshl_add_u32 v27, v25, 10, v27
v_lshl_add_u32 v27, v24, 3, v27
v_lshl_add_u32 v26, v23, 2, v26
v_lshl_add_u32 v26, v24, 30, v26
v_lshlrev_b32 v25, 2, v23
v_sub_u32 v22, v22, v25
v_lshlrev_b32 v25, 4, v24
v_sub_u32 v22, v22, v25
v_add_u32 v22, 16, v22
s_waitcnt lgkmcnt(0)
s_barrier
ds_read_b128 v[52:55], v20 offset:0
ds_read_b128 v[56:59], v20 offset:5120
ds_read_b128 v[60:63], v20 offset:10240
ds_read_b128 v[64:67], v20 offset:15360
ds_read_b32 v68, v21 offset:0
ds_read_b32 v69, v21 offset:5120
ds_read_b32 v70, v21 offset:10240
ds_read_b32 v71, v21 offset:15360
s_mul_i32 s42, s36, 81920
s_add_u32 s42, s42, 234881280
s_lshl_b32 s43, s54, 11
s_add_u32 s57, s42, s43
s_lshl_b32 s43, s54, 9
s_add_u32 s58, s42, s43
s_add_u32 s58, s58, 65536
v_mov_b32 v28, 0xff800000
s_waitcnt lgkmcnt(6)
v_add_f32 v52, v52, v56
s_waitcnt lgkmcnt(4)
v_add_f32 v60, v60, v64
v_add_f32 v52, v52, v60
v_cmp_le_i32 vcc, 0, v22
s_nop 1
v_cndmask_b32 v52, v28, v52, vcc
v_add_f32 v53, v53, v57
v_add_f32 v61, v61, v65
v_add_f32 v53, v53, v61
v_cmp_le_i32 vcc, 1, v22
s_nop 1
v_cndmask_b32 v53, v28, v53, vcc
v_add_f32 v54, v54, v58
v_add_f32 v62, v62, v66
v_add_f32 v54, v54, v62
v_cmp_le_i32 vcc, 2, v22
s_nop 1
v_cndmask_b32 v54, v28, v54, vcc
v_add_f32 v55, v55, v59
v_add_f32 v63, v63, v67
v_add_f32 v55, v55, v63
v_cmp_le_i32 vcc, 3, v22
s_nop 1
v_cndmask_b32 v55, v28, v55, vcc
s_waitcnt lgkmcnt(2)
v_add_f32 v68, v68, v69
s_waitcnt lgkmcnt(0)
v_add_f32 v70, v70, v71
v_add_f32 v68, v68, v70
v_max3_f32 v29, v52, v53, v54
v_max3_f32 v29, v29, v55, v68
s_nop 1
v_max_f32_dpp v29, v29, v29 quad_perm:[1,0,3,2] row_mask:0xf bank_mask:0xf
s_nop 1
v_max_f32_dpp v29, v29, v29 quad_perm:[2,3,0,1] row_mask:0xf bank_mask:0xf
s_nop 1
v_max_f32_dpp v29, v29, v29 row_half_mirror row_mask:0xf bank_mask:0xf
v_mul_f32 v29, s56, v29
v_fma_f32 v52, v52, s56, -v29
v_fma_f32 v53, v53, s56, -v29
v_fma_f32 v54, v54, s56, -v29
v_fma_f32 v55, v55, s56, -v29
v_fma_f32 v68, v68, s56, -v29
v_exp_f32 v52, v52
v_exp_f32 v53, v53
v_exp_f32 v54, v54
v_exp_f32 v55, v55
v_exp_f32 v68, v68
v_add_f32 v29, v52, v53
v_add_f32 v30, v54, v55
v_add_f32 v29, v29, v68
v_add_f32 v29, v29, v30
s_nop 1
v_add_f32_dpp v29, v29, v29 quad_perm:[1,0,3,2] row_mask:0xf bank_mask:0xf
s_nop 1
v_add_f32_dpp v29, v29, v29 quad_perm:[2,3,0,1] row_mask:0xf bank_mask:0xf
s_nop 1
v_add_f32_dpp v29, v29, v29 row_half_mirror row_mask:0xf bank_mask:0xf
v_rcp_f32 v29, v29
s_nop 1
v_mul_f32_dpp v30, v68, v29 quad_perm:[1,0,3,2] row_mask:0xf bank_mask:0xf
v_mul_f32 v52, v52, v29
v_mul_f32 v53, v53, v29
v_mul_f32 v54, v54, v29
v_mul_f32 v55, v55, v29
v_mul_f32 v68, v68, v29
v_cvt_pk_bf16_f32 v52, v52, v53
v_cvt_pk_bf16_f32 v53, v54, v55
v_cvt_pk_bf16_f32 v68, v68, v30
buffer_store_dwordx2 v[52:53], v27, s[96:99], s57 offen offset:0 sc1
buffer_store_dword v68, v26, s[96:99], s58 offen offset:0 sc1
s_waitcnt vmcnt(5)
ds_write_b128 v117, v[76:79] offset:57344
s_waitcnt vmcnt(4)
ds_write_b128 v117, v[80:83] offset:58368
s_waitcnt vmcnt(3)
ds_write_b64 v117, v[84:85] offset:59392
s_waitcnt vmcnt(2)
ds_write_b64 v117, v[86:87] offset:59400
s_cmp_eq_u32 s36, 0
s_cbranch_scc1 S1_w2_Z
v_readfirstlane_b32 s92, v40
s_nop 0
s_cmp_lg_u32 s92, -1
s_cbranch_scc1 S1_w2_Z
s_nop 1
v_mov_b32 v1, 0
s_add_u32 s42, s37, 16384
S1_w2_S:
buffer_load_dword v0, v1, s[20:23], s42 offen offset:3076 sc1
s_waitcnt vmcnt(0)
v_readfirstlane_b32 s92, v0
s_nop 0
s_cmp_lg_u32 s92, -1
s_cbranch_scc1 S1_w2_SK
s_sleep 2
s_sub_u32 s41, s41, 1
s_cbranch_scc0 S1_w2_S
s_mov_b32 s41, 0
S1_w2_SK:
S1_w2_Z:
s_add_u32 s36, s36, 1
s_cmp_lt_u32 s36, 64
s_cbranch_scc1 S1_w2_T
s_waitcnt vmcnt(0)
v_mov_b32 v42, s36
buffer_store_dword v42, v114, s[96:99], s59 offen offset:0 sc1
s_branch S1_END
S1_w2_FA:
s_sub_u32 s41, s41, 1
s_cbranch_scc0 S1_w2_L
s_mov_b32 s41, 0
s_branch S1_w2_HA
S1_w3:
v_bfe_u32 v1, v116, 4, 3
s_lshl_b32 s42, s54, 7
s_add_u32 s42, s42, 4198400
v_lshl_add_u32 v1, v1, 2, s42
s_nop 1
S1_w3_K0:
buffer_load_dword v0, v1, s[96:99], 0 offen offset:0 sc1
s_waitcnt vmcnt(0)
v_cmp_gt_u32 vcc, 1, v0
s_cbranch_vccz S1_w3_K0K
s_sleep 3
s_sub_u32 s41, s41, 1
s_cbranch_scc0 S1_w3_K0
s_mov_b32 s41, 0
S1_w3_K0K:
s_add_u32 s42, s36, 0
s_min_u32 s42, s42, 63
s_lshl_b32 s42, s42, 5
s_add_u32 s42, s42, s54
s_mul_i32 s42, s42, 12288
s_add_u32 s43, s42, 8399872
s_add_u32 s42, s42, 8394752
buffer_load_dwordx4 v[104:107], v116, s[96:99], s42 offen offset:0 sc1
buffer_load_dwordx4 v[108:111], v116, s[96:99], s42 offen offset:1024 sc1
buffer_load_dwordx2 v[100:101], v127, s[96:99], s43 offen offset:0 sc1
buffer_load_dwordx2 v[102:103], v127, s[96:99], s43 offen offset:512 sc1
s_add_u32 s42, s42, 25165824
s_add_u32 s43, s43, 25165824
buffer_load_dwordx4 v[76:79], v116, s[96:99], s42 offen offset:0 sc1
buffer_load_dwordx4 v[80:83], v116, s[96:99], s42 offen offset:1024 sc1
buffer_load_dwordx2 v[84:85], v127, s[96:99], s43 offen offset:0 sc1
buffer_load_dwordx2 v[86:87], v127, s[96:99], s43 offen offset:512 sc1
s_waitcnt vmcnt(3)
ds_write_b128 v117, v[76:79] offset:61440
s_waitcnt vmcnt(2)
ds_write_b128 v117, v[80:83] offset:62464
s_waitcnt vmcnt(1)
ds_write_b64 v117, v[84:85] offset:63488
s_waitcnt vmcnt(0)
ds_write_b64 v117, v[86:87] offset:63496
S1_w3_T:
s_add_u32 s42, s36, 0
s_lshl_b32 s37, s42, 16
s_lshr_b32 s43, s37, 8
s_xor_b32 s43, s43, s39
s_and_b32 s43, s43, 8192
s_or_b32 s37, s37, s43
s_add_u32 s57, s37, 4096
s_add_u32 s58, s37, 16384
v_mov_b32 v52, 0
v_mov_b32 v53, 0
v_mov_b32 v54, 0
v_mov_b32 v55, 0
v_mov_b32 v56, 0
v_mov_b32 v57, 0
v_mov_b32 v58, 0
v_mov_b32 v59, 0
v_mov_b32 v60, 0
v_mov_b32 v61, 0
v_mov_b32 v62, 0
v_mov_b32 v63, 0
v_mov_b32 v64, 0
v_mov_b32 v65, 0
v_mov_b32 v66, 0
v_mov_b32 v67, 0
v_mov_b32 v68, 0
v_mov_b32 v69, 0
v_mov_b32 v70, 0
v_mov_b32 v71, 0
v_mov_b32 v72, 0
v_mov_b32 v73, 0
v_mov_b32 v74, 0
v_mov_b32 v75, 0
ds_read_b128 v[76:79], v117 offset:12288
ds_read_b128 v[80:83], v117 offset:13312
ds_read_b128 v[84:87], v117 offset:14336
ds_read_b128 v[88:91], v117 offset:15360
ds_read_b128 v[92:95], v117 offset:28672
ds_read_b128 v[96:99], v117 offset:29696
s_waitcnt lgkmcnt(0)
S1_w3_W:
s_memrealtime s[94:95]
s_waitcnt lgkmcnt(0)
s_sub_u32 s94, s92, s94
s_lshl_b32 s94, s94, 1
s_cmp_gt_i32 s94, 20000
s_cbranch_scc1 S1_w3_WG
s_cmp_gt_i32 s94, 16
s_cbranch_scc1 S1_w3_W
s_cmp_lt_i32 s94, 6
s_cbranch_scc1 S1_w3_WG
s_nop 3
s_cmp_lt_i32 s94, 8
s_cbranch_scc1 S1_w3_WG
s_nop 3
s_cmp_lt_i32 s94, 10
s_cbranch_scc1 S1_w3_WG
s_nop 3
s_cmp_lt_i32 s94, 12
s_cbranch_scc1 S1_w3_WG
s_nop 3
s_cmp_lt_i32 s94, 14
s_cbranch_scc1 S1_w3_WG
s_nop 3
s_cmp_lt_i32 s94, 16
s_cbranch_scc1 S1_w3_WG
s_nop 3
S1_w3_WG:
S1_w3_L:
buffer_load_dwordx4 v[32:35], v116, s[20:23], s57 offen offset:2048 sc1
buffer_load_dwordx4 v[36:39], v116, s[20:23], s57 offen offset:3072 sc1
buffer_load_dwordx2 v[40:41], v118, s[20:23], s58 offen offset:1536 sc1
s_waitcnt vmcnt(2)
v_max3_u32 v0, v32, v33, v34
s_waitcnt vmcnt(1)
v_max3_u32 v0, v0, v35, v36
v_max3_u32 v0, v0, v37, v38
s_waitcnt vmcnt(0)
v_max3_u32 v0, v0, v39, v40
v_max_u32 v0, v0, v41
v_cmp_eq_u32 vcc, -1, v0
s_cbranch_vccnz S1_w3_FA
S1_w3_HA:
ds_write_b128 v116, v[32:35] offset:55296
ds_write_b128 v116, v[36:39] offset:56320
ds_write_b64 v118, v[40:41] offset:58880
v_smfmac_f32_16x16x64_bf16 v[52:55], v[32:35], v[160:167], v119
v_smfmac_f32_16x16x64_bf16 v[56:59], v[32:35], v[168:175], v119
v_smfmac_f32_16x16x64_bf16 v[60:63], v[32:35], v[176:183], v119
v_smfmac_f32_16x16x64_bf16 v[52:55], v[36:39], v[208:215], v119
s_waitcnt lgkmcnt(0)
s_barrier
ds_read_b128 v[0:3], v116 offset:57344
v_smfmac_f32_16x16x64_bf16 v[56:59], v[36:39], v[216:223], v119
ds_read_b128 v[4:7], v116 offset:49152
v_smfmac_f32_16x16x64_bf16 v[60:63], v[36:39], v[224:231], v119
ds_read_b128 v[8:11], v116 offset:50176
v_smfmac_f32_16x16x64_bf16 v[64:67], v[32:35], v[192:199], v119
ds_read_b128 v[12:15], v116 offset:51200
v_smfmac_f32_16x16x64_bf16 v[68:71], v[32:35], v[200:207], v119
ds_read_b128 v[16:19], v116 offset:52224
v_smfmac_f32_16x16x64_bf16 v[72:75], v[32:35], v[184:191], v119
ds_read_b128 v[20:23], v116 offset:53248
v_smfmac_f32_16x16x64_bf16 v[64:67], v[36:39], v[240:247], v119
ds_read_b128 v[24:27], v116 offset:54272
v_smfmac_f32_16x16x64_bf16 v[68:71], v[36:39], v[248:255], v119
ds_read_b128 v[28:31], v116 offset:58368
v_smfmac_f32_16x16x64_bf16 v[72:75], v[36:39], v[232:239], v119
S1_w3_C:
s_waitcnt lgkmcnt(7)
v_smfmac_f32_16x16x64_bf16 v[52:55], v[0:3], v[76:83], v119
ds_read_b128 v[76:79], v116 offset:12288
v_smfmac_f32_16x16x64_bf16 v[56:59], v[0:3], v[84:91], v119
s_waitcnt lgkmcnt(7)
ds_read_b128 v[80:83], v116 offset:13312
v_smfmac_f32_16x16x64_bf16 v[52:55], v[4:7], a[0:7], v119
ds_read_b128 v[84:87], v116 offset:14336
v_smfmac_f32_16x16x64_bf16 v[56:59], v[4:7], a[8:15], v119
s_waitcnt lgkmcnt(8)
ds_read_b128 v[88:91], v116 offset:15360
v_smfmac_f32_16x16x64_bf16 v[52:55], v[8:11], a[48:55], v119
v_smfmac_f32_16x16x64_bf16 v[56:59], v[8:11], a[56:63], v119
s_waitcnt lgkmcnt(8)
v_smfmac_f32_16x16x64_bf16 v[52:55], v[12:15], a[96:103], v119
v_smfmac_f32_16x16x64_bf16 v[56:59], v[12:15], a[104:111], v119
s_waitcnt lgkmcnt(7)
v_smfmac_f32_16x16x64_bf16 v[52:55], v[16:19], a[144:151], v119
v_smfmac_f32_16x16x64_bf16 v[56:59], v[16:19], a[152:159], v119
s_waitcnt lgkmcnt(6)
v_smfmac_f32_16x16x64_bf16 v[52:55], v[20:23], a[192:199], v119
v_smfmac_f32_16x16x64_bf16 v[56:59], v[20:23], a[200:207], v119
s_waitcnt lgkmcnt(5)
v_smfmac_f32_16x16x64_bf16 v[52:55], v[24:27], a[240:247], v119
v_smfmac_f32_16x16x64_bf16 v[56:59], v[24:27], a[248:255], v119
s_waitcnt lgkmcnt(2)
v_smfmac_f32_16x16x64_bf16 v[52:55], v[28:31], v[76:83], v119
s_waitcnt lgkmcnt(0)
ds_read_b128 v[76:79], v116 offset:28672
v_smfmac_f32_16x16x64_bf16 v[56:59], v[28:31], v[84:91], v119
ds_read_b128 v[80:83], v116 offset:29696
v_smfmac_f32_16x16x64_bf16 v[60:63], v[0:3], v[92:99], v119
ds_read_b128 v[84:87], v117 offset:45056
v_smfmac_f32_16x16x64_bf16 v[60:63], v[4:7], a[16:23], v119
ds_read_b128 v[88:91], v117 offset:46080
v_add_f32 v52, v52, v53
v_smfmac_f32_16x16x64_bf16 v[60:63], v[8:11], a[64:71], v119
ds_read_b128 v[92:95], v117 offset:47104
v_add_f32 v54, v54, v55
v_smfmac_f32_16x16x64_bf16 v[60:63], v[12:15], a[112:119], v119
ds_read_b128 v[96:99], v117 offset:48128
v_cvt_pk_bf16_f32 v52, v52, v54
v_smfmac_f32_16x16x64_bf16 v[60:63], v[16:19], a[160:167], v119
ds_write_b32 v120, v52 offset:1536
v_add_f32 v56, v56, v57
v_smfmac_f32_16x16x64_bf16 v[60:63], v[20:23], a[208:215], v119
v_add_f32 v58, v58, v59
v_smfmac_f32_16x16x64_bf16 v[60:63], v[24:27], v[128:135], v119
s_waitcnt lgkmcnt(5)
v_cvt_pk_bf16_f32 v56, v56, v58
v_smfmac_f32_16x16x64_bf16 v[60:63], v[28:31], v[76:83], v119
s_waitcnt lgkmcnt(3)
ds_read_b128 v[76:79], v116 offset:45056
ds_write_b32 v120, v56 offset:1792
v_smfmac_f32_16x16x64_bf16 v[64:67], v[0:3], v[84:91], v119
s_waitcnt lgkmcnt(3)
ds_read_b128 v[80:83], v116 offset:46080
v_smfmac_f32_16x16x64_bf16 v[68:71], v[0:3], v[92:99], v119
ds_read_b128 v[84:87], v116 offset:47104
v_smfmac_f32_16x16x64_bf16 v[64:67], v[4:7], a[32:39], v119
ds_read_b128 v[88:91], v116 offset:48128
v_add_f32 v60, v60, v61
v_smfmac_f32_16x16x64_bf16 v[68:71], v[4:7], a[40:47], v119
ds_read_b128 v[92:95], v117 offset:30720
v_add_f32 v62, v62, v63
v_smfmac_f32_16x16x64_bf16 v[64:67], v[8:11], a[80:87], v119
ds_read_b128 v[96:99], v117 offset:31744
v_cvt_pk_bf16_f32 v60, v60, v62
v_smfmac_f32_16x16x64_bf16 v[68:71], v[8:11], a[88:95], v119
ds_write_b32 v121, v60 offset:4672
v_mov_b32 v42, s36
v_smfmac_f32_16x16x64_bf16 v[64:67], v[12:15], a[128:135], v119
buffer_store_dword v42, v114, s[96:99], s59 offen offset:0 sc1
ds_read_b64_tr_b16 v[32:33], v122 offset:1536
ds_read_b64_tr_b16 v[34:35], v122 offset:1600
v_smfmac_f32_16x16x64_bf16 v[68:71], v[12:15], a[136:143], v119
ds_read_b64_tr_b16 v[112:113], v122 offset:4672
v_smfmac_f32_16x16x64_bf16 v[64:67], v[16:19], a[176:183], v119
v_smfmac_f32_16x16x64_bf16 v[68:71], v[16:19], a[184:191], v119
v_smfmac_f32_16x16x64_bf16 v[64:67], v[20:23], a[224:231], v119
v_smfmac_f32_16x16x64_bf16 v[68:71], v[20:23], a[232:239], v119
v_smfmac_f32_16x16x64_bf16 v[64:67], v[24:27], v[144:151], v119
v_smfmac_f32_16x16x64_bf16 v[68:71], v[24:27], v[152:159], v119
s_waitcnt lgkmcnt(8)
v_smfmac_f32_16x16x64_bf16 v[64:67], v[28:31], v[76:83], v119
s_waitcnt lgkmcnt(6)
ds_read_b128 v[76:79], v116 offset:30720
v_smfmac_f32_16x16x64_bf16 v[68:71], v[28:31], v[84:91], v119
s_waitcnt lgkmcnt(5)
ds_read_b128 v[80:83], v116 offset:31744
v_smfmac_f32_16x16x64_bf16 v[72:75], v[0:3], v[92:99], v119
s_waitcnt lgkmcnt(3)
v_mfma_f32_16x16x32_bf16 v[52:55], v[104:107], v[32:35], 0
v_add_f32 v64, v64, v65
v_add_f32 v66, v66, v67
v_mfma_f32_16x16x32_bf16 v[56:59], v[108:111], v[32:35], 0
s_waitcnt lgkmcnt(2)
v_cvt_pk_bf16_f32 v64, v64, v66
v_mfma_f32_16x16x32_bf16 v[52:55], v[100:103], v[112:115], v[52:55]
ds_write_b32 v120, v64 offset:3584
v_add_f32 v68, v68, v69
v_add_f32 v70, v70, v71
v_mfma_f32_16x16x32_bf16 v[56:59], v[102:105], v[112:115], v[56:59]
v_cvt_pk_bf16_f32 v68, v68, v70
v_smfmac_f32_16x16x64_bf16 v[72:75], v[4:7], a[24:31], v119
ds_write_b32 v120, v68 offset:3840
ds_read_b64_tr_b16 v[48:49], v122 offset:3584
ds_read_b64_tr_b16 v[50:51], v122 offset:3648
v_cndmask_b32 v52, v52, v54, s[78:79]
v_cndmask_b32 v53, v53, v55, s[78:79]
v_smfmac_f32_16x16x64_bf16 v[72:75], v[8:11], a[72:79], v119
ds_write_b64 v123, v[52:53] offset:3840
v_cndmask_b32 v56, v56, v58, s[78:79]
v_cndmask_b32 v57, v57, v59, s[78:79]
v_smfmac_f32_16x16x64_bf16 v[72:75], v[12:15], a[120:127], v119
ds_write_b64 v123, v[56:57] offset:4480
v_smfmac_f32_16x16x64_bf16 v[72:75], v[16:19], a[168:175], v119
v_smfmac_f32_16x16x64_bf16 v[72:75], v[20:23], a[216:223], v119
v_smfmac_f32_16x16x64_bf16 v[72:75], v[24:27], v[136:143], v119
s_waitcnt lgkmcnt(6)
s_waitcnt lgkmcnt(0)
s_barrier
v_smfmac_f32_16x16x64_bf16 v[72:75], v[28:31], v[76:83], v119
v_mfma_f32_16x16x32_bf16 v[60:63], v[32:35], v[48:51], 0
s_nop 6
v_add_f32 v72, v72, v73
v_add_f32 v74, v74, v75
v_cvt_pk_bf16_f32 v72, v72, v74
ds_write_b32 v121, v72 offset:5696
v_cndmask_b32 v60, v60, v61, s[78:79]
v_cndmask_b32 v62, v62, v63, s[78:79]
v_cndmask_b32 v60, v60, v62, s[76:77]
ds_write_b32 v118, v60 offset:59908
s_waitcnt lgkmcnt(0)
s_barrier
s_add_u32 s42, s37, 16384
buffer_load_dword v40, v114, s[20:23], s42 offen offset:3076 sc1
v_bfe_u32 v42, v116, 4, 3
s_lshl_b32 s42, s54, 7
s_add_u32 s42, s42, 4198400
v_lshl_add_u32 v42, v42, 2, s42
buffer_load_dword v41, v42, s[96:99], 0 offen offset:0 sc1
ds_read_b128 v[0:3], v117 offset:61440
ds_read_b128 v[4:7], v117 offset:62464
ds_read_b64 v[8:9], v117 offset:63488
ds_read_b64 v[12:13], v117 offset:63496
ds_read_b64_tr_b16 v[16:17], v122 offset:5696
v_mov_b32 v10, 0
v_mov_b32 v11, 0
v_mov_b32 v14, 0
v_mov_b32 v15, 0
v_mov_b32 v18, 0
v_mov_b32 v19, 0
s_waitcnt lgkmcnt(4)
v_mfma_f32_16x16x32_bf16 v[52:55], v[48:51], v[0:3], 0
s_waitcnt lgkmcnt(3)
v_mfma_f32_16x16x32_bf16 v[56:59], v[48:51], v[4:7], 0
v_mfma_f32_16x16x32_bf16 v[60:63], v[104:107], v[0:3], 0
v_mfma_f32_16x16x32_bf16 v[64:67], v[104:107], v[4:7], 0
v_mfma_f32_16x16x32_bf16 v[68:71], v[108:111], v[4:7], 0
s_waitcnt lgkmcnt(0)
v_mfma_f32_16x16x32_bf16 v[52:55], v[16:19], v[8:11], v[52:55]
v_mfma_f32_16x16x32_bf16 v[56:59], v[16:19], v[12:15], v[56:59]
v_mfma_f32_16x16x32_bf16 v[60:63], v[100:103], v[8:11], v[60:63]
v_mfma_f32_16x16x32_bf16 v[64:67], v[100:103], v[12:15], v[64:67]
v_mfma_f32_16x16x32_bf16 v[68:71], v[102:105], v[12:15], v[68:71]
v_lshrrev_b32 v22, 7, v116
v_bfe_u32 v23, v116, 5, 2
v_bfe_u32 v24, v116, 4, 1
v_lshlrev_b32 v20, 4, v22
v_lshl_add_u32 v20, v23, 8, v20
v_lshl_add_u32 v20, v24, 10, v20
v_add_u32 v20, 77952, v20
v_lshlrev_b32 v21, 4, v22
v_bfe_u32 v25, v116, 6, 1
v_lshl_add_u32 v21, v25, 8, v21
v_bfe_u32 v25, v116, 4, 2
v_lshl_add_u32 v21, v25, 2, v21
v_add_u32 v21, 75904, v21
s_add_u32 s58, s36, 2
s_min_u32 s58, s58, 64
ds_write_b128 v43, v[52:55] offset:15360
ds_write_b128 v43, v[56:59] offset:16384
ds_write_b128 v43, v[60:63] offset:17408
ds_write_b128 v43, v[64:67] offset:18432
ds_write_b128 v43, v[68:71] offset:19456
s_waitcnt vmcnt(0)
v_cmp_gt_u32 vcc, s58, v41
s_cbranch_vccz S1_w3_KK
s_nop 3
S1_w3_K:
buffer_load_dword v41, v42, s[96:99], 0 offen offset:0 sc1
s_waitcnt vmcnt(0)
v_cmp_gt_u32 vcc, s58, v41
s_cbranch_vccz S1_w3_KK
s_sleep 3
s_sub_u32 s41, s41, 1
s_cbranch_scc0 S1_w3_K
s_mov_b32 s41, 0
S1_w3_KK:
s_add_u32 s42, s36, 1
s_min_u32 s42, s42, 63
s_lshl_b32 s42, s42, 5
s_add_u32 s42, s42, s54
s_mul_i32 s42, s42, 12288
s_add_u32 s43, s42, 8399872
s_add_u32 s42, s42, 8394752
buffer_load_dwordx4 v[104:107], v116, s[96:99], s42 offen offset:0 sc1
buffer_load_dwordx4 v[108:111], v116, s[96:99], s42 offen offset:1024 sc1
buffer_load_dwordx2 v[100:101], v127, s[96:99], s43 offen offset:0 sc1
buffer_load_dwordx2 v[102:103], v127, s[96:99], s43 offen offset:512 sc1
s_add_u32 s42, s42, 25165824
s_add_u32 s43, s43, 25165824
buffer_load_dwordx4 v[76:79], v116, s[96:99], s42 offen offset:0 sc1
buffer_load_dwordx4 v[80:83], v116, s[96:99], s42 offen offset:1024 sc1
buffer_load_dwordx2 v[84:85], v127, s[96:99], s43 offen offset:0 sc1
buffer_load_dwordx2 v[86:87], v127, s[96:99], s43 offen offset:512 sc1
v_lshlrev_b32 v26, 4, v22
v_and_b32 v25, 1, v23
v_lshl_add_u32 v27, v25, 9, v26
v_lshrrev_b32 v25, 1, v23
v_lshl_add_u32 v27, v25, 10, v27
v_lshl_add_u32 v27, v24, 3, v27
v_lshl_add_u32 v26, v23, 2, v26
v_lshl_add_u32 v26, v24, 30, v26
v_lshlrev_b32 v25, 2, v23
v_sub_u32 v22, v22, v25
v_lshlrev_b32 v25, 4, v24
v_sub_u32 v22, v22, v25
v_add_u32 v22, 24, v22
s_waitcnt lgkmcnt(0)
s_barrier
ds_read_b128 v[52:55], v20 offset:0
ds_read_b128 v[56:59], v20 offset:5120
ds_read_b128 v[60:63], v20 offset:10240
ds_read_b128 v[64:67], v20 offset:15360
ds_read_b32 v68, v21 offset:0
ds_read_b32 v69, v21 offset:5120
ds_read_b32 v70, v21 offset:10240
ds_read_b32 v71, v21 offset:15360
s_mul_i32 s42, s36, 81920
s_add_u32 s42, s42, 234881408
s_lshl_b32 s43, s54, 11
s_add_u32 s57, s42, s43
s_lshl_b32 s43, s54, 9
s_add_u32 s58, s42, s43
s_add_u32 s58, s58, 65536
v_mov_b32 v28, 0xff800000
s_waitcnt lgkmcnt(6)
v_add_f32 v52, v52, v56
s_waitcnt lgkmcnt(4)
v_add_f32 v60, v60, v64
v_add_f32 v52, v52, v60
v_cmp_le_i32 vcc, 0, v22
s_nop 1
v_cndmask_b32 v52, v28, v52, vcc
v_add_f32 v53, v53, v57
v_add_f32 v61, v61, v65
v_add_f32 v53, v53, v61
v_cmp_le_i32 vcc, 1, v22
s_nop 1
v_cndmask_b32 v53, v28, v53, vcc
v_add_f32 v54, v54, v58
v_add_f32 v62, v62, v66
v_add_f32 v54, v54, v62
v_cmp_le_i32 vcc, 2, v22
s_nop 1
v_cndmask_b32 v54, v28, v54, vcc
v_add_f32 v55, v55, v59
v_add_f32 v63, v63, v67
v_add_f32 v55, v55, v63
v_cmp_le_i32 vcc, 3, v22
s_nop 1
v_cndmask_b32 v55, v28, v55, vcc
s_waitcnt lgkmcnt(2)
v_add_f32 v68, v68, v69
s_waitcnt lgkmcnt(0)
v_add_f32 v70, v70, v71
v_add_f32 v68, v68, v70
v_max3_f32 v29, v52, v53, v54
v_max3_f32 v29, v29, v55, v68
s_nop 1
v_max_f32_dpp v29, v29, v29 quad_perm:[1,0,3,2] row_mask:0xf bank_mask:0xf
s_nop 1
v_max_f32_dpp v29, v29, v29 quad_perm:[2,3,0,1] row_mask:0xf bank_mask:0xf
s_nop 1
v_max_f32_dpp v29, v29, v29 row_half_mirror row_mask:0xf bank_mask:0xf
v_mul_f32 v29, s56, v29
v_fma_f32 v52, v52, s56, -v29
v_fma_f32 v53, v53, s56, -v29
v_fma_f32 v54, v54, s56, -v29
v_fma_f32 v55, v55, s56, -v29
v_fma_f32 v68, v68, s56, -v29
v_exp_f32 v52, v52
v_exp_f32 v53, v53
v_exp_f32 v54, v54
v_exp_f32 v55, v55
v_exp_f32 v68, v68
v_add_f32 v29, v52, v53
v_add_f32 v30, v54, v55
v_add_f32 v29, v29, v68
v_add_f32 v29, v29, v30
s_nop 1
v_add_f32_dpp v29, v29, v29 quad_perm:[1,0,3,2] row_mask:0xf bank_mask:0xf
s_nop 1
v_add_f32_dpp v29, v29, v29 quad_perm:[2,3,0,1] row_mask:0xf bank_mask:0xf
s_nop 1
v_add_f32_dpp v29, v29, v29 row_half_mirror row_mask:0xf bank_mask:0xf
v_rcp_f32 v29, v29
s_nop 1
v_mul_f32_dpp v30, v68, v29 quad_perm:[1,0,3,2] row_mask:0xf bank_mask:0xf
v_mul_f32 v52, v52, v29
v_mul_f32 v53, v53, v29
v_mul_f32 v54, v54, v29
v_mul_f32 v55, v55, v29
v_mul_f32 v68, v68, v29
v_cvt_pk_bf16_f32 v52, v52, v53
v_cvt_pk_bf16_f32 v53, v54, v55
v_cvt_pk_bf16_f32 v68, v68, v30
buffer_store_dwordx2 v[52:53], v27, s[96:99], s57 offen offset:0 sc1
buffer_store_dword v68, v26, s[96:99], s58 offen offset:0 sc1
s_waitcnt vmcnt(5)
ds_write_b128 v117, v[76:79] offset:61440
s_waitcnt vmcnt(4)
ds_write_b128 v117, v[80:83] offset:62464
s_waitcnt vmcnt(3)
ds_write_b64 v117, v[84:85] offset:63488
s_waitcnt vmcnt(2)
ds_write_b64 v117, v[86:87] offset:63496
s_cmp_eq_u32 s36, 0
s_cbranch_scc1 S1_w3_Z
v_readfirstlane_b32 s92, v40
s_nop 0
s_cmp_lg_u32 s92, -1
s_cbranch_scc1 S1_w3_Z
s_nop 1
v_mov_b32 v1, 0
s_add_u32 s42, s37, 16384
S1_w3_S:
buffer_load_dword v0, v1, s[20:23], s42 offen offset:3076 sc1
s_waitcnt vmcnt(0)
v_readfirstlane_b32 s92, v0
s_nop 0
s_cmp_lg_u32 s92, -1
s_cbranch_scc1 S1_w3_SK
s_sleep 2
s_sub_u32 s41, s41, 1
s_cbranch_scc0 S1_w3_S
s_mov_b32 s41, 0
S1_w3_SK:
S1_w3_Z:
s_add_u32 s36, s36, 1
s_cmp_lt_u32 s36, 64
s_cbranch_scc1 S1_w3_T
s_waitcnt vmcnt(0)
v_mov_b32 v42, s36
buffer_store_dword v42, v114, s[96:99], s59 offen offset:0 sc1
s_branch S1_END
S1_w3_FA:
s_sub_u32 s41, s41, 1
s_cbranch_scc0 S1_w3_L
s_mov_b32 s41, 0
s_branch S1_w3_HA
S1_END:
s_endpgm
ROLE_SV:
s_mov_b64 s[48:49], %0
s_mov_b64 s[50:51], %1
s_mov_b64 s[52:53], %2
s_mov_b32 s54, %3
s_mov_b32 s40, %4
s_mov_b32 s39, %5
s_mov_b32 s41, 0x200000
s_mov_b32 s36, 0
s_lshl_b32 s55, s40, 10
v_mbcnt_lo_u32_b32 v31, -1, 0
v_mbcnt_hi_u32_b32 v31, -1, v31
s_lshl_b32 s42, s40, 8
s_add_u32 s42, s42, 147456
v_lshl_add_u32 v31, v31, 2, s42
ds_read_b32 v116, v31 offset:0
ds_read_b32 v118, v31 offset:1024
ds_read_b32 v119, v31 offset:2048
ds_read_b32 v120, v31 offset:3072
ds_read_b32 v121, v31 offset:4096
ds_read_b32 v122, v31 offset:5120
ds_read_b32 v123, v31 offset:6144
ds_read_b32 v0, v31 offset:7168
ds_read_b32 v1, v31 offset:8192
ds_read_b32 v2, v31 offset:9216
ds_read_b32 v3, v31 offset:10240
ds_read_b32 v4, v31 offset:11264
s_waitcnt lgkmcnt(0)
s_memrealtime s[94:95]
s_waitcnt lgkmcnt(0)
s_mov_b32 s92, s94
s_add_u32 s20, s48, 0x0
s_addc_u32 s21, s49, 0
s_and_b32 s21, s21, 0xffff
s_mov_b32 s22, 0x400000
s_mov_b32 s23, 0x20000
s_add_u32 s96, s48, 0x0
s_addc_u32 s97, s49, 0
s_and_b32 s97, s97, 0xffff
s_mov_b32 s98, 0xfe00000
s_mov_b32 s99, 0x20000
v_add_u32 v117, 98304, v116
v_add_u32 v7, s55, v116
v_add_u32 v7, s55, v7
v_add_u32 v7, s55, v7
v_add_u32 v7, s55, v7
v_add_co_u32 v8, vcc, s50, v0
v_mov_b32 v9, s51
s_nop 0
v_addc_co_u32 v9, vcc, 0, v9, vcc
s_nop 1
v_add_co_u32 v10, vcc, s50, v1
v_mov_b32 v11, s51
s_nop 0
v_addc_co_u32 v11, vcc, 0, v11, vcc
s_nop 1
v_add_co_u32 v12, vcc, s52, v2
v_mov_b32 v13, s53
s_nop 0
v_addc_co_u32 v13, vcc, 0, v13, vcc
s_lshl_b32 s42, s40, 6
s_lshl_b32 s43, s40, 5
s_xor_b32 s42, s42, s43
v_xor_b32 v5, s42, v3
v_xor_b32 v6, 128, v5
s_mov_b32 s44, 196608
s_mov_b32 s45, 0
s_mov_b32 s46, 983040
s_mov_b32 s47, 0
s_mov_b32 s76, 20480
s_mov_b32 s77, 0
s_mov_b32 s80, 0xfffc9100
s_mov_b32 s81, -1
s_add_u32 m0, s55, 0
s_nop 0
global_load_lds_dwordx4 v[8:9], off
v_lshl_add_u64 v[8:9], v[8:9], 0, s[44:45]
s_add_u32 m0, s55, 4096
s_nop 0
global_load_lds_dwordx4 v[8:9], off
v_lshl_add_u64 v[8:9], v[8:9], 0, s[44:45]
s_add_u32 m0, s55, 8192
s_nop 0
global_load_lds_dwordx4 v[8:9], off
v_lshl_add_u64 v[8:9], v[8:9], 0, s[44:45]
s_add_u32 m0, s55, 12288
s_nop 0
global_load_lds_dwordx4 v[8:9], off
v_lshl_add_u64 v[8:9], v[8:9], 0, s[44:45]
s_add_u32 m0, s55, 16384
s_nop 0
global_load_lds_dwordx4 v[8:9], off
v_lshl_add_u64 v[8:9], v[8:9], 0, s[44:45]
s_add_u32 m0, s55, 20480
s_nop 0
global_load_lds_dwordx4 v[8:9], off
v_lshl_add_u64 v[8:9], v[8:9], 0, s[44:45]
s_add_u32 m0, s55, 24576
s_nop 0
global_load_lds_dwordx4 v[8:9], off
v_lshl_add_u64 v[8:9], v[8:9], 0, s[44:45]
s_add_u32 m0, s55, 28672
s_nop 0
global_load_lds_dwordx4 v[8:9], off
v_lshl_add_u64 v[8:9], v[8:9], 0, s[44:45]
s_add_u32 m0, s55, 32768
s_nop 0
global_load_lds_dwordx4 v[8:9], off
v_lshl_add_u64 v[8:9], v[8:9], 0, s[44:45]
s_add_u32 m0, s55, 36864
s_nop 0
global_load_lds_dwordx4 v[8:9], off
v_lshl_add_u64 v[8:9], v[8:9], 0, s[44:45]
s_add_u32 m0, s55, 40960
s_nop 0
global_load_lds_dwordx4 v[8:9], off
v_lshl_add_u64 v[8:9], v[8:9], 0, s[44:45]
s_add_u32 m0, s55, 45056
s_nop 0
global_load_lds_dwordx4 v[8:9], off
v_lshl_add_u64 v[8:9], v[8:9], 0, s[44:45]
s_add_u32 m0, s55, 49152
s_nop 0
global_load_lds_dwordx4 v[8:9], off
v_lshl_add_u64 v[8:9], v[8:9], 0, s[44:45]
s_add_u32 m0, s55, 53248
s_nop 0
global_load_lds_dwordx4 v[8:9], off
v_lshl_add_u64 v[8:9], v[8:9], 0, s[44:45]
s_add_u32 m0, s55, 57344
s_nop 0
global_load_lds_dwordx4 v[8:9], off
v_lshl_add_u64 v[8:9], v[8:9], 0, s[44:45]
s_add_u32 m0, s55, 61440
s_nop 0
global_load_lds_dwordx4 v[8:9], off
v_lshl_add_u64 v[8:9], v[8:9], 0, s[44:45]
s_add_u32 m0, s55, 65536
s_nop 0
global_load_lds_dwordx4 v[8:9], off
v_lshl_add_u64 v[8:9], v[8:9], 0, s[44:45]
s_add_u32 m0, s55, 69632
s_nop 0
global_load_lds_dwordx4 v[8:9], off
v_lshl_add_u64 v[8:9], v[8:9], 0, s[44:45]
s_add_u32 m0, s55, 73728
s_nop 0
global_load_lds_dwordx4 v[8:9], off
v_lshl_add_u64 v[8:9], v[8:9], 0, s[44:45]
s_add_u32 m0, s55, 77824
s_nop 0
global_load_lds_dwordx4 v[8:9], off
v_lshl_add_u64 v[8:9], v[8:9], 0, s[44:45]
s_add_u32 m0, s55, 81920
s_nop 0
global_load_lds_dwordx4 v[8:9], off
v_lshl_add_u64 v[8:9], v[8:9], 0, s[44:45]
s_add_u32 m0, s55, 86016
s_nop 0
global_load_lds_dwordx4 v[8:9], off
v_lshl_add_u64 v[8:9], v[8:9], 0, s[44:45]
s_add_u32 m0, s55, 90112
s_nop 0
global_load_lds_dwordx4 v[8:9], off
v_lshl_add_u64 v[8:9], v[8:9], 0, s[44:45]
s_add_u32 m0, s55, 94208
s_nop 0
global_load_lds_dwordx4 v[8:9], off
v_lshl_add_u64 v[8:9], v[8:9], 0, s[44:45]
s_add_u32 m0, s55, 98304
s_nop 0
global_load_lds_dwordx4 v[8:9], off
v_lshl_add_u64 v[8:9], v[8:9], 0, s[44:45]
s_add_u32 m0, s55, 102400
s_nop 0
global_load_lds_dwordx4 v[8:9], off
v_lshl_add_u64 v[8:9], v[8:9], 0, s[44:45]
s_add_u32 m0, s55, 106496
s_nop 0
global_load_lds_dwordx4 v[8:9], off
v_lshl_add_u64 v[8:9], v[8:9], 0, s[44:45]
s_add_u32 m0, s55, 110592
s_nop 0
global_load_lds_dwordx4 v[8:9], off
v_lshl_add_u64 v[8:9], v[8:9], 0, s[44:45]
s_add_u32 m0, s55, 114688
s_nop 0
global_load_lds_dwordx4 v[8:9], off
v_lshl_add_u64 v[8:9], v[8:9], 0, s[44:45]
s_add_u32 m0, s55, 118784
s_nop 0
global_load_lds_dwordx4 v[8:9], off
v_lshl_add_u64 v[8:9], v[8:9], 0, s[44:45]
s_add_u32 m0, s55, 122880
s_nop 0
global_load_lds_dwordx4 v[8:9], off
v_lshl_add_u64 v[8:9], v[8:9], 0, s[44:45]
s_add_u32 m0, s55, 126976
s_nop 0
global_load_lds_dwordx4 v[8:9], off
v_lshl_add_u64 v[8:9], v[8:9], 0, s[44:45]
s_waitcnt vmcnt(28)
s_waitcnt lgkmcnt(0)
s_barrier
s_add_u32 m0, s55, 131072
s_nop 0
global_load_lds_dwordx4 v[10:11], off
v_lshl_add_u64 v[10:11], v[10:11], 0, s[44:45]
s_add_u32 m0, s55, 135168
s_nop 0
global_load_lds_dwordx4 v[10:11], off
v_lshl_add_u64 v[10:11], v[10:11], 0, s[44:45]
s_add_u32 m0, s55, 139264
s_nop 0
global_load_lds_dwordx4 v[10:11], off
v_lshl_add_u64 v[10:11], v[10:11], 0, s[44:45]
s_add_u32 m0, s55, 143360
s_nop 0
global_load_lds_dwordx4 v[10:11], off
v_lshl_add_u64 v[10:11], v[10:11], 0, s[46:47]
v_add_u32 v16, 0, v3
ds_read_b64_tr_b16 a[0:1], v16 offset:0
ds_read_b64_tr_b16 a[2:3], v16 offset:1024
ds_read_b64_tr_b16 a[4:5], v16 offset:8192
ds_read_b64_tr_b16 a[6:7], v16 offset:9216
v_add_u32 v17, 0, v4
ds_read_b64_tr_b16 a[8:9], v17 offset:0
ds_read_b64_tr_b16 a[10:11], v17 offset:1024
ds_read_b64_tr_b16 a[12:13], v17 offset:8192
ds_read_b64_tr_b16 a[14:15], v17 offset:9216
s_waitcnt vmcnt(28)
s_waitcnt lgkmcnt(0)
s_barrier
s_add_u32 m0, s55, 0
s_nop 0
global_load_lds_dwordx4 v[10:11], off
v_lshl_add_u64 v[10:11], v[10:11], 0, s[44:45]
s_add_u32 m0, s55, 4096
s_nop 0
global_load_lds_dwordx4 v[10:11], off
v_lshl_add_u64 v[10:11], v[10:11], 0, s[44:45]
s_add_u32 m0, s55, 8192
s_nop 0
global_load_lds_dwordx4 v[10:11], off
v_lshl_add_u64 v[10:11], v[10:11], 0, s[44:45]
s_add_u32 m0, s55, 12288
s_nop 0
global_load_lds_dwordx4 v[10:11], off
v_lshl_add_u64 v[10:11], v[10:11], 0, s[46:47]
v_add_u32 v16, 16384, v3
ds_read_b64_tr_b16 a[16:17], v16 offset:0
ds_read_b64_tr_b16 a[18:19], v16 offset:1024
ds_read_b64_tr_b16 a[20:21], v16 offset:8192
ds_read_b64_tr_b16 a[22:23], v16 offset:9216
v_add_u32 v17, 16384, v4
ds_read_b64_tr_b16 a[24:25], v17 offset:0
ds_read_b64_tr_b16 a[26:27], v17 offset:1024
ds_read_b64_tr_b16 a[28:29], v17 offset:8192
ds_read_b64_tr_b16 a[30:31], v17 offset:9216
s_waitcnt vmcnt(28)
s_waitcnt lgkmcnt(0)
s_barrier
s_add_u32 m0, s55, 16384
s_nop 0
global_load_lds_dwordx4 v[10:11], off
v_lshl_add_u64 v[10:11], v[10:11], 0, s[44:45]
s_add_u32 m0, s55, 20480
s_nop 0
global_load_lds_dwordx4 v[10:11], off
v_lshl_add_u64 v[10:11], v[10:11], 0, s[44:45]
s_add_u32 m0, s55, 24576
s_nop 0
global_load_lds_dwordx4 v[10:11], off
v_lshl_add_u64 v[10:11], v[10:11], 0, s[44:45]
s_add_u32 m0, s55, 28672
s_nop 0
global_load_lds_dwordx4 v[10:11], off
v_lshl_add_u64 v[10:11], v[10:11], 0, s[46:47]
v_add_u32 v16, 32768, v3
ds_read_b64_tr_b16 a[32:33], v16 offset:0
ds_read_b64_tr_b16 a[34:35], v16 offset:1024
ds_read_b64_tr_b16 a[36:37], v16 offset:8192
ds_read_b64_tr_b16 a[38:39], v16 offset:9216
v_add_u32 v17, 32768, v4
ds_read_b64_tr_b16 a[40:41], v17 offset:0
ds_read_b64_tr_b16 a[42:43], v17 offset:1024
ds_read_b64_tr_b16 a[44:45], v17 offset:8192
ds_read_b64_tr_b16 a[46:47], v17 offset:9216
s_waitcnt vmcnt(28)
s_waitcnt lgkmcnt(0)
s_barrier
s_add_u32 m0, s55, 32768
s_nop 0
global_load_lds_dwordx4 v[10:11], off
v_lshl_add_u64 v[10:11], v[10:11], 0, s[44:45]
s_add_u32 m0, s55, 36864
s_nop 0
global_load_lds_dwordx4 v[10:11], off
v_lshl_add_u64 v[10:11], v[10:11], 0, s[44:45]
s_add_u32 m0, s55, 40960
s_nop 0
global_load_lds_dwordx4 v[10:11], off
v_lshl_add_u64 v[10:11], v[10:11], 0, s[44:45]
s_add_u32 m0, s55, 45056
s_nop 0
global_load_lds_dwordx4 v[10:11], off
v_lshl_add_u64 v[10:11], v[10:11], 0, s[46:47]
v_add_u32 v16, 49152, v3
ds_read_b64_tr_b16 a[48:49], v16 offset:0
ds_read_b64_tr_b16 a[50:51], v16 offset:1024
ds_read_b64_tr_b16 a[52:53], v16 offset:8192
ds_read_b64_tr_b16 a[54:55], v16 offset:9216
v_add_u32 v17, 49152, v4
ds_read_b64_tr_b16 a[56:57], v17 offset:0
ds_read_b64_tr_b16 a[58:59], v17 offset:1024
ds_read_b64_tr_b16 a[60:61], v17 offset:8192
ds_read_b64_tr_b16 a[62:63], v17 offset:9216
s_waitcnt vmcnt(28)
s_waitcnt lgkmcnt(0)
s_barrier
s_add_u32 m0, s55, 49152
s_nop 0
global_load_lds_dwordx4 v[12:13], off
v_lshl_add_u64 v[12:13], v[12:13], 0, s[76:77]
s_add_u32 m0, s55, 53248
s_nop 0
global_load_lds_dwordx4 v[12:13], off
v_lshl_add_u64 v[12:13], v[12:13], 0, s[76:77]
s_add_u32 m0, s55, 57344
s_nop 0
global_load_lds_dwordx4 v[12:13], off
v_lshl_add_u64 v[12:13], v[12:13], 0, s[76:77]
s_add_u32 m0, s55, 61440
s_nop 0
global_load_lds_dwordx4 v[12:13], off
v_lshl_add_u64 v[12:13], v[12:13], 0, s[76:77]
v_add_u32 v16, 65536, v3
ds_read_b64_tr_b16 a[64:65], v16 offset:0
ds_read_b64_tr_b16 a[66:67], v16 offset:1024
ds_read_b64_tr_b16 a[68:69], v16 offset:8192
ds_read_b64_tr_b16 a[70:71], v16 offset:9216
v_add_u32 v17, 65536, v4
ds_read_b64_tr_b16 a[72:73], v17 offset:0
ds_read_b64_tr_b16 a[74:75], v17 offset:1024
ds_read_b64_tr_b16 a[76:77], v17 offset:8192
ds_read_b64_tr_b16 a[78:79], v17 offset:9216
s_waitcnt vmcnt(28)
s_waitcnt lgkmcnt(0)
s_barrier
s_add_u32 m0, s55, 65536
s_nop 0
global_load_lds_dwordx4 v[12:13], off
v_lshl_add_u64 v[12:13], v[12:13], 0, s[76:77]
s_add_u32 m0, s55, 69632
s_nop 0
global_load_lds_dwordx4 v[12:13], off
v_lshl_add_u64 v[12:13], v[12:13], 0, s[76:77]
s_add_u32 m0, s55, 73728
s_nop 0
global_load_lds_dwordx4 v[12:13], off
v_lshl_add_u64 v[12:13], v[12:13], 0, s[76:77]
s_add_u32 m0, s55, 77824
s_nop 0
global_load_lds_dwordx4 v[12:13], off
v_lshl_add_u64 v[12:13], v[12:13], 0, s[76:77]
v_add_u32 v16, 81920, v3
ds_read_b64_tr_b16 a[80:81], v16 offset:0
ds_read_b64_tr_b16 a[82:83], v16 offset:1024
ds_read_b64_tr_b16 a[84:85], v16 offset:8192
ds_read_b64_tr_b16 a[86:87], v16 offset:9216
v_add_u32 v17, 81920, v4
ds_read_b64_tr_b16 a[88:89], v17 offset:0
ds_read_b64_tr_b16 a[90:91], v17 offset:1024
ds_read_b64_tr_b16 a[92:93], v17 offset:8192
ds_read_b64_tr_b16 a[94:95], v17 offset:9216
s_waitcnt vmcnt(28)
s_waitcnt lgkmcnt(0)
s_barrier
s_add_u32 m0, s55, 81920
s_nop 0
global_load_lds_dwordx4 v[12:13], off
v_lshl_add_u64 v[12:13], v[12:13], 0, s[76:77]
s_add_u32 m0, s55, 86016
s_nop 0
global_load_lds_dwordx4 v[12:13], off
v_lshl_add_u64 v[12:13], v[12:13], 0, s[76:77]
s_add_u32 m0, s55, 90112
s_nop 0
global_load_lds_dwordx4 v[12:13], off
v_lshl_add_u64 v[12:13], v[12:13], 0, s[76:77]
s_add_u32 m0, s55, 94208
s_nop 0
global_load_lds_dwordx4 v[12:13], off
v_lshl_add_u64 v[12:13], v[12:13], 0, s[80:81]
v_add_u32 v16, 98304, v3
ds_read_b64_tr_b16 a[96:97], v16 offset:0
ds_read_b64_tr_b16 a[98:99], v16 offset:1024
ds_read_b64_tr_b16 a[100:101], v16 offset:8192
ds_read_b64_tr_b16 a[102:103], v16 offset:9216
v_add_u32 v17, 98304, v4
ds_read_b64_tr_b16 a[104:105], v17 offset:0
ds_read_b64_tr_b16 a[106:107], v17 offset:1024
ds_read_b64_tr_b16 a[108:109], v17 offset:8192
ds_read_b64_tr_b16 a[110:111], v17 offset:9216
s_waitcnt vmcnt(28)
s_waitcnt lgkmcnt(0)
s_barrier
s_add_u32 m0, s55, 98304
s_nop 0
global_load_lds_dwordx4 v[12:13], off
v_lshl_add_u64 v[12:13], v[12:13], 0, s[76:77]
s_add_u32 m0, s55, 102400
s_nop 0
global_load_lds_dwordx4 v[12:13], off
v_lshl_add_u64 v[12:13], v[12:13], 0, s[76:77]
s_add_u32 m0, s55, 106496
s_nop 0
global_load_lds_dwordx4 v[12:13], off
v_lshl_add_u64 v[12:13], v[12:13], 0, s[76:77]
s_add_u32 m0, s55, 110592
s_nop 0
global_load_lds_dwordx4 v[12:13], off
v_lshl_add_u64 v[12:13], v[12:13], 0, s[76:77]
v_add_u32 v16, 114688, v3
ds_read_b64_tr_b16 a[112:113], v16 offset:0
ds_read_b64_tr_b16 a[114:115], v16 offset:1024
ds_read_b64_tr_b16 a[116:117], v16 offset:8192
ds_read_b64_tr_b16 a[118:119], v16 offset:9216
v_add_u32 v17, 114688, v4
ds_read_b64_tr_b16 a[120:121], v17 offset:0
ds_read_b64_tr_b16 a[122:123], v17 offset:1024
ds_read_b64_tr_b16 a[124:125], v17 offset:8192
ds_read_b64_tr_b16 a[126:127], v17 offset:9216
s_waitcnt vmcnt(28)
s_waitcnt lgkmcnt(0)
s_barrier
s_add_u32 m0, s55, 114688
s_nop 0
global_load_lds_dwordx4 v[12:13], off
v_lshl_add_u64 v[12:13], v[12:13], 0, s[76:77]
s_add_u32 m0, s55, 118784
s_nop 0
global_load_lds_dwordx4 v[12:13], off
v_lshl_add_u64 v[12:13], v[12:13], 0, s[76:77]
s_add_u32 m0, s55, 122880
s_nop 0
global_load_lds_dwordx4 v[12:13], off
v_lshl_add_u64 v[12:13], v[12:13], 0, s[76:77]
s_add_u32 m0, s55, 126976
s_nop 0
global_load_lds_dwordx4 v[12:13], off
v_lshl_add_u64 v[12:13], v[12:13], 0, s[76:77]
v_add_u32 v16, 131072, v5
ds_read_b64_tr_b16 a[128:129], v16 offset:0
ds_read_b64_tr_b16 a[130:131], v16 offset:1024
ds_read_b64_tr_b16 a[132:133], v16 offset:8192
ds_read_b64_tr_b16 a[134:135], v16 offset:9216
v_add_u32 v17, 131072, v6
ds_read_b64_tr_b16 a[136:137], v17 offset:0
ds_read_b64_tr_b16 a[138:139], v17 offset:1024
ds_read_b64_tr_b16 a[140:141], v17 offset:8192
ds_read_b64_tr_b16 a[142:143], v17 offset:9216
s_waitcnt vmcnt(28)
s_waitcnt lgkmcnt(0)
s_barrier
s_add_u32 m0, s55, 131072
s_nop 0
global_load_lds_dwordx4 v[12:13], off
v_lshl_add_u64 v[12:13], v[12:13], 0, s[76:77]
s_add_u32 m0, s55, 135168
s_nop 0
global_load_lds_dwordx4 v[12:13], off
v_lshl_add_u64 v[12:13], v[12:13], 0, s[76:77]
s_add_u32 m0, s55, 139264
s_nop 0
global_load_lds_dwordx4 v[12:13], off
v_lshl_add_u64 v[12:13], v[12:13], 0, s[76:77]
s_add_u32 m0, s55, 143360
s_nop 0
global_load_lds_dwordx4 v[12:13], off
v_lshl_add_u64 v[12:13], v[12:13], 0, s[80:81]
v_add_u32 v16, 0, v5
ds_read_b64_tr_b16 a[144:145], v16 offset:0
ds_read_b64_tr_b16 a[146:147], v16 offset:1024
ds_read_b64_tr_b16 a[148:149], v16 offset:8192
ds_read_b64_tr_b16 a[150:151], v16 offset:9216
v_add_u32 v17, 0, v6
ds_read_b64_tr_b16 a[152:153], v17 offset:0
ds_read_b64_tr_b16 a[154:155], v17 offset:1024
ds_read_b64_tr_b16 a[156:157], v17 offset:8192
ds_read_b64_tr_b16 a[158:159], v17 offset:9216
s_waitcnt vmcnt(28)
s_waitcnt lgkmcnt(0)
s_barrier
s_add_u32 m0, s55, 0
s_nop 0
global_load_lds_dwordx4 v[12:13], off
v_lshl_add_u64 v[12:13], v[12:13], 0, s[76:77]
s_add_u32 m0, s55, 4096
s_nop 0
global_load_lds_dwordx4 v[12:13], off
v_lshl_add_u64 v[12:13], v[12:13], 0, s[76:77]
s_add_u32 m0, s55, 8192
s_nop 0
global_load_lds_dwordx4 v[12:13], off
v_lshl_add_u64 v[12:13], v[12:13], 0, s[76:77]
s_add_u32 m0, s55, 12288
s_nop 0
global_load_lds_dwordx4 v[12:13], off
v_lshl_add_u64 v[12:13], v[12:13], 0, s[76:77]
v_add_u32 v16, 16384, v5
ds_read_b64_tr_b16 a[160:161], v16 offset:0
ds_read_b64_tr_b16 a[162:163], v16 offset:1024
ds_read_b64_tr_b16 a[164:165], v16 offset:8192
ds_read_b64_tr_b16 a[166:167], v16 offset:9216
v_add_u32 v17, 16384, v6
ds_read_b64_tr_b16 a[168:169], v17 offset:0
ds_read_b64_tr_b16 a[170:171], v17 offset:1024
ds_read_b64_tr_b16 a[172:173], v17 offset:8192
ds_read_b64_tr_b16 a[174:175], v17 offset:9216
s_waitcnt vmcnt(28)
s_waitcnt lgkmcnt(0)
s_barrier
s_add_u32 m0, s55, 16384
s_nop 0
global_load_lds_dwordx4 v[12:13], off
v_lshl_add_u64 v[12:13], v[12:13], 0, s[76:77]
s_add_u32 m0, s55, 20480
s_nop 0
global_load_lds_dwordx4 v[12:13], off
v_lshl_add_u64 v[12:13], v[12:13], 0, s[76:77]
s_add_u32 m0, s55, 24576
s_nop 0
global_load_lds_dwordx4 v[12:13], off
v_lshl_add_u64 v[12:13], v[12:13], 0, s[76:77]
s_add_u32 m0, s55, 28672
s_nop 0
global_load_lds_dwordx4 v[12:13], off
v_lshl_add_u64 v[12:13], v[12:13], 0, s[76:77]
v_add_u32 v16, 32768, v5
ds_read_b64_tr_b16 a[176:177], v16 offset:0
ds_read_b64_tr_b16 a[178:179], v16 offset:1024
ds_read_b64_tr_b16 a[180:181], v16 offset:8192
ds_read_b64_tr_b16 a[182:183], v16 offset:9216
v_add_u32 v17, 32768, v6
ds_read_b64_tr_b16 a[184:185], v17 offset:0
ds_read_b64_tr_b16 a[186:187], v17 offset:1024
ds_read_b64_tr_b16 a[188:189], v17 offset:8192
ds_read_b64_tr_b16 a[190:191], v17 offset:9216
s_waitcnt vmcnt(28)
s_waitcnt lgkmcnt(0)
s_barrier
s_add_u32 m0, s55, 32768
s_nop 0
global_load_lds_dwordx4 v[12:13], off
v_lshl_add_u64 v[12:13], v[12:13], 0, s[76:77]
s_add_u32 m0, s55, 36864
s_nop 0
global_load_lds_dwordx4 v[12:13], off
v_lshl_add_u64 v[12:13], v[12:13], 0, s[76:77]
s_add_u32 m0, s55, 40960
s_nop 0
global_load_lds_dwordx4 v[12:13], off
v_lshl_add_u64 v[12:13], v[12:13], 0, s[76:77]
s_add_u32 m0, s55, 45056
s_nop 0
global_load_lds_dwordx4 v[12:13], off
v_lshl_add_u64 v[12:13], v[12:13], 0, s[80:81]
v_add_u32 v16, 49152, v3
ds_read_b64_tr_b16 a[192:193], v16 offset:0
ds_read_b64_tr_b16 a[194:195], v16 offset:1024
ds_read_b64_tr_b16 a[196:197], v16 offset:8192
ds_read_b64_tr_b16 a[198:199], v16 offset:9216
v_add_u32 v17, 49152, v4
ds_read_b64_tr_b16 a[200:201], v17 offset:0
ds_read_b64_tr_b16 a[202:203], v17 offset:1024
ds_read_b64_tr_b16 a[204:205], v17 offset:8192
ds_read_b64_tr_b16 a[206:207], v17 offset:9216
s_waitcnt vmcnt(28)
s_waitcnt lgkmcnt(0)
s_barrier
s_add_u32 m0, s55, 49152
s_nop 0
global_load_lds_dwordx4 v[12:13], off
v_lshl_add_u64 v[12:13], v[12:13], 0, s[76:77]
s_add_u32 m0, s55, 53248
s_nop 0
global_load_lds_dwordx4 v[12:13], off
v_lshl_add_u64 v[12:13], v[12:13], 0, s[76:77]
s_add_u32 m0, s55, 57344
s_nop 0
global_load_lds_dwordx4 v[12:13], off
v_lshl_add_u64 v[12:13], v[12:13], 0, s[76:77]
s_add_u32 m0, s55, 61440
s_nop 0
global_load_lds_dwordx4 v[12:13], off
v_lshl_add_u64 v[12:13], v[12:13], 0, s[76:77]
v_add_u32 v16, 65536, v3
ds_read_b64_tr_b16 a[208:209], v16 offset:0
ds_read_b64_tr_b16 a[210:211], v16 offset:1024
ds_read_b64_tr_b16 a[212:213], v16 offset:8192
ds_read_b64_tr_b16 a[214:215], v16 offset:9216
v_add_u32 v17, 65536, v4
ds_read_b64_tr_b16 a[216:217], v17 offset:0
ds_read_b64_tr_b16 a[218:219], v17 offset:1024
ds_read_b64_tr_b16 a[220:221], v17 offset:8192
ds_read_b64_tr_b16 a[222:223], v17 offset:9216
s_waitcnt vmcnt(28)
s_waitcnt lgkmcnt(0)
s_barrier
s_add_u32 m0, s55, 65536
s_nop 0
global_load_lds_dwordx4 v[12:13], off
v_lshl_add_u64 v[12:13], v[12:13], 0, s[76:77]
s_add_u32 m0, s55, 69632
s_nop 0
global_load_lds_dwordx4 v[12:13], off
v_lshl_add_u64 v[12:13], v[12:13], 0, s[76:77]
s_add_u32 m0, s55, 73728
s_nop 0
global_load_lds_dwordx4 v[12:13], off
v_lshl_add_u64 v[12:13], v[12:13], 0, s[76:77]
s_add_u32 m0, s55, 77824
s_nop 0
global_load_lds_dwordx4 v[12:13], off
v_lshl_add_u64 v[12:13], v[12:13], 0, s[76:77]
v_add_u32 v16, 81920, v3
ds_read_b64_tr_b16 a[224:225], v16 offset:0
ds_read_b64_tr_b16 a[226:227], v16 offset:1024
ds_read_b64_tr_b16 a[228:229], v16 offset:8192
ds_read_b64_tr_b16 a[230:231], v16 offset:9216
v_add_u32 v17, 81920, v4
ds_read_b64_tr_b16 a[232:233], v17 offset:0
ds_read_b64_tr_b16 a[234:235], v17 offset:1024
ds_read_b64_tr_b16 a[236:237], v17 offset:8192
ds_read_b64_tr_b16 a[238:239], v17 offset:9216
s_waitcnt vmcnt(28)
s_waitcnt lgkmcnt(0)
s_barrier
s_add_u32 m0, s55, 81920
s_nop 0
global_load_lds_dwordx4 v[12:13], off
v_lshl_add_u64 v[12:13], v[12:13], 0, s[76:77]
s_add_u32 m0, s55, 86016
s_nop 0
global_load_lds_dwordx4 v[12:13], off
v_lshl_add_u64 v[12:13], v[12:13], 0, s[76:77]
s_add_u32 m0, s55, 90112
s_nop 0
global_load_lds_dwordx4 v[12:13], off
v_lshl_add_u64 v[12:13], v[12:13], 0, s[76:77]
s_add_u32 m0, s55, 94208
s_nop 0
global_load_lds_dwordx4 v[12:13], off
v_lshl_add_u64 v[12:13], v[12:13], 0, s[80:81]
v_add_u32 v16, 98304, v3
ds_read_b64_tr_b16 a[240:241], v16 offset:0
ds_read_b64_tr_b16 a[242:243], v16 offset:1024
ds_read_b64_tr_b16 a[244:245], v16 offset:8192
ds_read_b64_tr_b16 a[246:247], v16 offset:9216
v_add_u32 v17, 98304, v4
ds_read_b64_tr_b16 a[248:249], v17 offset:0
ds_read_b64_tr_b16 a[250:251], v17 offset:1024
ds_read_b64_tr_b16 a[252:253], v17 offset:8192
ds_read_b64_tr_b16 a[254:255], v17 offset:9216
s_waitcnt vmcnt(28)
s_waitcnt lgkmcnt(0)
s_barrier
s_add_u32 m0, s55, 98304
s_nop 0
global_load_lds_dwordx4 v[8:9], off
v_lshl_add_u64 v[8:9], v[8:9], 0, s[44:45]
s_add_u32 m0, s55, 102400
s_nop 0
global_load_lds_dwordx4 v[8:9], off
v_lshl_add_u64 v[8:9], v[8:9], 0, s[44:45]
s_add_u32 m0, s55, 106496
s_nop 0
global_load_lds_dwordx4 v[8:9], off
v_lshl_add_u64 v[8:9], v[8:9], 0, s[44:45]
s_add_u32 m0, s55, 110592
s_nop 0
global_load_lds_dwordx4 v[8:9], off
v_lshl_add_u64 v[8:9], v[8:9], 0, s[44:45]
v_add_u32 v16, 114688, v3
ds_read_b64_tr_b16 v[128:129], v16 offset:0
ds_read_b64_tr_b16 v[130:131], v16 offset:1024
ds_read_b64_tr_b16 v[132:133], v16 offset:8192
ds_read_b64_tr_b16 v[134:135], v16 offset:9216
v_add_u32 v17, 114688, v4
ds_read_b64_tr_b16 v[136:137], v17 offset:0
ds_read_b64_tr_b16 v[138:139], v17 offset:1024
ds_read_b64_tr_b16 v[140:141], v17 offset:8192
ds_read_b64_tr_b16 v[142:143], v17 offset:9216
s_waitcnt vmcnt(28)
s_waitcnt lgkmcnt(0)
s_barrier
s_add_u32 m0, s55, 114688
s_nop 0
global_load_lds_dwordx4 v[8:9], off
v_lshl_add_u64 v[8:9], v[8:9], 0, s[44:45]
s_add_u32 m0, s55, 118784
s_nop 0
global_load_lds_dwordx4 v[8:9], off
v_lshl_add_u64 v[8:9], v[8:9], 0, s[44:45]
s_add_u32 m0, s55, 122880
s_nop 0
global_load_lds_dwordx4 v[8:9], off
v_lshl_add_u64 v[8:9], v[8:9], 0, s[44:45]
s_add_u32 m0, s55, 126976
s_nop 0
global_load_lds_dwordx4 v[8:9], off
v_lshl_add_u64 v[8:9], v[8:9], 0, s[44:45]
v_add_u32 v16, 131072, v3
ds_read_b64_tr_b16 v[144:145], v16 offset:0
ds_read_b64_tr_b16 v[146:147], v16 offset:1024
ds_read_b64_tr_b16 v[148:149], v16 offset:8192
ds_read_b64_tr_b16 v[150:151], v16 offset:9216
v_add_u32 v17, 131072, v4
ds_read_b64_tr_b16 v[152:153], v17 offset:0
ds_read_b64_tr_b16 v[154:155], v17 offset:1024
ds_read_b64_tr_b16 v[156:157], v17 offset:8192
ds_read_b64_tr_b16 v[158:159], v17 offset:9216
s_waitcnt vmcnt(28)
s_waitcnt lgkmcnt(0)
s_barrier
s_add_u32 m0, s55, 131072
s_nop 0
global_load_lds_dwordx4 v[10:11], off
v_lshl_add_u64 v[10:11], v[10:11], 0, s[44:45]
s_add_u32 m0, s55, 135168
s_nop 0
global_load_lds_dwordx4 v[10:11], off
v_lshl_add_u64 v[10:11], v[10:11], 0, s[44:45]
s_add_u32 m0, s55, 139264
s_nop 0
global_load_lds_dwordx4 v[10:11], off
v_lshl_add_u64 v[10:11], v[10:11], 0, s[44:45]
s_add_u32 m0, s55, 143360
s_nop 0
global_load_lds_dwordx4 v[10:11], off
v_lshl_add_u64 v[10:11], v[10:11], 0, s[46:47]
v_add_u32 v16, 0, v3
ds_read_b64_tr_b16 v[160:161], v16 offset:0
ds_read_b64_tr_b16 v[162:163], v16 offset:1024
ds_read_b64_tr_b16 v[164:165], v16 offset:8192
ds_read_b64_tr_b16 v[166:167], v16 offset:9216
v_add_u32 v17, 0, v4
ds_read_b64_tr_b16 v[168:169], v17 offset:0
ds_read_b64_tr_b16 v[170:171], v17 offset:1024
ds_read_b64_tr_b16 v[172:173], v17 offset:8192
ds_read_b64_tr_b16 v[174:175], v17 offset:9216
s_waitcnt vmcnt(28)
s_waitcnt lgkmcnt(0)
s_barrier
s_add_u32 m0, s55, 0
s_nop 0
global_load_lds_dwordx4 v[12:13], off
v_lshl_add_u64 v[12:13], v[12:13], 0, s[76:77]
s_add_u32 m0, s55, 4096
s_nop 0
global_load_lds_dwordx4 v[12:13], off
v_lshl_add_u64 v[12:13], v[12:13], 0, s[76:77]
s_add_u32 m0, s55, 8192
s_nop 0
global_load_lds_dwordx4 v[12:13], off
v_lshl_add_u64 v[12:13], v[12:13], 0, s[76:77]
s_add_u32 m0, s55, 12288
s_nop 0
global_load_lds_dwordx4 v[12:13], off
v_lshl_add_u64 v[12:13], v[12:13], 0, s[76:77]
v_add_u32 v16, 16384, v3
ds_read_b64_tr_b16 v[176:177], v16 offset:0
ds_read_b64_tr_b16 v[178:179], v16 offset:1024
ds_read_b64_tr_b16 v[180:181], v16 offset:8192
ds_read_b64_tr_b16 v[182:183], v16 offset:9216
v_add_u32 v17, 16384, v4
ds_read_b64_tr_b16 v[184:185], v17 offset:0
ds_read_b64_tr_b16 v[186:187], v17 offset:1024
ds_read_b64_tr_b16 v[188:189], v17 offset:8192
ds_read_b64_tr_b16 v[190:191], v17 offset:9216
s_waitcnt vmcnt(28)
s_waitcnt lgkmcnt(0)
s_barrier
s_add_u32 m0, s55, 16384
s_nop 0
global_load_lds_dwordx4 v[12:13], off
v_lshl_add_u64 v[12:13], v[12:13], 0, s[76:77]
s_add_u32 m0, s55, 20480
s_nop 0
global_load_lds_dwordx4 v[12:13], off
v_lshl_add_u64 v[12:13], v[12:13], 0, s[76:77]
s_add_u32 m0, s55, 24576
s_nop 0
global_load_lds_dwordx4 v[12:13], off
v_lshl_add_u64 v[12:13], v[12:13], 0, s[76:77]
s_add_u32 m0, s55, 28672
s_nop 0
global_load_lds_dwordx4 v[12:13], off
v_lshl_add_u64 v[12:13], v[12:13], 0, s[76:77]
v_add_u32 v16, 32768, v3
ds_read_b64_tr_b16 v[192:193], v16 offset:0
ds_read_b64_tr_b16 v[194:195], v16 offset:1024
ds_read_b64_tr_b16 v[196:197], v16 offset:8192
ds_read_b64_tr_b16 v[198:199], v16 offset:9216
v_add_u32 v17, 32768, v4
ds_read_b64_tr_b16 v[200:201], v17 offset:0
ds_read_b64_tr_b16 v[202:203], v17 offset:1024
ds_read_b64_tr_b16 v[204:205], v17 offset:8192
ds_read_b64_tr_b16 v[206:207], v17 offset:9216
s_waitcnt vmcnt(28)
s_waitcnt lgkmcnt(0)
s_barrier
s_add_u32 m0, s55, 32768
s_nop 0
global_load_lds_dwordx4 v[12:13], off
v_lshl_add_u64 v[12:13], v[12:13], 0, s[76:77]
s_add_u32 m0, s55, 36864
s_nop 0
global_load_lds_dwordx4 v[12:13], off
v_lshl_add_u64 v[12:13], v[12:13], 0, s[76:77]
s_add_u32 m0, s55, 40960
s_nop 0
global_load_lds_dwordx4 v[12:13], off
v_lshl_add_u64 v[12:13], v[12:13], 0, s[76:77]
s_add_u32 m0, s55, 45056
s_nop 0
global_load_lds_dwordx4 v[12:13], off
v_lshl_add_u64 v[12:13], v[12:13], 0, s[80:81]
v_add_u32 v16, 49152, v3
ds_read_b64_tr_b16 v[208:209], v16 offset:0
ds_read_b64_tr_b16 v[210:211], v16 offset:1024
ds_read_b64_tr_b16 v[212:213], v16 offset:8192
ds_read_b64_tr_b16 v[214:215], v16 offset:9216
v_add_u32 v17, 49152, v4
ds_read_b64_tr_b16 v[216:217], v17 offset:0
ds_read_b64_tr_b16 v[218:219], v17 offset:1024
ds_read_b64_tr_b16 v[220:221], v17 offset:8192
ds_read_b64_tr_b16 v[222:223], v17 offset:9216
s_waitcnt vmcnt(28)
s_waitcnt lgkmcnt(0)
s_barrier
v_add_u32 v16, 65536, v3
ds_read_b64_tr_b16 v[224:225], v16 offset:0
ds_read_b64_tr_b16 v[226:227], v16 offset:1024
ds_read_b64_tr_b16 v[228:229], v16 offset:8192
ds_read_b64_tr_b16 v[230:231], v16 offset:9216
v_add_u32 v17, 65536, v4
ds_read_b64_tr_b16 v[232:233], v17 offset:0
ds_read_b64_tr_b16 v[234:235], v17 offset:1024
ds_read_b64_tr_b16 v[236:237], v17 offset:8192
ds_read_b64_tr_b16 v[238:239], v17 offset:9216
s_waitcnt vmcnt(24)
s_waitcnt lgkmcnt(0)
s_barrier
v_add_u32 v16, 81920, v3
ds_read_b64_tr_b16 v[240:241], v16 offset:0
ds_read_b64_tr_b16 v[242:243], v16 offset:1024
ds_read_b64_tr_b16 v[244:245], v16 offset:8192
ds_read_b64_tr_b16 v[246:247], v16 offset:9216
v_add_u32 v17, 81920, v4
ds_read_b64_tr_b16 v[248:249], v17 offset:0
ds_read_b64_tr_b16 v[250:251], v17 offset:1024
ds_read_b64_tr_b16 v[252:253], v17 offset:8192
ds_read_b64_tr_b16 v[254:255], v17 offset:9216
s_waitcnt vmcnt(20)
s_waitcnt lgkmcnt(0)
s_barrier
v_add_u32 v16, 98304, v3
ds_read_b64_tr_b16 v[32:33], v16 offset:0
ds_read_b64_tr_b16 v[34:35], v16 offset:1024
ds_read_b64_tr_b16 v[36:37], v16 offset:8192
ds_read_b64_tr_b16 v[38:39], v16 offset:9216
v_add_u32 v17, 98304, v4
ds_read_b64_tr_b16 v[40:41], v17 offset:0
ds_read_b64_tr_b16 v[42:43], v17 offset:1024
ds_read_b64_tr_b16 v[44:45], v17 offset:8192
ds_read_b64_tr_b16 v[46:47], v17 offset:9216
s_waitcnt vmcnt(16)
s_waitcnt lgkmcnt(0)
s_barrier
v_add_u32 v16, 98304, v7
ds_write_b128 v16, v[32:35] offset:0
ds_write_b128 v16, v[36:39] offset:1024
ds_write_b128 v16, v[40:43] offset:2048
ds_write_b128 v16, v[44:47] offset:3072
v_add_u32 v16, 114688, v3
ds_read_b64_tr_b16 v[32:33], v16 offset:0
ds_read_b64_tr_b16 v[34:35], v16 offset:1024
ds_read_b64_tr_b16 v[36:37], v16 offset:8192
ds_read_b64_tr_b16 v[38:39], v16 offset:9216
v_add_u32 v17, 114688, v4
ds_read_b64_tr_b16 v[40:41], v17 offset:0
ds_read_b64_tr_b16 v[42:43], v17 offset:1024
ds_read_b64_tr_b16 v[44:45], v17 offset:8192
ds_read_b64_tr_b16 v[46:47], v17 offset:9216
s_waitcnt vmcnt(12)
s_waitcnt lgkmcnt(0)
s_barrier
v_add_u32 v16, 114688, v7
ds_write_b128 v16, v[32:35] offset:0
ds_write_b128 v16, v[36:39] offset:1024
ds_write_b128 v16, v[40:43] offset:2048
ds_write_b128 v16, v[44:47] offset:3072
v_add_u32 v16, 131072, v5
ds_read_b64_tr_b16 v[32:33], v16 offset:0
ds_read_b64_tr_b16 v[34:35], v16 offset:1024
ds_read_b64_tr_b16 v[36:37], v16 offset:8192
ds_read_b64_tr_b16 v[38:39], v16 offset:9216
v_add_u32 v17, 131072, v6
ds_read_b64_tr_b16 v[40:41], v17 offset:0
ds_read_b64_tr_b16 v[42:43], v17 offset:1024
ds_read_b64_tr_b16 v[44:45], v17 offset:8192
ds_read_b64_tr_b16 v[46:47], v17 offset:9216
s_waitcnt vmcnt(8)
s_waitcnt lgkmcnt(0)
s_barrier
v_add_u32 v16, 131072, v7
ds_write_b128 v16, v[32:35] offset:0
ds_write_b128 v16, v[36:39] offset:1024
ds_write_b128 v16, v[40:43] offset:2048
ds_write_b128 v16, v[44:47] offset:3072
v_add_u32 v16, 0, v3
ds_read_b64_tr_b16 v[32:33], v16 offset:0
ds_read_b64_tr_b16 v[34:35], v16 offset:1024
ds_read_b64_tr_b16 v[36:37], v16 offset:8192
ds_read_b64_tr_b16 v[38:39], v16 offset:9216
v_add_u32 v17, 0, v4
ds_read_b64_tr_b16 v[40:41], v17 offset:0
ds_read_b64_tr_b16 v[42:43], v17 offset:1024
ds_read_b64_tr_b16 v[44:45], v17 offset:8192
ds_read_b64_tr_b16 v[46:47], v17 offset:9216
s_waitcnt vmcnt(4)
s_waitcnt lgkmcnt(0)
s_barrier
v_add_u32 v16, 0, v7
ds_write_b128 v16, v[32:35] offset:0
ds_write_b128 v16, v[36:39] offset:1024
ds_write_b128 v16, v[40:43] offset:2048
ds_write_b128 v16, v[44:47] offset:3072
v_add_u32 v16, 16384, v3
ds_read_b64_tr_b16 v[32:33], v16 offset:0
ds_read_b64_tr_b16 v[34:35], v16 offset:1024
ds_read_b64_tr_b16 v[36:37], v16 offset:8192
ds_read_b64_tr_b16 v[38:39], v16 offset:9216
v_add_u32 v17, 16384, v4
ds_read_b64_tr_b16 v[40:41], v17 offset:0
ds_read_b64_tr_b16 v[42:43], v17 offset:1024
ds_read_b64_tr_b16 v[44:45], v17 offset:8192
ds_read_b64_tr_b16 v[46:47], v17 offset:9216
s_waitcnt vmcnt(0)
s_waitcnt lgkmcnt(0)
s_barrier
v_add_u32 v16, 16384, v7
ds_write_b128 v16, v[32:35] offset:0
ds_write_b128 v16, v[36:39] offset:1024
ds_write_b128 v16, v[40:43] offset:2048
ds_write_b128 v16, v[44:47] offset:3072
v_add_u32 v16, 32768, v3
ds_read_b64_tr_b16 v[32:33], v16 offset:0
ds_read_b64_tr_b16 v[34:35], v16 offset:1024
ds_read_b64_tr_b16 v[36:37], v16 offset:8192
ds_read_b64_tr_b16 v[38:39], v16 offset:9216
v_add_u32 v17, 32768, v4
ds_read_b64_tr_b16 v[40:41], v17 offset:0
ds_read_b64_tr_b16 v[42:43], v17 offset:1024
ds_read_b64_tr_b16 v[44:45], v17 offset:8192
ds_read_b64_tr_b16 v[46:47], v17 offset:9216
s_waitcnt lgkmcnt(0)
s_barrier
v_add_u32 v16, 32768, v7
ds_write_b128 v16, v[32:35] offset:0
ds_write_b128 v16, v[36:39] offset:1024
ds_write_b128 v16, v[40:43] offset:2048
ds_write_b128 v16, v[44:47] offset:3072
s_waitcnt lgkmcnt(0)
s_barrier
s_add_u32 s24, s48, 0xa000000
s_addc_u32 s25, s49, 0
s_and_b32 s25, s25, 0xffff
s_mov_b32 s26, 0x4000000
s_mov_b32 s27, 0x20000
s_cmp_eq_u32 s40, 1
s_cbranch_scc1 SV_w1
s_cmp_eq_u32 s40, 2
s_cbranch_scc1 SV_w2
s_cmp_eq_u32 s40, 3
s_cbranch_scc1 SV_w3
SV_w0:
s_add_u32 s42, s36, 0
s_lshl_b32 s37, s42, 16
s_lshr_b32 s43, s37, 8
s_xor_b32 s43, s43, s39
s_and_b32 s43, s43, 8192
s_or_b32 s37, s37, s43
s_add_u32 s57, s37, 0
s_add_u32 s58, s37, 16384
s_add_u32 s42, s36, 0
s_lshl_b32 s38, s42, 20
s_lshr_b32 s43, s38, 8
s_xor_b32 s43, s43, s39
s_and_b32 s43, s43, 8192
s_or_b32 s38, s38, s43
v_mov_b32 v44, 0
v_mov_b32 v45, 0
v_mov_b32 v46, 0
v_mov_b32 v47, 0
v_mov_b32 v48, 0
v_mov_b32 v49, 0
v_mov_b32 v50, 0
v_mov_b32 v51, 0
v_mov_b32 v52, 0
v_mov_b32 v53, 0
v_mov_b32 v54, 0
v_mov_b32 v55, 0
v_mov_b32 v72, 0
v_mov_b32 v73, 0
v_mov_b32 v74, 0
v_mov_b32 v75, 0
v_mov_b32 v76, 0
v_mov_b32 v77, 0
v_mov_b32 v78, 0
v_mov_b32 v79, 0
v_mov_b32 v80, 0
v_mov_b32 v81, 0
v_mov_b32 v82, 0
v_mov_b32 v83, 0
v_mov_b32 v84, 0
v_mov_b32 v85, 0
v_mov_b32 v86, 0
v_mov_b32 v87, 0
v_mov_b32 v88, 0
v_mov_b32 v89, 0
v_mov_b32 v90, 0
v_mov_b32 v91, 0
v_mov_b32 v92, 0
v_mov_b32 v93, 0
v_mov_b32 v94, 0
v_mov_b32 v95, 0
v_mov_b32 v96, 0
v_mov_b32 v97, 0
v_mov_b32 v98, 0
v_mov_b32 v99, 0
v_mov_b32 v100, 0
v_mov_b32 v101, 0
v_mov_b32 v102, 0
v_mov_b32 v103, 0
v_mov_b32 v104, 0
v_mov_b32 v105, 0
v_mov_b32 v106, 0
v_mov_b32 v107, 0
v_mov_b32 v108, 0
v_mov_b32 v109, 0
v_mov_b32 v110, 0
v_mov_b32 v111, 0
ds_read_b128 v[56:59], v117 offset:0
ds_read_b128 v[60:63], v117 offset:1024
ds_read_b128 v[64:67], v117 offset:2048
ds_read_b128 v[68:71], v117 offset:3072
s_waitcnt lgkmcnt(0)
s_waitcnt vmcnt(0)
s_waitcnt lgkmcnt(0)
SV_w0_W:
s_memrealtime s[94:95]
s_waitcnt lgkmcnt(0)
s_sub_u32 s94, s92, s94
s_lshl_b32 s94, s94, 1
s_cmp_gt_i32 s94, 20000
s_cbranch_scc1 SV_w0_WG
s_cmp_gt_i32 s94, 16
s_cbranch_scc1 SV_w0_W
s_cmp_lt_i32 s94, 6
s_cbranch_scc1 SV_w0_WG
s_nop 3
s_cmp_lt_i32 s94, 8
s_cbranch_scc1 SV_w0_WG
s_nop 3
s_cmp_lt_i32 s94, 10
s_cbranch_scc1 SV_w0_WG
s_nop 3
s_cmp_lt_i32 s94, 12
s_cbranch_scc1 SV_w0_WG
s_nop 3
s_cmp_lt_i32 s94, 14
s_cbranch_scc1 SV_w0_WG
s_nop 3
s_cmp_lt_i32 s94, 16
s_cbranch_scc1 SV_w0_WG
s_nop 3
SV_w0_WG:
SV_w0_L:
buffer_load_dwordx4 v[32:35], v116, s[20:23], s57 offen offset:0 sc1
buffer_load_dwordx4 v[36:39], v116, s[20:23], s57 offen offset:1024 sc1
buffer_load_dwordx2 v[40:41], v118, s[20:23], s58 offen offset:0 sc1
s_waitcnt vmcnt(2)
v_max3_u32 v0, v32, v33, v34
s_waitcnt vmcnt(1)
v_max3_u32 v0, v0, v35, v36
v_max3_u32 v0, v0, v37, v38
s_waitcnt vmcnt(0)
v_max3_u32 v0, v0, v39, v40
v_max_u32 v0, v0, v41
v_cmp_eq_u32 vcc, -1, v0
s_cbranch_vccnz SV_w0_F
SV_w0_H:
ds_write_b128 v116, v[32:35] offset:49152
ds_write_b128 v116, v[36:39] offset:50176
ds_write_b64 v118, v[40:41] offset:57344
v_smfmac_f32_16x16x64_bf16 v[44:47], v[32:35], a[0:7], v119
v_smfmac_f32_16x16x64_bf16 v[48:51], v[32:35], a[8:15], v119
v_smfmac_f32_16x16x64_bf16 v[52:55], v[32:35], a[128:135], v119
v_smfmac_f32_16x16x64_bf16 v[44:47], v[36:39], a[16:23], v119
s_waitcnt lgkmcnt(0)
s_barrier
ds_read_b128 v[0:3], v116 offset:57344
ds_read_b128 v[4:7], v116 offset:51200
v_smfmac_f32_16x16x64_bf16 v[48:51], v[36:39], a[24:31], v119
ds_read_b128 v[8:11], v116 offset:52224
v_smfmac_f32_16x16x64_bf16 v[52:55], v[36:39], a[136:143], v119
ds_read_b128 v[12:15], v116 offset:53248
s_waitcnt lgkmcnt(3)
v_smfmac_f32_16x16x64_bf16 v[44:47], v[0:3], v[56:63], v119
ds_read_b128 v[16:19], v116 offset:54272
v_smfmac_f32_16x16x64_bf16 v[48:51], v[0:3], v[64:71], v119
s_waitcnt lgkmcnt(3)
ds_read_b128 v[20:23], v116 offset:55296
v_smfmac_f32_16x16x64_bf16 v[44:47], v[4:7], a[32:39], v119
ds_read_b128 v[24:27], v116 offset:56320
v_smfmac_f32_16x16x64_bf16 v[48:51], v[4:7], a[40:47], v119
s_waitcnt lgkmcnt(4)
ds_read_b128 v[28:31], v116 offset:58368
v_smfmac_f32_16x16x64_bf16 v[44:47], v[8:11], a[48:55], v119
ds_read_b128 v[56:59], v117 offset:16384
v_smfmac_f32_16x16x64_bf16 v[48:51], v[8:11], a[56:63], v119
s_waitcnt lgkmcnt(5)
ds_read_b128 v[60:63], v117 offset:17408
v_smfmac_f32_16x16x64_bf16 v[44:47], v[12:15], a[64:71], v119
ds_read_b128 v[64:67], v117 offset:18432
v_smfmac_f32_16x16x64_bf16 v[48:51], v[12:15], a[72:79], v119
s_waitcnt lgkmcnt(6)
ds_read_b128 v[68:71], v117 offset:19456
v_smfmac_f32_16x16x64_bf16 v[44:47], v[16:19], a[80:87], v119
ds_read_b128 v[32:35], v117 offset:32768
v_smfmac_f32_16x16x64_bf16 v[48:51], v[16:19], a[88:95], v119
s_waitcnt lgkmcnt(7)
ds_read_b128 v[36:39], v117 offset:33792
v_smfmac_f32_16x16x64_bf16 v[44:47], v[20:23], a[96:103], v119
v_smfmac_f32_16x16x64_bf16 v[48:51], v[20:23], a[104:111], v119
s_waitcnt lgkmcnt(7)
v_smfmac_f32_16x16x64_bf16 v[44:47], v[24:27], a[112:119], v119
v_smfmac_f32_16x16x64_bf16 v[48:51], v[24:27], a[120:127], v119
s_waitcnt lgkmcnt(4)
v_smfmac_f32_16x16x64_bf16 v[44:47], v[28:31], v[56:63], v119
s_waitcnt lgkmcnt(2)
ds_read_b128 v[56:59], v117 offset:34816
v_smfmac_f32_16x16x64_bf16 v[48:51], v[28:31], v[64:71], v119
s_waitcnt lgkmcnt(1)
ds_read_b128 v[60:63], v117 offset:35840
v_smfmac_f32_16x16x64_bf16 v[52:55], v[0:3], v[32:39], v119
ds_read_b128 v[64:67], v116 offset:0
v_smfmac_f32_16x16x64_bf16 v[52:55], v[4:7], a[144:151], v119
ds_read_b128 v[68:71], v116 offset:1024
v_add_f32 v44, v44, v45
v_add_f32 v46, v46, v47
v_smfmac_f32_16x16x64_bf16 v[52:55], v[8:11], a[152:159], v119
ds_read_b128 v[32:35], v116 offset:16384
v_cvt_pk_bf16_f32 v44, v44, v46
v_smfmac_f32_16x16x64_bf16 v[52:55], v[12:15], a[160:167], v119
ds_write_b16 v120, v44 offset:59392
ds_read_b128 v[36:39], v116 offset:17408
ds_write_b16_d16_hi v120, v44 offset:59396
v_add_f32 v48, v48, v49
v_add_f32 v50, v50, v51
v_smfmac_f32_16x16x64_bf16 v[52:55], v[16:19], a[168:175], v119
ds_read_b128 v[12:15], v116 offset:32768
v_cvt_pk_bf16_f32 v48, v48, v50
v_smfmac_f32_16x16x64_bf16 v[52:55], v[20:23], a[176:183], v119
ds_write_b16 v120, v48 offset:59648
ds_read_b128 v[16:19], v116 offset:33792
ds_write_b16_d16_hi v120, v48 offset:59652
v_smfmac_f32_16x16x64_bf16 v[52:55], v[24:27], a[184:191], v119
s_waitcnt lgkmcnt(10)
s_waitcnt lgkmcnt(0)
s_barrier
ds_read_b64_tr_b16 v[0:1], v122 offset:59392
v_smfmac_f32_16x16x64_bf16 v[52:55], v[28:31], v[56:63], v119
ds_read_b64_tr_b16 v[2:3], v122 offset:59520
ds_read_b64_tr_b16 v[4:5], v122 offset:60416
ds_read_b64_tr_b16 v[6:7], v122 offset:60544
s_nop 4
v_add_f32 v52, v52, v53
v_add_f32 v54, v54, v55
v_cvt_pk_bf16_f32 v52, v52, v54
ds_write_b16 v120, v52 offset:61440
ds_write_b16_d16_hi v120, v52 offset:61444
s_waitcnt lgkmcnt(4)
v_smfmac_f32_16x16x64_bf16 v[72:75], v[0:3], a[192:199], v119
s_waitcnt lgkmcnt(2)
v_smfmac_f32_16x16x64_bf16 v[72:75], v[4:7], a[208:215], v119
v_smfmac_f32_16x16x64_bf16 v[76:79], v[0:3], a[200:207], v119
s_waitcnt lgkmcnt(0)
s_barrier
ds_read_b64_tr_b16 v[8:9], v122 offset:61440
ds_read_b64_tr_b16 v[10:11], v122 offset:61568
v_smfmac_f32_16x16x64_bf16 v[76:79], v[4:7], a[216:223], v119
v_smfmac_f32_16x16x64_bf16 v[80:83], v[0:3], a[240:247], v119
ds_read_b128 v[20:23], v116 offset:2048
v_smfmac_f32_16x16x64_bf16 v[80:83], v[4:7], v[128:135], v119
ds_read_b128 v[24:27], v116 offset:3072
v_smfmac_f32_16x16x64_bf16 v[84:87], v[0:3], a[248:255], v119
ds_read_b128 v[56:59], v116 offset:18432
v_smfmac_f32_16x16x64_bf16 v[84:87], v[4:7], v[136:143], v119
s_waitcnt lgkmcnt(3)
ds_read_b128 v[60:63], v116 offset:19456
v_smfmac_f32_16x16x64_bf16 v[72:75], v[8:11], a[224:231], v119
ds_read_b128 v[44:47], v116 offset:34816
v_smfmac_f32_16x16x64_bf16 v[76:79], v[8:11], a[232:239], v119
ds_read_b128 v[48:51], v116 offset:35840
v_smfmac_f32_16x16x64_bf16 v[80:83], v[8:11], v[144:151], v119
v_smfmac_f32_16x16x64_bf16 v[84:87], v[8:11], v[152:159], v119
v_smfmac_f32_16x16x64_bf16 v[88:91], v[0:3], v[160:167], v119
s_nop 1
v_add_f32 v72, v72, v73
v_add_f32 v74, v74, v75
v_smfmac_f32_16x16x64_bf16 v[88:91], v[4:7], v[176:183], v119
v_cvt_pk_bf16_f32 v112, v72, v74
v_smfmac_f32_16x16x64_bf16 v[88:91], v[8:11], v[192:199], v119
v_add_f32 v76, v76, v77
v_add_f32 v78, v78, v79
v_smfmac_f32_16x16x64_bf16 v[92:95], v[0:3], v[168:175], v119
v_cvt_pk_bf16_f32 v113, v76, v78
v_smfmac_f32_16x16x64_bf16 v[92:95], v[4:7], v[184:191], v119
v_add_f32 v80, v80, v81
v_add_f32 v82, v82, v83
v_smfmac_f32_16x16x64_bf16 v[92:95], v[8:11], v[200:207], v119
v_cvt_pk_bf16_f32 v114, v80, v82
v_smfmac_f32_16x16x64_bf16 v[96:99], v[0:3], v[208:215], v119
v_add_f32 v84, v84, v85
v_add_f32 v86, v86, v87
v_smfmac_f32_16x16x64_bf16 v[96:99], v[4:7], v[224:231], v119
v_cvt_pk_bf16_f32 v115, v84, v86
v_smfmac_f32_16x16x64_bf16 v[96:99], v[8:11], v[240:247], v119
v_permlane32_swap_b32 v112, v114
v_permlane32_swap_b32 v113, v115
v_smfmac_f32_16x16x64_bf16 v[100:103], v[0:3], v[216:223], v119
s_nop 0
v_permlane16_swap_b32 v112, v113
v_smfmac_f32_16x16x64_bf16 v[100:103], v[4:7], v[232:239], v119
v_permlane16_swap_b32 v114, v115
v_smfmac_f32_16x16x64_bf16 v[100:103], v[8:11], v[248:255], v119
s_add_u32 s42, s38, 0
buffer_store_dwordx4 v[112:115], v121, s[24:27], s42 offen offset:0 sc1
v_add_f32 v88, v88, v89
v_smfmac_f32_16x16x64_bf16 v[104:107], v[0:3], v[64:71], v119
v_add_f32 v90, v90, v91
v_cvt_pk_bf16_f32 v28, v88, v90
v_smfmac_f32_16x16x64_bf16 v[104:107], v[4:7], v[32:39], v119
v_add_f32 v92, v92, v93
v_smfmac_f32_16x16x64_bf16 v[104:107], v[8:11], v[12:19], v119
s_waitcnt lgkmcnt(4)
v_add_f32 v94, v94, v95
v_cvt_pk_bf16_f32 v29, v92, v94
v_smfmac_f32_16x16x64_bf16 v[108:111], v[0:3], v[20:27], v119
s_waitcnt lgkmcnt(2)
v_add_f32 v96, v96, v97
v_smfmac_f32_16x16x64_bf16 v[108:111], v[4:7], v[56:63], v119
s_waitcnt lgkmcnt(0)
v_add_f32 v98, v98, v99
v_cvt_pk_bf16_f32 v30, v96, v98
v_smfmac_f32_16x16x64_bf16 v[108:111], v[8:11], v[44:51], v119
v_add_f32 v100, v100, v101
v_add_f32 v102, v102, v103
v_cvt_pk_bf16_f32 v31, v100, v102
v_permlane32_swap_b32 v28, v30
s_nop 0
v_permlane32_swap_b32 v29, v31
s_nop 1
v_permlane16_swap_b32 v28, v29
v_permlane16_swap_b32 v30, v31
s_add_u32 s42, s38, 262144
buffer_store_dwordx4 v[28:31], v121, s[24:27], s42 offen offset:0 sc1
v_add_f32 v104, v104, v105
v_add_f32 v106, v106, v107
v_cvt_pk_bf16_f32 v112, v104, v106
v_add_f32 v108, v108, v109
v_add_f32 v110, v110, v111
v_cvt_pk_bf16_f32 v113, v108, v110
v_permlane32_swap_b32 v112, v114
s_nop 0
v_permlane32_swap_b32 v113, v115
s_nop 1
v_permlane16_swap_b32 v112, v113
v_permlane16_swap_b32 v114, v115
s_add_u32 s42, s38, 524288
s_mov_b32 exec_hi, 0
buffer_store_dwordx4 v[112:115], v121, s[24:27], s42 offen offset:0 sc1
s_mov_b32 exec_hi, -1
s_cmp_gt_u32 s36, 61
s_cbranch_scc1 SV_w0_NA
s_add_u32 s42, s36, 2
s_lshl_b32 s38, s42, 20
s_lshr_b32 s43, s38, 8
s_xor_b32 s43, s43, s39
s_and_b32 s43, s43, 8192
s_or_b32 s38, s38, s43
v_mov_b32 v32, -1
v_mov_b32 v33, -1
v_mov_b32 v34, -1
v_mov_b32 v35, -1
s_add_u32 s42, s38, 0
buffer_store_dwordx4 v[32:35], v121, s[24:27], s42 offen offset:0 sc1
s_add_u32 s42, s38, 262144
buffer_store_dwordx4 v[32:35], v121, s[24:27], s42 offen offset:0 sc1
s_add_u32 s42, s38, 524288
s_mov_b32 exec_hi, 0
buffer_store_dwordx4 v[32:35], v121, s[24:27], s42 offen offset:0 sc1
s_mov_b64 exec, -1
SV_w0_NA:
s_cmp_eq_u32 s36, 0
s_cbranch_scc1 SV_w0_Z
s_cmp_eq_u32 s36, 63
s_cbranch_scc1 SV_END
v_mov_b32 v1, 0
s_add_u32 s42, s37, 16384
SV_w0_S:
buffer_load_dword v0, v1, s[20:23], s42 offen offset:3076 sc1
s_waitcnt vmcnt(0)
v_readfirstlane_b32 s92, v0
s_nop 0
s_cmp_lg_u32 s92, -1
s_cbranch_scc1 SV_w0_SK
s_sleep 2
s_sub_u32 s41, s41, 1
s_cbranch_scc0 SV_w0_S
s_mov_b32 s41, 0
SV_w0_SK:
SV_w0_Z:
s_add_u32 s36, s36, 1
s_branch SV_w0
SV_w0_F:
s_sub_u32 s41, s41, 1
s_cbranch_scc1 SV_w0_G
s_branch SV_w0_L
SV_w0_G:
s_mov_b32 s41, 0
s_branch SV_w0_H
SV_w1:
s_add_u32 s42, s36, 0
s_lshl_b32 s37, s42, 16
s_lshr_b32 s43, s37, 8
s_xor_b32 s43, s43, s39
s_and_b32 s43, s43, 8192
s_or_b32 s37, s37, s43
s_add_u32 s57, s37, 0
s_add_u32 s58, s37, 16384
s_add_u32 s42, s36, 0
s_lshl_b32 s38, s42, 20
s_lshr_b32 s43, s38, 8
s_xor_b32 s43, s43, s39
s_and_b32 s43, s43, 8192
s_or_b32 s38, s38, s43
v_mov_b32 v44, 0
v_mov_b32 v45, 0
v_mov_b32 v46, 0
v_mov_b32 v47, 0
v_mov_b32 v48, 0
v_mov_b32 v49, 0
v_mov_b32 v50, 0
v_mov_b32 v51, 0
v_mov_b32 v52, 0
v_mov_b32 v53, 0
v_mov_b32 v54, 0
v_mov_b32 v55, 0
v_mov_b32 v72, 0
v_mov_b32 v73, 0
v_mov_b32 v74, 0
v_mov_b32 v75, 0
v_mov_b32 v76, 0
v_mov_b32 v77, 0
v_mov_b32 v78, 0
v_mov_b32 v79, 0
v_mov_b32 v80, 0
v_mov_b32 v81, 0
v_mov_b32 v82, 0
v_mov_b32 v83, 0
v_mov_b32 v84, 0
v_mov_b32 v85, 0
v_mov_b32 v86, 0
v_mov_b32 v87, 0
v_mov_b32 v88, 0
v_mov_b32 v89, 0
v_mov_b32 v90, 0
v_mov_b32 v91, 0
v_mov_b32 v92, 0
v_mov_b32 v93, 0
v_mov_b32 v94, 0
v_mov_b32 v95, 0
v_mov_b32 v96, 0
v_mov_b32 v97, 0
v_mov_b32 v98, 0
v_mov_b32 v99, 0
v_mov_b32 v100, 0
v_mov_b32 v101, 0
v_mov_b32 v102, 0
v_mov_b32 v103, 0
v_mov_b32 v104, 0
v_mov_b32 v105, 0
v_mov_b32 v106, 0
v_mov_b32 v107, 0
v_mov_b32 v108, 0
v_mov_b32 v109, 0
v_mov_b32 v110, 0
v_mov_b32 v111, 0
ds_read_b128 v[56:59], v117 offset:4096
ds_read_b128 v[60:63], v117 offset:5120
ds_read_b128 v[64:67], v117 offset:6144
ds_read_b128 v[68:71], v117 offset:7168
s_waitcnt lgkmcnt(0)
s_waitcnt vmcnt(0)
s_waitcnt lgkmcnt(0)
SV_w1_W:
s_memrealtime s[94:95]
s_waitcnt lgkmcnt(0)
s_sub_u32 s94, s92, s94
s_lshl_b32 s94, s94, 1
s_cmp_gt_i32 s94, 20000
s_cbranch_scc1 SV_w1_WG
s_cmp_gt_i32 s94, 16
s_cbranch_scc1 SV_w1_W
s_cmp_lt_i32 s94, 6
s_cbranch_scc1 SV_w1_WG
s_nop 3
s_cmp_lt_i32 s94, 8
s_cbranch_scc1 SV_w1_WG
s_nop 3
s_cmp_lt_i32 s94, 10
s_cbranch_scc1 SV_w1_WG
s_nop 3
s_cmp_lt_i32 s94, 12
s_cbranch_scc1 SV_w1_WG
s_nop 3
s_cmp_lt_i32 s94, 14
s_cbranch_scc1 SV_w1_WG
s_nop 3
s_cmp_lt_i32 s94, 16
s_cbranch_scc1 SV_w1_WG
s_nop 3
SV_w1_WG:
SV_w1_L:
buffer_load_dwordx4 v[32:35], v116, s[20:23], s57 offen offset:2048 sc1
buffer_load_dwordx4 v[36:39], v116, s[20:23], s57 offen offset:3072 sc1
buffer_load_dwordx2 v[40:41], v118, s[20:23], s58 offen offset:512 sc1
s_waitcnt vmcnt(2)
v_max3_u32 v0, v32, v33, v34
s_waitcnt vmcnt(1)
v_max3_u32 v0, v0, v35, v36
v_max3_u32 v0, v0, v37, v38
s_waitcnt vmcnt(0)
v_max3_u32 v0, v0, v39, v40
v_max_u32 v0, v0, v41
v_cmp_eq_u32 vcc, -1, v0
s_cbranch_vccnz SV_w1_F
SV_w1_H:
ds_write_b128 v116, v[32:35] offset:51200
ds_write_b128 v116, v[36:39] offset:52224
ds_write_b64 v118, v[40:41] offset:57856
v_smfmac_f32_16x16x64_bf16 v[44:47], v[32:35], a[32:39], v119
v_smfmac_f32_16x16x64_bf16 v[48:51], v[32:35], a[40:47], v119
v_smfmac_f32_16x16x64_bf16 v[52:55], v[32:35], a[144:151], v119
v_smfmac_f32_16x16x64_bf16 v[44:47], v[36:39], a[48:55], v119
s_waitcnt lgkmcnt(0)
s_barrier
ds_read_b128 v[0:3], v116 offset:57344
ds_read_b128 v[4:7], v116 offset:53248
v_smfmac_f32_16x16x64_bf16 v[48:51], v[36:39], a[56:63], v119
ds_read_b128 v[8:11], v116 offset:54272
v_smfmac_f32_16x16x64_bf16 v[52:55], v[36:39], a[152:159], v119
ds_read_b128 v[12:15], v116 offset:55296
s_waitcnt lgkmcnt(3)
v_smfmac_f32_16x16x64_bf16 v[44:47], v[0:3], v[56:63], v119
ds_read_b128 v[16:19], v116 offset:56320
v_smfmac_f32_16x16x64_bf16 v[48:51], v[0:3], v[64:71], v119
s_waitcnt lgkmcnt(3)
ds_read_b128 v[20:23], v116 offset:49152
v_smfmac_f32_16x16x64_bf16 v[44:47], v[4:7], a[64:71], v119
ds_read_b128 v[24:27], v116 offset:50176
v_smfmac_f32_16x16x64_bf16 v[48:51], v[4:7], a[72:79], v119
s_waitcnt lgkmcnt(4)
ds_read_b128 v[28:31], v116 offset:58368
v_smfmac_f32_16x16x64_bf16 v[44:47], v[8:11], a[80:87], v119
ds_read_b128 v[56:59], v117 offset:20480
v_smfmac_f32_16x16x64_bf16 v[48:51], v[8:11], a[88:95], v119
s_waitcnt lgkmcnt(5)
ds_read_b128 v[60:63], v117 offset:21504
v_smfmac_f32_16x16x64_bf16 v[44:47], v[12:15], a[96:103], v119
ds_read_b128 v[64:67], v117 offset:22528
v_smfmac_f32_16x16x64_bf16 v[48:51], v[12:15], a[104:111], v119
s_waitcnt lgkmcnt(6)
ds_read_b128 v[68:71], v117 offset:23552
v_smfmac_f32_16x16x64_bf16 v[44:47], v[16:19], a[112:119], v119
ds_read_b128 v[32:35], v117 offset:36864
v_smfmac_f32_16x16x64_bf16 v[48:51], v[16:19], a[120:127], v119
s_waitcnt lgkmcnt(7)
ds_read_b128 v[36:39], v117 offset:37888
v_smfmac_f32_16x16x64_bf16 v[44:47], v[20:23], a[0:7], v119
v_smfmac_f32_16x16x64_bf16 v[48:51], v[20:23], a[8:15], v119
s_waitcnt lgkmcnt(7)
v_smfmac_f32_16x16x64_bf16 v[44:47], v[24:27], a[16:23], v119
v_smfmac_f32_16x16x64_bf16 v[48:51], v[24:27], a[24:31], v119
s_waitcnt lgkmcnt(4)
v_smfmac_f32_16x16x64_bf16 v[44:47], v[28:31], v[56:63], v119
s_waitcnt lgkmcnt(2)
ds_read_b128 v[56:59], v117 offset:38912
v_smfmac_f32_16x16x64_bf16 v[48:51], v[28:31], v[64:71], v119
s_waitcnt lgkmcnt(1)
ds_read_b128 v[60:63], v117 offset:39936
v_smfmac_f32_16x16x64_bf16 v[52:55], v[0:3], v[32:39], v119
ds_read_b128 v[64:67], v116 offset:4096
v_smfmac_f32_16x16x64_bf16 v[52:55], v[4:7], a[160:167], v119
ds_read_b128 v[68:71], v116 offset:5120
v_add_f32 v44, v44, v45
v_add_f32 v46, v46, v47
v_smfmac_f32_16x16x64_bf16 v[52:55], v[8:11], a[168:175], v119
ds_read_b128 v[32:35], v116 offset:20480
v_cvt_pk_bf16_f32 v44, v44, v46
v_smfmac_f32_16x16x64_bf16 v[52:55], v[12:15], a[176:183], v119
ds_write_b16 v120, v44 offset:59904
ds_read_b128 v[36:39], v116 offset:21504
ds_write_b16_d16_hi v120, v44 offset:59908
v_add_f32 v48, v48, v49
v_add_f32 v50, v50, v51
v_smfmac_f32_16x16x64_bf16 v[52:55], v[16:19], a[184:191], v119
ds_read_b128 v[12:15], v116 offset:36864
v_cvt_pk_bf16_f32 v48, v48, v50
v_smfmac_f32_16x16x64_bf16 v[52:55], v[20:23], a[128:135], v119
ds_write_b16 v120, v48 offset:60160
ds_read_b128 v[16:19], v116 offset:37888
ds_write_b16_d16_hi v120, v48 offset:60164
v_smfmac_f32_16x16x64_bf16 v[52:55], v[24:27], a[136:143], v119
s_waitcnt lgkmcnt(10)
s_waitcnt lgkmcnt(0)
s_barrier
ds_read_b64_tr_b16 v[0:1], v122 offset:59392
v_smfmac_f32_16x16x64_bf16 v[52:55], v[28:31], v[56:63], v119
ds_read_b64_tr_b16 v[2:3], v122 offset:59520
ds_read_b64_tr_b16 v[4:5], v122 offset:60416
ds_read_b64_tr_b16 v[6:7], v122 offset:60544
s_nop 4
v_add_f32 v52, v52, v53
v_add_f32 v54, v54, v55
v_cvt_pk_bf16_f32 v52, v52, v54
ds_write_b16 v120, v52 offset:61696
ds_write_b16_d16_hi v120, v52 offset:61700
s_waitcnt lgkmcnt(4)
v_smfmac_f32_16x16x64_bf16 v[72:75], v[0:3], a[192:199], v119
s_waitcnt lgkmcnt(2)
v_smfmac_f32_16x16x64_bf16 v[72:75], v[4:7], a[208:215], v119
v_smfmac_f32_16x16x64_bf16 v[76:79], v[0:3], a[200:207], v119
s_waitcnt lgkmcnt(0)
s_barrier
ds_read_b64_tr_b16 v[8:9], v122 offset:61440
ds_read_b64_tr_b16 v[10:11], v122 offset:61568
v_smfmac_f32_16x16x64_bf16 v[76:79], v[4:7], a[216:223], v119
v_smfmac_f32_16x16x64_bf16 v[80:83], v[0:3], a[240:247], v119
ds_read_b128 v[20:23], v116 offset:6144
v_smfmac_f32_16x16x64_bf16 v[80:83], v[4:7], v[128:135], v119
ds_read_b128 v[24:27], v116 offset:7168
v_smfmac_f32_16x16x64_bf16 v[84:87], v[0:3], a[248:255], v119
ds_read_b128 v[56:59], v116 offset:22528
v_smfmac_f32_16x16x64_bf16 v[84:87], v[4:7], v[136:143], v119
s_waitcnt lgkmcnt(3)
ds_read_b128 v[60:63], v116 offset:23552
v_smfmac_f32_16x16x64_bf16 v[72:75], v[8:11], a[224:231], v119
ds_read_b128 v[44:47], v116 offset:38912
v_smfmac_f32_16x16x64_bf16 v[76:79], v[8:11], a[232:239], v119
ds_read_b128 v[48:51], v116 offset:39936
v_smfmac_f32_16x16x64_bf16 v[80:83], v[8:11], v[144:151], v119
v_smfmac_f32_16x16x64_bf16 v[84:87], v[8:11], v[152:159], v119
v_smfmac_f32_16x16x64_bf16 v[88:91], v[0:3], v[160:167], v119
s_nop 1
v_add_f32 v72, v72, v73
v_add_f32 v74, v74, v75
v_smfmac_f32_16x16x64_bf16 v[88:91], v[4:7], v[176:183], v119
v_cvt_pk_bf16_f32 v112, v72, v74
v_smfmac_f32_16x16x64_bf16 v[88:91], v[8:11], v[192:199], v119
v_add_f32 v76, v76, v77
v_add_f32 v78, v78, v79
v_smfmac_f32_16x16x64_bf16 v[92:95], v[0:3], v[168:175], v119
v_cvt_pk_bf16_f32 v113, v76, v78
v_smfmac_f32_16x16x64_bf16 v[92:95], v[4:7], v[184:191], v119
v_add_f32 v80, v80, v81
v_add_f32 v82, v82, v83
v_smfmac_f32_16x16x64_bf16 v[92:95], v[8:11], v[200:207], v119
v_cvt_pk_bf16_f32 v114, v80, v82
v_smfmac_f32_16x16x64_bf16 v[96:99], v[0:3], v[208:215], v119
v_add_f32 v84, v84, v85
v_add_f32 v86, v86, v87
v_smfmac_f32_16x16x64_bf16 v[96:99], v[4:7], v[224:231], v119
v_cvt_pk_bf16_f32 v115, v84, v86
v_smfmac_f32_16x16x64_bf16 v[96:99], v[8:11], v[240:247], v119
v_permlane32_swap_b32 v112, v114
v_permlane32_swap_b32 v113, v115
v_smfmac_f32_16x16x64_bf16 v[100:103], v[0:3], v[216:223], v119
s_nop 0
v_permlane16_swap_b32 v112, v113
v_smfmac_f32_16x16x64_bf16 v[100:103], v[4:7], v[232:239], v119
v_permlane16_swap_b32 v114, v115
v_smfmac_f32_16x16x64_bf16 v[100:103], v[8:11], v[248:255], v119
s_add_u32 s42, s38, 32768
buffer_store_dwordx4 v[112:115], v121, s[24:27], s42 offen offset:0 sc1
v_add_f32 v88, v88, v89
v_smfmac_f32_16x16x64_bf16 v[104:107], v[0:3], v[64:71], v119
v_add_f32 v90, v90, v91
v_cvt_pk_bf16_f32 v28, v88, v90
v_smfmac_f32_16x16x64_bf16 v[104:107], v[4:7], v[32:39], v119
v_add_f32 v92, v92, v93
v_smfmac_f32_16x16x64_bf16 v[104:107], v[8:11], v[12:19], v119
s_waitcnt lgkmcnt(4)
v_add_f32 v94, v94, v95
v_cvt_pk_bf16_f32 v29, v92, v94
v_smfmac_f32_16x16x64_bf16 v[108:111], v[0:3], v[20:27], v119
s_waitcnt lgkmcnt(2)
v_add_f32 v96, v96, v97
v_smfmac_f32_16x16x64_bf16 v[108:111], v[4:7], v[56:63], v119
s_waitcnt lgkmcnt(0)
v_add_f32 v98, v98, v99
v_cvt_pk_bf16_f32 v30, v96, v98
v_smfmac_f32_16x16x64_bf16 v[108:111], v[8:11], v[44:51], v119
v_add_f32 v100, v100, v101
v_add_f32 v102, v102, v103
v_cvt_pk_bf16_f32 v31, v100, v102
v_permlane32_swap_b32 v28, v30
s_nop 0
v_permlane32_swap_b32 v29, v31
s_nop 1
v_permlane16_swap_b32 v28, v29
v_permlane16_swap_b32 v30, v31
s_add_u32 s42, s38, 294912
buffer_store_dwordx4 v[28:31], v121, s[24:27], s42 offen offset:0 sc1
v_add_f32 v104, v104, v105
v_add_f32 v106, v106, v107
v_cvt_pk_bf16_f32 v112, v104, v106
v_add_f32 v108, v108, v109
v_add_f32 v110, v110, v111
v_cvt_pk_bf16_f32 v113, v108, v110
v_permlane32_swap_b32 v112, v114
s_nop 0
v_permlane32_swap_b32 v113, v115
s_nop 1
v_permlane16_swap_b32 v112, v113
v_permlane16_swap_b32 v114, v115
s_add_u32 s42, s38, 557056
s_mov_b32 exec_hi, 0
buffer_store_dwordx4 v[112:115], v121, s[24:27], s42 offen offset:0 sc1
s_mov_b32 exec_hi, -1
s_cmp_gt_u32 s36, 61
s_cbranch_scc1 SV_w1_NA
s_add_u32 s42, s36, 2
s_lshl_b32 s38, s42, 20
s_lshr_b32 s43, s38, 8
s_xor_b32 s43, s43, s39
s_and_b32 s43, s43, 8192
s_or_b32 s38, s38, s43
v_mov_b32 v32, -1
v_mov_b32 v33, -1
v_mov_b32 v34, -1
v_mov_b32 v35, -1
s_add_u32 s42, s38, 32768
buffer_store_dwordx4 v[32:35], v121, s[24:27], s42 offen offset:0 sc1
s_add_u32 s42, s38, 294912
buffer_store_dwordx4 v[32:35], v121, s[24:27], s42 offen offset:0 sc1
s_add_u32 s42, s38, 557056
s_mov_b32 exec_hi, 0
buffer_store_dwordx4 v[32:35], v121, s[24:27], s42 offen offset:0 sc1
s_mov_b64 exec, -1
SV_w1_NA:
s_cmp_eq_u32 s36, 0
s_cbranch_scc1 SV_w1_Z
s_cmp_eq_u32 s36, 63
s_cbranch_scc1 SV_END
v_mov_b32 v1, 0
s_add_u32 s42, s37, 16384
SV_w1_S:
buffer_load_dword v0, v1, s[20:23], s42 offen offset:3076 sc1
s_waitcnt vmcnt(0)
v_readfirstlane_b32 s92, v0
s_nop 0
s_cmp_lg_u32 s92, -1
s_cbranch_scc1 SV_w1_SK
s_sleep 2
s_sub_u32 s41, s41, 1
s_cbranch_scc0 SV_w1_S
s_mov_b32 s41, 0
SV_w1_SK:
SV_w1_Z:
s_add_u32 s36, s36, 1
s_branch SV_w1
SV_w1_F:
s_sub_u32 s41, s41, 1
s_cbranch_scc1 SV_w1_G
s_branch SV_w1_L
SV_w1_G:
s_mov_b32 s41, 0
s_branch SV_w1_H
SV_w2:
s_add_u32 s42, s36, 0
s_lshl_b32 s37, s42, 16
s_lshr_b32 s43, s37, 8
s_xor_b32 s43, s43, s39
s_and_b32 s43, s43, 8192
s_or_b32 s37, s37, s43
s_add_u32 s57, s37, 4096
s_add_u32 s58, s37, 16384
s_add_u32 s42, s36, 0
s_lshl_b32 s38, s42, 20
s_lshr_b32 s43, s38, 8
s_xor_b32 s43, s43, s39
s_and_b32 s43, s43, 8192
s_or_b32 s38, s38, s43
v_mov_b32 v44, 0
v_mov_b32 v45, 0
v_mov_b32 v46, 0
v_mov_b32 v47, 0
v_mov_b32 v48, 0
v_mov_b32 v49, 0
v_mov_b32 v50, 0
v_mov_b32 v51, 0
v_mov_b32 v52, 0
v_mov_b32 v53, 0
v_mov_b32 v54, 0
v_mov_b32 v55, 0
v_mov_b32 v72, 0
v_mov_b32 v73, 0
v_mov_b32 v74, 0
v_mov_b32 v75, 0
v_mov_b32 v76, 0
v_mov_b32 v77, 0
v_mov_b32 v78, 0
v_mov_b32 v79, 0
v_mov_b32 v80, 0
v_mov_b32 v81, 0
v_mov_b32 v82, 0
v_mov_b32 v83, 0
v_mov_b32 v84, 0
v_mov_b32 v85, 0
v_mov_b32 v86, 0
v_mov_b32 v87, 0
v_mov_b32 v88, 0
v_mov_b32 v89, 0
v_mov_b32 v90, 0
v_mov_b32 v91, 0
v_mov_b32 v92, 0
v_mov_b32 v93, 0
v_mov_b32 v94, 0
v_mov_b32 v95, 0
v_mov_b32 v96, 0
v_mov_b32 v97, 0
v_mov_b32 v98, 0
v_mov_b32 v99, 0
v_mov_b32 v100, 0
v_mov_b32 v101, 0
v_mov_b32 v102, 0
v_mov_b32 v103, 0
v_mov_b32 v104, 0
v_mov_b32 v105, 0
v_mov_b32 v106, 0
v_mov_b32 v107, 0
v_mov_b32 v108, 0
v_mov_b32 v109, 0
v_mov_b32 v110, 0
v_mov_b32 v111, 0
ds_read_b128 v[56:59], v117 offset:8192
ds_read_b128 v[60:63], v117 offset:9216
ds_read_b128 v[64:67], v117 offset:10240
ds_read_b128 v[68:71], v117 offset:11264
s_waitcnt lgkmcnt(0)
s_waitcnt vmcnt(0)
s_waitcnt lgkmcnt(0)
SV_w2_W:
s_memrealtime s[94:95]
s_waitcnt lgkmcnt(0)
s_sub_u32 s94, s92, s94
s_lshl_b32 s94, s94, 1
s_cmp_gt_i32 s94, 20000
s_cbranch_scc1 SV_w2_WG
s_cmp_gt_i32 s94, 16
s_cbranch_scc1 SV_w2_W
s_cmp_lt_i32 s94, 6
s_cbranch_scc1 SV_w2_WG
s_nop 3
s_cmp_lt_i32 s94, 8
s_cbranch_scc1 SV_w2_WG
s_nop 3
s_cmp_lt_i32 s94, 10
s_cbranch_scc1 SV_w2_WG
s_nop 3
s_cmp_lt_i32 s94, 12
s_cbranch_scc1 SV_w2_WG
s_nop 3
s_cmp_lt_i32 s94, 14
s_cbranch_scc1 SV_w2_WG
s_nop 3
s_cmp_lt_i32 s94, 16
s_cbranch_scc1 SV_w2_WG
s_nop 3
SV_w2_WG:
SV_w2_L:
buffer_load_dwordx4 v[32:35], v116, s[20:23], s57 offen offset:0 sc1
buffer_load_dwordx4 v[36:39], v116, s[20:23], s57 offen offset:1024 sc1
buffer_load_dwordx2 v[40:41], v118, s[20:23], s58 offen offset:1024 sc1
s_waitcnt vmcnt(2)
v_max3_u32 v0, v32, v33, v34
s_waitcnt vmcnt(1)
v_max3_u32 v0, v0, v35, v36
v_max3_u32 v0, v0, v37, v38
s_waitcnt vmcnt(0)
v_max3_u32 v0, v0, v39, v40
v_max_u32 v0, v0, v41
v_cmp_eq_u32 vcc, -1, v0
s_cbranch_vccnz SV_w2_F
SV_w2_H:
ds_write_b128 v116, v[32:35] offset:53248
ds_write_b128 v116, v[36:39] offset:54272
ds_write_b64 v118, v[40:41] offset:58368
v_smfmac_f32_16x16x64_bf16 v[44:47], v[32:35], a[64:71], v119
v_smfmac_f32_16x16x64_bf16 v[48:51], v[32:35], a[72:79], v119
v_smfmac_f32_16x16x64_bf16 v[52:55], v[32:35], a[160:167], v119
v_smfmac_f32_16x16x64_bf16 v[44:47], v[36:39], a[80:87], v119
s_waitcnt lgkmcnt(0)
s_barrier
ds_read_b128 v[0:3], v116 offset:57344
ds_read_b128 v[4:7], v116 offset:55296
v_smfmac_f32_16x16x64_bf16 v[48:51], v[36:39], a[88:95], v119
ds_read_b128 v[8:11], v116 offset:56320
v_smfmac_f32_16x16x64_bf16 v[52:55], v[36:39], a[168:175], v119
ds_read_b128 v[12:15], v116 offset:49152
s_waitcnt lgkmcnt(3)
v_smfmac_f32_16x16x64_bf16 v[44:47], v[0:3], v[56:63], v119
ds_read_b128 v[16:19], v116 offset:50176
v_smfmac_f32_16x16x64_bf16 v[48:51], v[0:3], v[64:71], v119
s_waitcnt lgkmcnt(3)
ds_read_b128 v[20:23], v116 offset:51200
v_smfmac_f32_16x16x64_bf16 v[44:47], v[4:7], a[96:103], v119
ds_read_b128 v[24:27], v116 offset:52224
v_smfmac_f32_16x16x64_bf16 v[48:51], v[4:7], a[104:111], v119
s_waitcnt lgkmcnt(4)
ds_read_b128 v[28:31], v116 offset:58368
v_smfmac_f32_16x16x64_bf16 v[44:47], v[8:11], a[112:119], v119
ds_read_b128 v[56:59], v117 offset:24576
v_smfmac_f32_16x16x64_bf16 v[48:51], v[8:11], a[120:127], v119
s_waitcnt lgkmcnt(5)
ds_read_b128 v[60:63], v117 offset:25600
v_smfmac_f32_16x16x64_bf16 v[44:47], v[12:15], a[0:7], v119
ds_read_b128 v[64:67], v117 offset:26624
v_smfmac_f32_16x16x64_bf16 v[48:51], v[12:15], a[8:15], v119
s_waitcnt lgkmcnt(6)
ds_read_b128 v[68:71], v117 offset:27648
v_smfmac_f32_16x16x64_bf16 v[44:47], v[16:19], a[16:23], v119
ds_read_b128 v[32:35], v117 offset:40960
v_smfmac_f32_16x16x64_bf16 v[48:51], v[16:19], a[24:31], v119
s_waitcnt lgkmcnt(7)
ds_read_b128 v[36:39], v117 offset:41984
v_smfmac_f32_16x16x64_bf16 v[44:47], v[20:23], a[32:39], v119
v_smfmac_f32_16x16x64_bf16 v[48:51], v[20:23], a[40:47], v119
s_waitcnt lgkmcnt(7)
v_smfmac_f32_16x16x64_bf16 v[44:47], v[24:27], a[48:55], v119
v_smfmac_f32_16x16x64_bf16 v[48:51], v[24:27], a[56:63], v119
s_waitcnt lgkmcnt(4)
v_smfmac_f32_16x16x64_bf16 v[44:47], v[28:31], v[56:63], v119
s_waitcnt lgkmcnt(2)
ds_read_b128 v[56:59], v117 offset:43008
v_smfmac_f32_16x16x64_bf16 v[48:51], v[28:31], v[64:71], v119
s_waitcnt lgkmcnt(1)
ds_read_b128 v[60:63], v117 offset:44032
v_smfmac_f32_16x16x64_bf16 v[52:55], v[0:3], v[32:39], v119
ds_read_b128 v[64:67], v116 offset:8192
v_smfmac_f32_16x16x64_bf16 v[52:55], v[4:7], a[176:183], v119
ds_read_b128 v[68:71], v116 offset:9216
v_add_f32 v44, v44, v45
v_add_f32 v46, v46, v47
v_smfmac_f32_16x16x64_bf16 v[52:55], v[8:11], a[184:191], v119
ds_read_b128 v[32:35], v116 offset:24576
v_cvt_pk_bf16_f32 v44, v44, v46
v_smfmac_f32_16x16x64_bf16 v[52:55], v[12:15], a[128:135], v119
ds_write_b16 v120, v44 offset:60416
ds_read_b128 v[36:39], v116 offset:25600
ds_write_b16_d16_hi v120, v44 offset:60420
v_add_f32 v48, v48, v49
v_add_f32 v50, v50, v51
v_smfmac_f32_16x16x64_bf16 v[52:55], v[16:19], a[136:143], v119
ds_read_b128 v[12:15], v116 offset:40960
v_cvt_pk_bf16_f32 v48, v48, v50
v_smfmac_f32_16x16x64_bf16 v[52:55], v[20:23], a[144:151], v119
ds_write_b16 v120, v48 offset:60672
ds_read_b128 v[16:19], v116 offset:41984
ds_write_b16_d16_hi v120, v48 offset:60676
v_smfmac_f32_16x16x64_bf16 v[52:55], v[24:27], a[152:159], v119
s_waitcnt lgkmcnt(10)
s_waitcnt lgkmcnt(0)
s_barrier
ds_read_b64_tr_b16 v[0:1], v122 offset:59392
v_smfmac_f32_16x16x64_bf16 v[52:55], v[28:31], v[56:63], v119
ds_read_b64_tr_b16 v[2:3], v122 offset:59520
ds_read_b64_tr_b16 v[4:5], v122 offset:60416
ds_read_b64_tr_b16 v[6:7], v122 offset:60544
s_nop 4
v_add_f32 v52, v52, v53
v_add_f32 v54, v54, v55
v_cvt_pk_bf16_f32 v52, v52, v54
ds_write_b16 v120, v52 offset:61952
ds_write_b16_d16_hi v120, v52 offset:61956
s_waitcnt lgkmcnt(4)
v_smfmac_f32_16x16x64_bf16 v[72:75], v[0:3], a[192:199], v119
s_waitcnt lgkmcnt(2)
v_smfmac_f32_16x16x64_bf16 v[72:75], v[4:7], a[208:215], v119
v_smfmac_f32_16x16x64_bf16 v[76:79], v[0:3], a[200:207], v119
s_waitcnt lgkmcnt(0)
s_barrier
ds_read_b64_tr_b16 v[8:9], v122 offset:61440
ds_read_b64_tr_b16 v[10:11], v122 offset:61568
v_smfmac_f32_16x16x64_bf16 v[76:79], v[4:7], a[216:223], v119
v_smfmac_f32_16x16x64_bf16 v[80:83], v[0:3], a[240:247], v119
ds_read_b128 v[20:23], v116 offset:10240
v_smfmac_f32_16x16x64_bf16 v[80:83], v[4:7], v[128:135], v119
ds_read_b128 v[24:27], v116 offset:11264
v_smfmac_f32_16x16x64_bf16 v[84:87], v[0:3], a[248:255], v119
ds_read_b128 v[56:59], v116 offset:26624
v_smfmac_f32_16x16x64_bf16 v[84:87], v[4:7], v[136:143], v119
s_waitcnt lgkmcnt(3)
ds_read_b128 v[60:63], v116 offset:27648
v_smfmac_f32_16x16x64_bf16 v[72:75], v[8:11], a[224:231], v119
ds_read_b128 v[44:47], v116 offset:43008
v_smfmac_f32_16x16x64_bf16 v[76:79], v[8:11], a[232:239], v119
ds_read_b128 v[48:51], v116 offset:44032
v_smfmac_f32_16x16x64_bf16 v[80:83], v[8:11], v[144:151], v119
v_smfmac_f32_16x16x64_bf16 v[84:87], v[8:11], v[152:159], v119
v_smfmac_f32_16x16x64_bf16 v[88:91], v[0:3], v[160:167], v119
s_nop 1
v_add_f32 v72, v72, v73
v_add_f32 v74, v74, v75
v_smfmac_f32_16x16x64_bf16 v[88:91], v[4:7], v[176:183], v119
v_cvt_pk_bf16_f32 v112, v72, v74
v_smfmac_f32_16x16x64_bf16 v[88:91], v[8:11], v[192:199], v119
v_add_f32 v76, v76, v77
v_add_f32 v78, v78, v79
v_smfmac_f32_16x16x64_bf16 v[92:95], v[0:3], v[168:175], v119
v_cvt_pk_bf16_f32 v113, v76, v78
v_smfmac_f32_16x16x64_bf16 v[92:95], v[4:7], v[184:191], v119
v_add_f32 v80, v80, v81
v_add_f32 v82, v82, v83
v_smfmac_f32_16x16x64_bf16 v[92:95], v[8:11], v[200:207], v119
v_cvt_pk_bf16_f32 v114, v80, v82
v_smfmac_f32_16x16x64_bf16 v[96:99], v[0:3], v[208:215], v119
v_add_f32 v84, v84, v85
v_add_f32 v86, v86, v87
v_smfmac_f32_16x16x64_bf16 v[96:99], v[4:7], v[224:231], v119
v_cvt_pk_bf16_f32 v115, v84, v86
v_smfmac_f32_16x16x64_bf16 v[96:99], v[8:11], v[240:247], v119
v_permlane32_swap_b32 v112, v114
v_permlane32_swap_b32 v113, v115
v_smfmac_f32_16x16x64_bf16 v[100:103], v[0:3], v[216:223], v119
s_nop 0
v_permlane16_swap_b32 v112, v113
v_smfmac_f32_16x16x64_bf16 v[100:103], v[4:7], v[232:239], v119
v_permlane16_swap_b32 v114, v115
v_smfmac_f32_16x16x64_bf16 v[100:103], v[8:11], v[248:255], v119
s_add_u32 s42, s38, 65536
buffer_store_dwordx4 v[112:115], v121, s[24:27], s42 offen offset:0 sc1
v_add_f32 v88, v88, v89
v_smfmac_f32_16x16x64_bf16 v[104:107], v[0:3], v[64:71], v119
v_add_f32 v90, v90, v91
v_cvt_pk_bf16_f32 v28, v88, v90
v_smfmac_f32_16x16x64_bf16 v[104:107], v[4:7], v[32:39], v119
v_add_f32 v92, v92, v93
v_smfmac_f32_16x16x64_bf16 v[104:107], v[8:11], v[12:19], v119
s_waitcnt lgkmcnt(4)
v_add_f32 v94, v94, v95
v_cvt_pk_bf16_f32 v29, v92, v94
v_smfmac_f32_16x16x64_bf16 v[108:111], v[0:3], v[20:27], v119
s_waitcnt lgkmcnt(2)
v_add_f32 v96, v96, v97
v_smfmac_f32_16x16x64_bf16 v[108:111], v[4:7], v[56:63], v119
s_waitcnt lgkmcnt(0)
v_add_f32 v98, v98, v99
v_cvt_pk_bf16_f32 v30, v96, v98
v_smfmac_f32_16x16x64_bf16 v[108:111], v[8:11], v[44:51], v119
v_add_f32 v100, v100, v101
v_add_f32 v102, v102, v103
v_cvt_pk_bf16_f32 v31, v100, v102
v_permlane32_swap_b32 v28, v30
s_nop 0
v_permlane32_swap_b32 v29, v31
s_nop 1
v_permlane16_swap_b32 v28, v29
v_permlane16_swap_b32 v30, v31
s_add_u32 s42, s38, 327680
buffer_store_dwordx4 v[28:31], v121, s[24:27], s42 offen offset:0 sc1
v_add_f32 v104, v104, v105
v_add_f32 v106, v106, v107
v_cvt_pk_bf16_f32 v112, v104, v106
v_add_f32 v108, v108, v109
v_add_f32 v110, v110, v111
v_cvt_pk_bf16_f32 v113, v108, v110
v_permlane32_swap_b32 v112, v114
s_nop 0
v_permlane32_swap_b32 v113, v115
s_nop 1
v_permlane16_swap_b32 v112, v113
v_permlane16_swap_b32 v114, v115
s_add_u32 s42, s38, 589824
s_mov_b32 exec_hi, 0
buffer_store_dwordx4 v[112:115], v121, s[24:27], s42 offen offset:0 sc1
s_mov_b32 exec_hi, -1
s_cmp_gt_u32 s36, 61
s_cbranch_scc1 SV_w2_NA
s_add_u32 s42, s36, 2
s_lshl_b32 s38, s42, 20
s_lshr_b32 s43, s38, 8
s_xor_b32 s43, s43, s39
s_and_b32 s43, s43, 8192
s_or_b32 s38, s38, s43
v_mov_b32 v32, -1
v_mov_b32 v33, -1
v_mov_b32 v34, -1
v_mov_b32 v35, -1
s_add_u32 s42, s38, 65536
buffer_store_dwordx4 v[32:35], v121, s[24:27], s42 offen offset:0 sc1
s_add_u32 s42, s38, 327680
buffer_store_dwordx4 v[32:35], v121, s[24:27], s42 offen offset:0 sc1
s_add_u32 s42, s38, 589824
s_mov_b32 exec_hi, 0
buffer_store_dwordx4 v[32:35], v121, s[24:27], s42 offen offset:0 sc1
s_mov_b64 exec, -1
SV_w2_NA:
s_cmp_eq_u32 s36, 0
s_cbranch_scc1 SV_w2_Z
s_cmp_eq_u32 s36, 63
s_cbranch_scc1 SV_END
v_mov_b32 v1, 0
s_add_u32 s42, s37, 16384
SV_w2_S:
buffer_load_dword v0, v1, s[20:23], s42 offen offset:3076 sc1
s_waitcnt vmcnt(0)
v_readfirstlane_b32 s92, v0
s_nop 0
s_cmp_lg_u32 s92, -1
s_cbranch_scc1 SV_w2_SK
s_sleep 2
s_sub_u32 s41, s41, 1
s_cbranch_scc0 SV_w2_S
s_mov_b32 s41, 0
SV_w2_SK:
SV_w2_Z:
s_add_u32 s36, s36, 1
s_branch SV_w2
SV_w2_F:
s_sub_u32 s41, s41, 1
s_cbranch_scc1 SV_w2_G
s_branch SV_w2_L
SV_w2_G:
s_mov_b32 s41, 0
s_branch SV_w2_H
SV_w3:
s_add_u32 s42, s36, 0
s_lshl_b32 s37, s42, 16
s_lshr_b32 s43, s37, 8
s_xor_b32 s43, s43, s39
s_and_b32 s43, s43, 8192
s_or_b32 s37, s37, s43
s_add_u32 s57, s37, 4096
s_add_u32 s58, s37, 16384
s_add_u32 s42, s36, 0
s_lshl_b32 s38, s42, 20
s_lshr_b32 s43, s38, 8
s_xor_b32 s43, s43, s39
s_and_b32 s43, s43, 8192
s_or_b32 s38, s38, s43
v_mov_b32 v44, 0
v_mov_b32 v45, 0
v_mov_b32 v46, 0
v_mov_b32 v47, 0
v_mov_b32 v48, 0
v_mov_b32 v49, 0
v_mov_b32 v50, 0
v_mov_b32 v51, 0
v_mov_b32 v52, 0
v_mov_b32 v53, 0
v_mov_b32 v54, 0
v_mov_b32 v55, 0
v_mov_b32 v72, 0
v_mov_b32 v73, 0
v_mov_b32 v74, 0
v_mov_b32 v75, 0
v_mov_b32 v76, 0
v_mov_b32 v77, 0
v_mov_b32 v78, 0
v_mov_b32 v79, 0
v_mov_b32 v80, 0
v_mov_b32 v81, 0
v_mov_b32 v82, 0
v_mov_b32 v83, 0
v_mov_b32 v84, 0
v_mov_b32 v85, 0
v_mov_b32 v86, 0
v_mov_b32 v87, 0
v_mov_b32 v88, 0
v_mov_b32 v89, 0
v_mov_b32 v90, 0
v_mov_b32 v91, 0
v_mov_b32 v92, 0
v_mov_b32 v93, 0
v_mov_b32 v94, 0
v_mov_b32 v95, 0
v_mov_b32 v96, 0
v_mov_b32 v97, 0
v_mov_b32 v98, 0
v_mov_b32 v99, 0
v_mov_b32 v100, 0
v_mov_b32 v101, 0
v_mov_b32 v102, 0
v_mov_b32 v103, 0
v_mov_b32 v104, 0
v_mov_b32 v105, 0
v_mov_b32 v106, 0
v_mov_b32 v107, 0
v_mov_b32 v108, 0
v_mov_b32 v109, 0
v_mov_b32 v110, 0
v_mov_b32 v111, 0
ds_read_b128 v[56:59], v117 offset:12288
ds_read_b128 v[60:63], v117 offset:13312
ds_read_b128 v[64:67], v117 offset:14336
ds_read_b128 v[68:71], v117 offset:15360
s_waitcnt lgkmcnt(0)
s_waitcnt vmcnt(0)
s_waitcnt lgkmcnt(0)
SV_w3_W:
s_memrealtime s[94:95]
s_waitcnt lgkmcnt(0)
s_sub_u32 s94, s92, s94
s_lshl_b32 s94, s94, 1
s_cmp_gt_i32 s94, 20000
s_cbranch_scc1 SV_w3_WG
s_cmp_gt_i32 s94, 16
s_cbranch_scc1 SV_w3_W
s_cmp_lt_i32 s94, 6
s_cbranch_scc1 SV_w3_WG
s_nop 3
s_cmp_lt_i32 s94, 8
s_cbranch_scc1 SV_w3_WG
s_nop 3
s_cmp_lt_i32 s94, 10
s_cbranch_scc1 SV_w3_WG
s_nop 3
s_cmp_lt_i32 s94, 12
s_cbranch_scc1 SV_w3_WG
s_nop 3
s_cmp_lt_i32 s94, 14
s_cbranch_scc1 SV_w3_WG
s_nop 3
s_cmp_lt_i32 s94, 16
s_cbranch_scc1 SV_w3_WG
s_nop 3
SV_w3_WG:
SV_w3_L:
buffer_load_dwordx4 v[32:35], v116, s[20:23], s57 offen offset:2048 sc1
buffer_load_dwordx4 v[36:39], v116, s[20:23], s57 offen offset:3072 sc1
buffer_load_dwordx2 v[40:41], v118, s[20:23], s58 offen offset:1536 sc1
s_waitcnt vmcnt(2)
v_max3_u32 v0, v32, v33, v34
s_waitcnt vmcnt(1)
v_max3_u32 v0, v0, v35, v36
v_max3_u32 v0, v0, v37, v38
s_waitcnt vmcnt(0)
v_max3_u32 v0, v0, v39, v40
v_max_u32 v0, v0, v41
v_cmp_eq_u32 vcc, -1, v0
s_cbranch_vccnz SV_w3_F
SV_w3_H:
ds_write_b128 v116, v[32:35] offset:55296
ds_write_b128 v116, v[36:39] offset:56320
ds_write_b64 v118, v[40:41] offset:58880
v_smfmac_f32_16x16x64_bf16 v[44:47], v[32:35], a[96:103], v119
v_smfmac_f32_16x16x64_bf16 v[48:51], v[32:35], a[104:111], v119
v_smfmac_f32_16x16x64_bf16 v[52:55], v[32:35], a[176:183], v119
v_smfmac_f32_16x16x64_bf16 v[44:47], v[36:39], a[112:119], v119
s_waitcnt lgkmcnt(0)
s_barrier
ds_read_b128 v[0:3], v116 offset:57344
ds_read_b128 v[4:7], v116 offset:49152
v_smfmac_f32_16x16x64_bf16 v[48:51], v[36:39], a[120:127], v119
ds_read_b128 v[8:11], v116 offset:50176
v_smfmac_f32_16x16x64_bf16 v[52:55], v[36:39], a[184:191], v119
ds_read_b128 v[12:15], v116 offset:51200
s_waitcnt lgkmcnt(3)
v_smfmac_f32_16x16x64_bf16 v[44:47], v[0:3], v[56:63], v119
ds_read_b128 v[16:19], v116 offset:52224
v_smfmac_f32_16x16x64_bf16 v[48:51], v[0:3], v[64:71], v119
s_waitcnt lgkmcnt(3)
ds_read_b128 v[20:23], v116 offset:53248
v_smfmac_f32_16x16x64_bf16 v[44:47], v[4:7], a[0:7], v119
ds_read_b128 v[24:27], v116 offset:54272
v_smfmac_f32_16x16x64_bf16 v[48:51], v[4:7], a[8:15], v119
s_waitcnt lgkmcnt(4)
ds_read_b128 v[28:31], v116 offset:58368
v_smfmac_f32_16x16x64_bf16 v[44:47], v[8:11], a[16:23], v119
ds_read_b128 v[56:59], v117 offset:28672
v_smfmac_f32_16x16x64_bf16 v[48:51], v[8:11], a[24:31], v119
s_waitcnt lgkmcnt(5)
ds_read_b128 v[60:63], v117 offset:29696
v_smfmac_f32_16x16x64_bf16 v[44:47], v[12:15], a[32:39], v119
ds_read_b128 v[64:67], v117 offset:30720
v_smfmac_f32_16x16x64_bf16 v[48:51], v[12:15], a[40:47], v119
s_waitcnt lgkmcnt(6)
ds_read_b128 v[68:71], v117 offset:31744
v_smfmac_f32_16x16x64_bf16 v[44:47], v[16:19], a[48:55], v119
ds_read_b128 v[32:35], v117 offset:45056
v_smfmac_f32_16x16x64_bf16 v[48:51], v[16:19], a[56:63], v119
s_waitcnt lgkmcnt(7)
ds_read_b128 v[36:39], v117 offset:46080
v_smfmac_f32_16x16x64_bf16 v[44:47], v[20:23], a[64:71], v119
v_smfmac_f32_16x16x64_bf16 v[48:51], v[20:23], a[72:79], v119
s_waitcnt lgkmcnt(7)
v_smfmac_f32_16x16x64_bf16 v[44:47], v[24:27], a[80:87], v119
v_smfmac_f32_16x16x64_bf16 v[48:51], v[24:27], a[88:95], v119
s_waitcnt lgkmcnt(4)
v_smfmac_f32_16x16x64_bf16 v[44:47], v[28:31], v[56:63], v119
s_waitcnt lgkmcnt(2)
ds_read_b128 v[56:59], v117 offset:47104
v_smfmac_f32_16x16x64_bf16 v[48:51], v[28:31], v[64:71], v119
s_waitcnt lgkmcnt(1)
ds_read_b128 v[60:63], v117 offset:48128
v_smfmac_f32_16x16x64_bf16 v[52:55], v[0:3], v[32:39], v119
ds_read_b128 v[64:67], v116 offset:12288
v_smfmac_f32_16x16x64_bf16 v[52:55], v[4:7], a[128:135], v119
ds_read_b128 v[68:71], v116 offset:13312
v_add_f32 v44, v44, v45
v_add_f32 v46, v46, v47
v_smfmac_f32_16x16x64_bf16 v[52:55], v[8:11], a[136:143], v119
ds_read_b128 v[32:35], v116 offset:28672
v_cvt_pk_bf16_f32 v44, v44, v46
v_smfmac_f32_16x16x64_bf16 v[52:55], v[12:15], a[144:151], v119
ds_write_b16 v120, v44 offset:60928
ds_read_b128 v[36:39], v116 offset:29696
ds_write_b16_d16_hi v120, v44 offset:60932
v_add_f32 v48, v48, v49
v_add_f32 v50, v50, v51
v_smfmac_f32_16x16x64_bf16 v[52:55], v[16:19], a[152:159], v119
ds_read_b128 v[12:15], v116 offset:45056
v_cvt_pk_bf16_f32 v48, v48, v50
v_smfmac_f32_16x16x64_bf16 v[52:55], v[20:23], a[160:167], v119
ds_write_b16 v120, v48 offset:61184
ds_read_b128 v[16:19], v116 offset:46080
ds_write_b16_d16_hi v120, v48 offset:61188
v_smfmac_f32_16x16x64_bf16 v[52:55], v[24:27], a[168:175], v119
s_waitcnt lgkmcnt(10)
s_waitcnt lgkmcnt(0)
s_barrier
ds_read_b64_tr_b16 v[0:1], v122 offset:59392
v_smfmac_f32_16x16x64_bf16 v[52:55], v[28:31], v[56:63], v119
ds_read_b64_tr_b16 v[2:3], v122 offset:59520
ds_read_b64_tr_b16 v[4:5], v122 offset:60416
ds_read_b64_tr_b16 v[6:7], v122 offset:60544
s_nop 4
v_add_f32 v52, v52, v53
v_add_f32 v54, v54, v55
v_cvt_pk_bf16_f32 v52, v52, v54
ds_write_b16 v120, v52 offset:62208
ds_write_b16_d16_hi v120, v52 offset:62212
s_waitcnt lgkmcnt(4)
v_smfmac_f32_16x16x64_bf16 v[72:75], v[0:3], a[192:199], v119
s_waitcnt lgkmcnt(2)
v_smfmac_f32_16x16x64_bf16 v[72:75], v[4:7], a[208:215], v119
v_smfmac_f32_16x16x64_bf16 v[76:79], v[0:3], a[200:207], v119
s_waitcnt lgkmcnt(0)
s_barrier
ds_read_b64_tr_b16 v[8:9], v122 offset:61440
ds_read_b64_tr_b16 v[10:11], v122 offset:61568
v_smfmac_f32_16x16x64_bf16 v[76:79], v[4:7], a[216:223], v119
v_smfmac_f32_16x16x64_bf16 v[80:83], v[0:3], a[240:247], v119
ds_read_b128 v[20:23], v116 offset:14336
v_smfmac_f32_16x16x64_bf16 v[80:83], v[4:7], v[128:135], v119
ds_read_b128 v[24:27], v116 offset:15360
v_smfmac_f32_16x16x64_bf16 v[84:87], v[0:3], a[248:255], v119
ds_read_b128 v[56:59], v116 offset:30720
v_smfmac_f32_16x16x64_bf16 v[84:87], v[4:7], v[136:143], v119
s_waitcnt lgkmcnt(3)
ds_read_b128 v[60:63], v116 offset:31744
v_smfmac_f32_16x16x64_bf16 v[72:75], v[8:11], a[224:231], v119
ds_read_b128 v[44:47], v116 offset:47104
v_smfmac_f32_16x16x64_bf16 v[76:79], v[8:11], a[232:239], v119
ds_read_b128 v[48:51], v116 offset:48128
v_smfmac_f32_16x16x64_bf16 v[80:83], v[8:11], v[144:151], v119
v_smfmac_f32_16x16x64_bf16 v[84:87], v[8:11], v[152:159], v119
v_smfmac_f32_16x16x64_bf16 v[88:91], v[0:3], v[160:167], v119
s_nop 1
v_add_f32 v72, v72, v73
v_add_f32 v74, v74, v75
v_smfmac_f32_16x16x64_bf16 v[88:91], v[4:7], v[176:183], v119
v_cvt_pk_bf16_f32 v112, v72, v74
v_smfmac_f32_16x16x64_bf16 v[88:91], v[8:11], v[192:199], v119
v_add_f32 v76, v76, v77
v_add_f32 v78, v78, v79
v_smfmac_f32_16x16x64_bf16 v[92:95], v[0:3], v[168:175], v119
v_cvt_pk_bf16_f32 v113, v76, v78
v_smfmac_f32_16x16x64_bf16 v[92:95], v[4:7], v[184:191], v119
v_add_f32 v80, v80, v81
v_add_f32 v82, v82, v83
v_smfmac_f32_16x16x64_bf16 v[92:95], v[8:11], v[200:207], v119
v_cvt_pk_bf16_f32 v114, v80, v82
v_smfmac_f32_16x16x64_bf16 v[96:99], v[0:3], v[208:215], v119
v_add_f32 v84, v84, v85
v_add_f32 v86, v86, v87
v_smfmac_f32_16x16x64_bf16 v[96:99], v[4:7], v[224:231], v119
v_cvt_pk_bf16_f32 v115, v84, v86
v_smfmac_f32_16x16x64_bf16 v[96:99], v[8:11], v[240:247], v119
v_permlane32_swap_b32 v112, v114
v_permlane32_swap_b32 v113, v115
v_smfmac_f32_16x16x64_bf16 v[100:103], v[0:3], v[216:223], v119
s_nop 0
v_permlane16_swap_b32 v112, v113
v_smfmac_f32_16x16x64_bf16 v[100:103], v[4:7], v[232:239], v119
v_permlane16_swap_b32 v114, v115
v_smfmac_f32_16x16x64_bf16 v[100:103], v[8:11], v[248:255], v119
s_add_u32 s42, s38, 98304
buffer_store_dwordx4 v[112:115], v121, s[24:27], s42 offen offset:0 sc1
v_add_f32 v88, v88, v89
v_smfmac_f32_16x16x64_bf16 v[104:107], v[0:3], v[64:71], v119
v_add_f32 v90, v90, v91
v_cvt_pk_bf16_f32 v28, v88, v90
v_smfmac_f32_16x16x64_bf16 v[104:107], v[4:7], v[32:39], v119
v_add_f32 v92, v92, v93
v_smfmac_f32_16x16x64_bf16 v[104:107], v[8:11], v[12:19], v119
s_waitcnt lgkmcnt(4)
v_add_f32 v94, v94, v95
v_cvt_pk_bf16_f32 v29, v92, v94
v_smfmac_f32_16x16x64_bf16 v[108:111], v[0:3], v[20:27], v119
s_waitcnt lgkmcnt(2)
v_add_f32 v96, v96, v97
v_smfmac_f32_16x16x64_bf16 v[108:111], v[4:7], v[56:63], v119
s_waitcnt lgkmcnt(0)
v_add_f32 v98, v98, v99
v_cvt_pk_bf16_f32 v30, v96, v98
v_smfmac_f32_16x16x64_bf16 v[108:111], v[8:11], v[44:51], v119
v_add_f32 v100, v100, v101
v_add_f32 v102, v102, v103
v_cvt_pk_bf16_f32 v31, v100, v102
v_permlane32_swap_b32 v28, v30
s_nop 0
v_permlane32_swap_b32 v29, v31
s_nop 1
v_permlane16_swap_b32 v28, v29
v_permlane16_swap_b32 v30, v31
s_add_u32 s42, s38, 360448
buffer_store_dwordx4 v[28:31], v121, s[24:27], s42 offen offset:0 sc1
v_add_f32 v104, v104, v105
v_add_f32 v106, v106, v107
v_cvt_pk_bf16_f32 v112, v104, v106
v_add_f32 v108, v108, v109
v_add_f32 v110, v110, v111
v_cvt_pk_bf16_f32 v113, v108, v110
v_permlane32_swap_b32 v112, v114
s_nop 0
v_permlane32_swap_b32 v113, v115
s_nop 1
v_permlane16_swap_b32 v112, v113
v_permlane16_swap_b32 v114, v115
s_add_u32 s42, s38, 622592
s_mov_b32 exec_hi, 0
buffer_store_dwordx4 v[112:115], v121, s[24:27], s42 offen offset:0 sc1
s_mov_b32 exec_hi, -1
s_cmp_gt_u32 s36, 61
s_cbranch_scc1 SV_w3_NA
s_add_u32 s42, s36, 2
s_lshl_b32 s38, s42, 20
s_lshr_b32 s43, s38, 8
s_xor_b32 s43, s43, s39
s_and_b32 s43, s43, 8192
s_or_b32 s38, s38, s43
v_mov_b32 v32, -1
v_mov_b32 v33, -1
v_mov_b32 v34, -1
v_mov_b32 v35, -1
s_add_u32 s42, s38, 98304
buffer_store_dwordx4 v[32:35], v121, s[24:27], s42 offen offset:0 sc1
s_add_u32 s42, s38, 360448
buffer_store_dwordx4 v[32:35], v121, s[24:27], s42 offen offset:0 sc1
s_add_u32 s42, s38, 622592
s_mov_b32 exec_hi, 0
buffer_store_dwordx4 v[32:35], v121, s[24:27], s42 offen offset:0 sc1
s_mov_b64 exec, -1
SV_w3_NA:
s_cmp_eq_u32 s36, 0
s_cbranch_scc1 SV_w3_Z
s_cmp_eq_u32 s36, 63
s_cbranch_scc1 SV_END
v_mov_b32 v1, 0
s_add_u32 s42, s37, 16384
SV_w3_S:
buffer_load_dword v0, v1, s[20:23], s42 offen offset:3076 sc1
s_waitcnt vmcnt(0)
v_readfirstlane_b32 s92, v0
s_nop 0
s_cmp_lg_u32 s92, -1
s_cbranch_scc1 SV_w3_SK
s_sleep 2
s_sub_u32 s41, s41, 1
s_cbranch_scc0 SV_w3_S
s_mov_b32 s41, 0
SV_w3_SK:
SV_w3_Z:
s_add_u32 s36, s36, 1
s_branch SV_w3
SV_w3_F:
s_sub_u32 s41, s41, 1
s_cbranch_scc1 SV_w3_G
s_branch SV_w3_L
SV_w3_G:
s_mov_b32 s41, 0
s_branch SV_w3_H
SV_END:
s_endpgm
ROLE_S2:
s_mov_b64 s[48:49], %0
s_mov_b64 s[50:51], %1
s_mov_b64 s[52:53], %2
s_mov_b32 s54, %3
s_mov_b32 s40, %4
s_mov_b32 s39, %5
s_mov_b32 s41, 0x200000
s_mov_b32 s36, 0
s_lshl_b32 s55, s40, 10
v_mbcnt_lo_u32_b32 v31, -1, 0
v_mbcnt_hi_u32_b32 v31, -1, v31
s_lshl_b32 s42, s40, 8
s_add_u32 s42, s42, 147456
v_lshl_add_u32 v31, v31, 2, s42
ds_read_b32 v144, v31 offset:0
ds_read_b32 v145, v31 offset:1024
ds_read_b32 v146, v31 offset:2048
ds_read_b32 v147, v31 offset:3072
ds_read_b32 v148, v31 offset:4096
ds_read_b32 v153, v31 offset:5120
s_waitcnt lgkmcnt(0)
s_memrealtime s[94:95]
s_waitcnt lgkmcnt(0)
s_mov_b32 s92, s94
s_add_u32 s20, s48, 0x0
s_addc_u32 s21, s49, 0
s_and_b32 s21, s21, 0xffff
s_mov_b32 s22, 0x400000
s_mov_b32 s23, 0x20000
s_add_u32 s96, s48, 0x0
s_addc_u32 s97, s49, 0
s_and_b32 s97, s97, 0xffff
s_mov_b32 s98, 0xfe00000
s_mov_b32 s99, 0x20000
s_add_u32 s24, s48, 0xa000000
s_addc_u32 s25, s49, 0
s_and_b32 s25, s25, 0xffff
s_mov_b32 s26, 0x4000000
s_mov_b32 s27, 0x20000
s_add_u32 s28, s48, 0x5000000
s_addc_u32 s29, s49, 0
s_and_b32 s29, s29, 0xffff
s_mov_b32 s30, 0x5000000
s_mov_b32 s31, 0x20000
v_and_b32 v151, 16, v144
v_cmp_ne_u32 s[78:79], 0, v151
s_mov_b32 s93, s92
s_nop 3
s_cmp_eq_u32 s40, 1
s_cbranch_scc1 S2_H
S2_T:
s_add_u32 s42, s36, 1
s_lshl_b32 s38, s42, 16
s_lshr_b32 s43, s38, 8
s_xor_b32 s43, s43, s39
s_and_b32 s43, s43, 8192
s_or_b32 s38, s38, s43
s_add_u32 s42, s36, 0
s_lshl_b32 s37, s42, 16
s_lshr_b32 s43, s37, 8
s_xor_b32 s43, s43, s39
s_and_b32 s43, s43, 8192
s_or_b32 s37, s37, s43
s_add_u32 s58, s36, 1
S2_V:
buffer_load_dword v151, v153, s[96:99], 0 offen offset:0 sc1
s_waitcnt vmcnt(0)
v_cmp_gt_u32 vcc, s58, v151
s_cbranch_vccz S2_VK
s_sleep 3
s_sub_u32 s41, s41, 1
s_cbranch_scc0 S2_V
s_mov_b32 s41, 0
S2_VK:
s_mul_i32 s42, s36, 40
s_add_u32 s42, s42, s54
s_lshl_b32 s57, s42, 15
buffer_load_dwordx4 a[0:3], v144, s[28:31], s57 offen offset:0 sc1
buffer_load_dwordx4 a[4:7], v144, s[28:31], s57 offen offset:1024 sc1
buffer_load_dwordx4 a[8:11], v144, s[28:31], s57 offen offset:2048 sc1
buffer_load_dwordx4 a[12:15], v144, s[28:31], s57 offen offset:3072 sc1
s_add_u32 s57, s57, 4096
buffer_load_dwordx4 a[16:19], v144, s[28:31], s57 offen offset:0 sc1
buffer_load_dwordx4 a[20:23], v144, s[28:31], s57 offen offset:1024 sc1
buffer_load_dwordx4 a[24:27], v144, s[28:31], s57 offen offset:2048 sc1
buffer_load_dwordx4 a[28:31], v144, s[28:31], s57 offen offset:3072 sc1
s_add_u32 s57, s57, 4096
buffer_load_dwordx4 a[32:35], v144, s[28:31], s57 offen offset:0 sc1
buffer_load_dwordx4 a[36:39], v144, s[28:31], s57 offen offset:1024 sc1
buffer_load_dwordx4 a[40:43], v144, s[28:31], s57 offen offset:2048 sc1
buffer_load_dwordx4 a[44:47], v144, s[28:31], s57 offen offset:3072 sc1
s_add_u32 s57, s57, 4096
buffer_load_dwordx4 a[48:51], v144, s[28:31], s57 offen offset:0 sc1
buffer_load_dwordx4 a[52:55], v144, s[28:31], s57 offen offset:1024 sc1
buffer_load_dwordx4 a[56:59], v144, s[28:31], s57 offen offset:2048 sc1
buffer_load_dwordx4 a[60:63], v144, s[28:31], s57 offen offset:3072 sc1
s_add_u32 s57, s57, 4096
buffer_load_dwordx4 a[64:67], v144, s[28:31], s57 offen offset:0 sc1
buffer_load_dwordx4 a[68:71], v144, s[28:31], s57 offen offset:1024 sc1
buffer_load_dwordx4 a[72:75], v144, s[28:31], s57 offen offset:2048 sc1
buffer_load_dwordx4 a[76:79], v144, s[28:31], s57 offen offset:3072 sc1
s_add_u32 s57, s57, 4096
buffer_load_dwordx4 a[80:83], v144, s[28:31], s57 offen offset:0 sc1
buffer_load_dwordx4 a[84:87], v144, s[28:31], s57 offen offset:1024 sc1
buffer_load_dwordx4 a[88:91], v144, s[28:31], s57 offen offset:2048 sc1
buffer_load_dwordx4 a[92:95], v144, s[28:31], s57 offen offset:3072 sc1
s_add_u32 s57, s57, 4096
buffer_load_dwordx4 a[96:99], v144, s[28:31], s57 offen offset:0 sc1
buffer_load_dwordx4 a[100:103], v144, s[28:31], s57 offen offset:1024 sc1
buffer_load_dwordx4 a[104:107], v144, s[28:31], s57 offen offset:2048 sc1
buffer_load_dwordx4 a[108:111], v144, s[28:31], s57 offen offset:3072 sc1
s_add_u32 s57, s57, 4096
buffer_load_dwordx4 a[112:115], v144, s[28:31], s57 offen offset:0 sc1
buffer_load_dwordx4 a[116:119], v144, s[28:31], s57 offen offset:1024 sc1
buffer_load_dwordx4 a[120:123], v144, s[28:31], s57 offen offset:2048 sc1
buffer_load_dwordx4 a[124:127], v144, s[28:31], s57 offen offset:3072 sc1
s_add_u32 s82, s37, 20480
s_add_u32 s83, s37, 32768
s_add_u32 s84, s37, 36864
s_add_u32 s85, s37, 49152
s_add_u32 s86, s37, 53248
s_and_b32 s42, s36, 1
s_lshl_b32 s42, s42, 13
v_add_u32 v149, s42, v144
s_waitcnt vmcnt(0)
s_cmp_eq_u32 s36, 0
s_cbranch_scc1 S2_TSZ
v_mov_b32 v152, 0
s_add_u32 s42, s37, 16384
S2_TS:
buffer_load_dword v151, v152, s[20:23], s42 offen offset:3072 sc1
s_waitcnt vmcnt(0)
v_readfirstlane_b32 s92, v151
s_nop 0
s_cmp_lg_u32 s92, -1
s_cbranch_scc1 S2_TSK
s_sleep 2
s_sub_u32 s41, s41, 1
s_cbranch_scc0 S2_TS
s_mov_b32 s41, 0
S2_TSK:
S2_TSZ:
v_mov_b32 v112, 0
v_mov_b32 v113, 0
v_mov_b32 v114, 0
v_mov_b32 v115, 0
s_waitcnt lgkmcnt(0)
S2_W:
s_memrealtime s[94:95]
s_waitcnt lgkmcnt(0)
s_sub_u32 s94, s92, s94
s_lshl_b32 s94, s94, 1
s_cmp_gt_i32 s94, 20000
s_cbranch_scc1 S2_WG
s_cmp_gt_i32 s94, 16
s_cbranch_scc1 S2_W
s_cmp_lt_i32 s94, 6
s_cbranch_scc1 S2_WG
s_nop 3
s_cmp_lt_i32 s94, 8
s_cbranch_scc1 S2_WG
s_nop 3
s_cmp_lt_i32 s94, 10
s_cbranch_scc1 S2_WG
s_nop 3
s_cmp_lt_i32 s94, 12
s_cbranch_scc1 S2_WG
s_nop 3
s_cmp_lt_i32 s94, 14
s_cbranch_scc1 S2_WG
s_nop 3
s_cmp_lt_i32 s94, 16
s_cbranch_scc1 S2_WG
s_nop 3
S2_WG:
buffer_load_dwordx4 v[0:3], v144, s[20:23], s82 offen offset:0 sc1
buffer_load_dwordx4 v[4:7], v144, s[20:23], s82 offen offset:1024 sc1
buffer_load_dwordx4 v[8:11], v144, s[20:23], s82 offen offset:2048 sc1
buffer_load_dwordx4 v[12:15], v144, s[20:23], s82 offen offset:3072 sc1
buffer_load_dwordx4 v[16:19], v144, s[20:23], s83 offen offset:0 sc1
buffer_load_dwordx4 v[20:23], v144, s[20:23], s83 offen offset:1024 sc1
buffer_load_dwordx4 v[24:27], v144, s[20:23], s83 offen offset:2048 sc1
buffer_load_dwordx4 v[28:31], v144, s[20:23], s83 offen offset:3072 sc1
buffer_load_dwordx4 v[32:35], v144, s[20:23], s84 offen offset:0 sc1
buffer_load_dwordx4 v[36:39], v144, s[20:23], s84 offen offset:1024 sc1
buffer_load_dwordx4 v[40:43], v144, s[20:23], s84 offen offset:2048 sc1
buffer_load_dwordx4 v[44:47], v144, s[20:23], s84 offen offset:3072 sc1
buffer_load_dwordx4 v[48:51], v144, s[20:23], s85 offen offset:0 sc1
buffer_load_dwordx4 v[52:55], v144, s[20:23], s85 offen offset:1024 sc1
buffer_load_dwordx4 v[56:59], v144, s[20:23], s85 offen offset:2048 sc1
buffer_load_dwordx4 v[60:63], v144, s[20:23], s85 offen offset:3072 sc1
buffer_load_dwordx4 v[64:67], v148, s[20:23], s86 offen offset:0 sc1
buffer_load_dwordx4 v[68:71], v148, s[20:23], s86 offen offset:1024 sc1
buffer_load_dwordx4 v[72:75], v148, s[20:23], s86 offen offset:2048 sc1
buffer_load_dwordx4 v[76:79], v148, s[20:23], s86 offen offset:3072 sc1
s_waitcnt vmcnt(19)
v_smfmac_f32_16x16x64_bf16 v[112:115], v[0:3], a[0:7], v145
s_waitcnt vmcnt(18)
v_smfmac_f32_16x16x64_bf16 v[112:115], v[4:7], a[8:15], v145
s_waitcnt vmcnt(17)
v_smfmac_f32_16x16x64_bf16 v[112:115], v[8:11], a[16:23], v145
s_waitcnt vmcnt(16)
v_smfmac_f32_16x16x64_bf16 v[112:115], v[12:15], a[24:31], v145
s_waitcnt vmcnt(15)
v_smfmac_f32_16x16x64_bf16 v[112:115], v[16:19], a[32:39], v145
s_waitcnt vmcnt(14)
v_smfmac_f32_16x16x64_bf16 v[112:115], v[20:23], a[40:47], v145
s_waitcnt vmcnt(13)
v_smfmac_f32_16x16x64_bf16 v[112:115], v[24:27], a[48:55], v145
s_waitcnt vmcnt(12)
v_smfmac_f32_16x16x64_bf16 v[112:115], v[28:31], a[56:63], v145
s_waitcnt vmcnt(11)
v_smfmac_f32_16x16x64_bf16 v[112:115], v[32:35], a[64:71], v145
s_waitcnt vmcnt(10)
v_smfmac_f32_16x16x64_bf16 v[112:115], v[36:39], a[72:79], v145
s_waitcnt vmcnt(9)
s_barrier
ds_read_b128 v[80:83], v149 offset:0
v_smfmac_f32_16x16x64_bf16 v[112:115], v[40:43], a[80:87], v145
s_waitcnt vmcnt(8)
ds_read_b128 v[84:87], v149 offset:1024
ds_read_b128 v[88:91], v149 offset:2048
v_smfmac_f32_16x16x64_bf16 v[112:115], v[44:47], a[88:95], v145
s_waitcnt vmcnt(7)
ds_read_b128 v[92:95], v149 offset:3072
ds_read_b128 v[96:99], v149 offset:4096
v_smfmac_f32_16x16x64_bf16 v[112:115], v[48:51], a[96:103], v145
s_waitcnt vmcnt(6)
ds_read_b128 v[100:103], v149 offset:5120
ds_read_b128 v[104:107], v149 offset:6144
v_smfmac_f32_16x16x64_bf16 v[112:115], v[52:55], a[104:111], v145
s_waitcnt vmcnt(5)
ds_read_b128 v[108:111], v149 offset:7168
v_smfmac_f32_16x16x64_bf16 v[112:115], v[56:59], a[112:119], v145
s_waitcnt vmcnt(4)
v_smfmac_f32_16x16x64_bf16 v[112:115], v[60:63], a[120:127], v145
s_waitcnt vmcnt(3) lgkmcnt(6)
v_smfmac_f32_16x16x64_bf16 v[112:115], v[64:67], v[80:87], v145
s_waitcnt vmcnt(2) lgkmcnt(4)
v_smfmac_f32_16x16x64_bf16 v[112:115], v[68:71], v[88:95], v145
s_waitcnt vmcnt(1) lgkmcnt(2)
v_smfmac_f32_16x16x64_bf16 v[112:115], v[72:75], v[96:103], v145
s_waitcnt vmcnt(0) lgkmcnt(0)
v_smfmac_f32_16x16x64_bf16 v[112:115], v[76:79], v[104:111], v145
s_nop 7
v_add_f32 v116, v112, v113
v_add_f32 v117, v114, v115
v_cndmask_b32 v152, v117, v116, s[78:79]
v_cndmask_b32 v151, v116, v117, s[78:79]
v_cmp_u_f32 vcc, v116, v117
v_mov_b32_dpp v152, v152 quad_perm:[1,0,3,2] row_mask:0xf bank_mask:0xf
v_cndmask_b32 v150, v151, v152, s[78:79]
v_cndmask_b32 v151, v152, v151, s[78:79]
v_cvt_pk_bf16_f32 v150, v150, v151
s_cbranch_vccnz S2_S
buffer_store_dword v150, v146, s[20:23], s38 offen offset:0 sc1
S2_N:
s_cmp_lg_u32 s54, 0
s_cbranch_scc1 S2_NM
s_memrealtime s[94:95]
s_waitcnt lgkmcnt(0)
s_add_u32 s94, s94, 4
s_sub_u32 s42, s93, s94
s_lshl_b32 s42, s42, 1
s_add_u32 s94, s94, 60
s_cmp_lt_i32 s42, 0
s_cselect_b32 s93, s94, s93
s_add_u32 s42, s93, 128
s_and_b32 s42, s42, 0x7fffffff
s_add_u32 s93, s93, 205
s_and_b32 s93, s93, 0x7fffffff
v_mov_b32 v116, s42
v_mov_b32 v117, s93
v_mov_b32 v151, 0
s_add_u32 s42, s38, 16384
buffer_store_dwordx2 v[116:117], v151, s[20:23], s42 offen offset:3072 sc1
S2_NM:
s_add_u32 s36, s36, 1
s_cmp_lt_u32 s36, 63
s_nop 7
s_cbranch_scc1 S2_T
s_branch S2_END
S2_S:
s_nop 7
buffer_load_dwordx4 v[0:3], v144, s[20:23], s82 offen offset:0 sc1
buffer_load_dwordx4 v[4:7], v144, s[20:23], s82 offen offset:1024 sc1
buffer_load_dwordx4 v[8:11], v144, s[20:23], s82 offen offset:2048 sc1
buffer_load_dwordx4 v[12:15], v144, s[20:23], s82 offen offset:3072 sc1
buffer_load_dwordx4 v[16:19], v144, s[20:23], s83 offen offset:0 sc1
buffer_load_dwordx4 v[20:23], v144, s[20:23], s83 offen offset:1024 sc1
buffer_load_dwordx4 v[24:27], v144, s[20:23], s83 offen offset:2048 sc1
buffer_load_dwordx4 v[28:31], v144, s[20:23], s83 offen offset:3072 sc1
buffer_load_dwordx4 v[32:35], v144, s[20:23], s84 offen offset:0 sc1
buffer_load_dwordx4 v[36:39], v144, s[20:23], s84 offen offset:1024 sc1
buffer_load_dwordx4 v[40:43], v144, s[20:23], s84 offen offset:2048 sc1
buffer_load_dwordx4 v[44:47], v144, s[20:23], s84 offen offset:3072 sc1
buffer_load_dwordx4 v[48:51], v144, s[20:23], s85 offen offset:0 sc1
buffer_load_dwordx4 v[52:55], v144, s[20:23], s85 offen offset:1024 sc1
buffer_load_dwordx4 v[56:59], v144, s[20:23], s85 offen offset:2048 sc1
buffer_load_dwordx4 v[60:63], v144, s[20:23], s85 offen offset:3072 sc1
buffer_load_dwordx4 v[64:67], v148, s[20:23], s86 offen offset:0 sc1
buffer_load_dwordx4 v[68:71], v148, s[20:23], s86 offen offset:1024 sc1
buffer_load_dwordx4 v[72:75], v148, s[20:23], s86 offen offset:2048 sc1
buffer_load_dwordx4 v[76:79], v148, s[20:23], s86 offen offset:3072 sc1
s_waitcnt vmcnt(19)
v_max3_u32 v150, v0, v1, v2
s_waitcnt vmcnt(18)
v_max3_u32 v150, v150, v3, v4
v_max3_u32 v150, v150, v5, v6
s_waitcnt vmcnt(17)
v_max3_u32 v150, v150, v7, v8
v_max3_u32 v150, v150, v9, v10
s_waitcnt vmcnt(16)
v_max3_u32 v150, v150, v11, v12
v_max3_u32 v150, v150, v13, v14
s_waitcnt vmcnt(15)
v_max3_u32 v150, v150, v15, v16
v_max3_u32 v150, v150, v17, v18
s_waitcnt vmcnt(14)
v_max3_u32 v150, v150, v19, v20
v_max3_u32 v150, v150, v21, v22
s_waitcnt vmcnt(13)
v_max3_u32 v150, v150, v23, v24
v_max3_u32 v150, v150, v25, v26
s_waitcnt vmcnt(12)
v_max3_u32 v150, v150, v27, v28
v_max3_u32 v150, v150, v29, v30
s_waitcnt vmcnt(11)
v_max3_u32 v150, v150, v31, v32
v_max3_u32 v150, v150, v33, v34
s_waitcnt vmcnt(10)
v_max3_u32 v150, v150, v35, v36
v_max3_u32 v150, v150, v37, v38
s_waitcnt vmcnt(9)
v_max3_u32 v150, v150, v39, v40
v_max3_u32 v150, v150, v41, v42
s_waitcnt vmcnt(8)
v_max3_u32 v150, v150, v43, v44
v_max3_u32 v150, v150, v45, v46
s_waitcnt vmcnt(7)
v_max3_u32 v150, v150, v47, v48
v_max3_u32 v150, v150, v49, v50
s_waitcnt vmcnt(6)
v_max3_u32 v150, v150, v51, v52
v_max3_u32 v150, v150, v53, v54
s_waitcnt vmcnt(5)
v_max3_u32 v150, v150, v55, v56
v_max3_u32 v150, v150, v57, v58
s_waitcnt vmcnt(4)
v_max3_u32 v150, v150, v59, v60
v_max3_u32 v150, v150, v61, v62
s_waitcnt vmcnt(3)
v_max3_u32 v150, v150, v63, v64
v_max3_u32 v150, v150, v65, v66
s_waitcnt vmcnt(2)
v_max3_u32 v150, v150, v67, v68
v_max3_u32 v150, v150, v69, v70
s_waitcnt vmcnt(1)
v_max3_u32 v150, v150, v71, v72
v_max3_u32 v150, v150, v73, v74
s_waitcnt vmcnt(0)
v_max3_u32 v150, v150, v75, v76
v_max3_u32 v150, v150, v77, v78
v_max_u32 v150, v150, v79
v_cmp_eq_u32 vcc, -1, v150
s_cbranch_vccz S2_SH
s_sub_u32 s41, s41, 1
s_cbranch_scc0 S2_S
s_mov_b32 s41, 0
S2_SH:
v_mov_b32 v112, 0
v_mov_b32 v113, 0
v_mov_b32 v114, 0
v_mov_b32 v115, 0
s_nop 1
v_smfmac_f32_16x16x64_bf16 v[112:115], v[0:3], a[0:7], v145
v_smfmac_f32_16x16x64_bf16 v[112:115], v[4:7], a[8:15], v145
v_smfmac_f32_16x16x64_bf16 v[112:115], v[8:11], a[16:23], v145
v_smfmac_f32_16x16x64_bf16 v[112:115], v[12:15], a[24:31], v145
v_smfmac_f32_16x16x64_bf16 v[112:115], v[16:19], a[32:39], v145
v_smfmac_f32_16x16x64_bf16 v[112:115], v[20:23], a[40:47], v145
v_smfmac_f32_16x16x64_bf16 v[112:115], v[24:27], a[48:55], v145
v_smfmac_f32_16x16x64_bf16 v[112:115], v[28:31], a[56:63], v145
v_smfmac_f32_16x16x64_bf16 v[112:115], v[32:35], a[64:71], v145
v_smfmac_f32_16x16x64_bf16 v[112:115], v[36:39], a[72:79], v145
v_smfmac_f32_16x16x64_bf16 v[112:115], v[40:43], a[80:87], v145
v_smfmac_f32_16x16x64_bf16 v[112:115], v[44:47], a[88:95], v145
v_smfmac_f32_16x16x64_bf16 v[112:115], v[48:51], a[96:103], v145
v_smfmac_f32_16x16x64_bf16 v[112:115], v[52:55], a[104:111], v145
v_smfmac_f32_16x16x64_bf16 v[112:115], v[56:59], a[112:119], v145
v_smfmac_f32_16x16x64_bf16 v[112:115], v[60:63], a[120:127], v145
v_smfmac_f32_16x16x64_bf16 v[112:115], v[64:67], v[80:87], v145
v_smfmac_f32_16x16x64_bf16 v[112:115], v[68:71], v[88:95], v145
v_smfmac_f32_16x16x64_bf16 v[112:115], v[72:75], v[96:103], v145
v_smfmac_f32_16x16x64_bf16 v[112:115], v[76:79], v[104:111], v145
s_nop 7
v_add_f32 v116, v112, v113
v_add_f32 v117, v114, v115
v_cndmask_b32 v152, v117, v116, s[78:79]
v_cndmask_b32 v151, v116, v117, s[78:79]
v_cmp_u_f32 vcc, v116, v117
v_mov_b32_dpp v152, v152 quad_perm:[1,0,3,2] row_mask:0xf bank_mask:0xf
v_cndmask_b32 v150, v151, v152, s[78:79]
v_cndmask_b32 v151, v152, v151, s[78:79]
v_cvt_pk_bf16_f32 v150, v150, v151
buffer_store_dword v150, v146, s[20:23], s38 offen offset:0 sc1
s_branch S2_N
S2_H:
s_add_u32 s42, s36, 0
s_lshl_b32 s37, s42, 16
s_lshr_b32 s43, s37, 8
s_xor_b32 s43, s43, s39
s_and_b32 s43, s43, 8192
s_or_b32 s37, s37, s43
s_add_u32 s42, s36, 0
s_lshl_b32 s57, s42, 20
s_lshr_b32 s43, s57, 8
s_xor_b32 s43, s43, s39
s_and_b32 s43, s43, 8192
s_or_b32 s57, s57, s43
s_lshl_b32 s42, s54, 14
s_add_u32 s57, s57, s42
s_add_u32 s58, s57, 4096
s_and_b32 s42, s36, 1
s_lshl_b32 s42, s42, 13
v_add_u32 v149, s42, v144
s_sleep 24
s_cmp_eq_u32 s36, 0
s_cbranch_scc1 S2_HSZ
v_mov_b32 v152, 0
s_add_u32 s42, s37, 16384
S2_HS:
buffer_load_dword v151, v152, s[20:23], s42 offen offset:3072 sc1
s_waitcnt vmcnt(0)
v_readfirstlane_b32 s92, v151
s_nop 0
s_cmp_lg_u32 s92, -1
s_cbranch_scc1 S2_HSK
s_sleep 2
s_sub_u32 s41, s41, 1
s_cbranch_scc0 S2_HS
s_mov_b32 s41, 0
S2_HSK:
s_add_u32 s92, s92, -10
S2_HSZ:
s_waitcnt lgkmcnt(0)
S2_HW:
s_memrealtime s[94:95]
s_waitcnt lgkmcnt(0)
s_sub_u32 s94, s92, s94
s_lshl_b32 s94, s94, 1
s_cmp_gt_i32 s94, 20000
s_cbranch_scc1 S2_HWG
s_cmp_gt_i32 s94, 16
s_cbranch_scc1 S2_HW
s_cmp_lt_i32 s94, 6
s_cbranch_scc1 S2_HWG
s_nop 3
s_cmp_lt_i32 s94, 8
s_cbranch_scc1 S2_HWG
s_nop 3
s_cmp_lt_i32 s94, 10
s_cbranch_scc1 S2_HWG
s_nop 3
s_cmp_lt_i32 s94, 12
s_cbranch_scc1 S2_HWG
s_nop 3
s_cmp_lt_i32 s94, 14
s_cbranch_scc1 S2_HWG
s_nop 3
s_cmp_lt_i32 s94, 16
s_cbranch_scc1 S2_HWG
s_nop 3
S2_HWG:
S2_U:
buffer_load_dwordx2 v[80:81], v147, s[24:27], s57 offen offset:0 sc1
buffer_load_dwordx2 v[82:83], v147, s[24:27], s57 offen offset:256 sc1
buffer_load_dwordx2 v[84:85], v147, s[24:27], s57 offen offset:1024 sc1
buffer_load_dwordx2 v[86:87], v147, s[24:27], s57 offen offset:1280 sc1
buffer_load_dwordx2 v[88:89], v147, s[24:27], s57 offen offset:2048 sc1
buffer_load_dwordx2 v[90:91], v147, s[24:27], s57 offen offset:2304 sc1
buffer_load_dwordx2 v[92:93], v147, s[24:27], s57 offen offset:3072 sc1
buffer_load_dwordx2 v[94:95], v147, s[24:27], s57 offen offset:3328 sc1
buffer_load_dwordx2 v[96:97], v147, s[24:27], s58 offen offset:0 sc1
buffer_load_dwordx2 v[98:99], v147, s[24:27], s58 offen offset:256 sc1
buffer_load_dwordx2 v[100:101], v147, s[24:27], s58 offen offset:1024 sc1
buffer_load_dwordx2 v[102:103], v147, s[24:27], s58 offen offset:1280 sc1
buffer_load_dwordx2 v[104:105], v147, s[24:27], s58 offen offset:2048 sc1
buffer_load_dwordx2 v[106:107], v147, s[24:27], s58 offen offset:2304 sc1
buffer_load_dwordx2 v[108:109], v147, s[24:27], s58 offen offset:3072 sc1
buffer_load_dwordx2 v[110:111], v147, s[24:27], s58 offen offset:3328 sc1
s_waitcnt vmcnt(14)
v_max3_u32 v150, v80, v81, v82
v_max_u32 v150, v150, v83
v_swap_b32 v81, v82
ds_write_b128 v149, v[80:83] offset:0
s_waitcnt vmcnt(13)
v_max3_u32 v150, v150, v84, v85
s_waitcnt vmcnt(12)
v_max3_u32 v150, v150, v86, v87
v_swap_b32 v85, v86
ds_write_b128 v149, v[84:87] offset:1024
s_waitcnt vmcnt(11)
v_max3_u32 v150, v150, v88, v89
s_waitcnt vmcnt(10)
v_max3_u32 v150, v150, v90, v91
v_swap_b32 v89, v90
ds_write_b128 v149, v[88:91] offset:2048
s_waitcnt vmcnt(9)
v_max3_u32 v150, v150, v92, v93
s_waitcnt vmcnt(8)
v_max3_u32 v150, v150, v94, v95
v_swap_b32 v93, v94
ds_write_b128 v149, v[92:95] offset:3072
s_waitcnt vmcnt(7)
v_max3_u32 v150, v150, v96, v97
s_waitcnt vmcnt(6)
v_max3_u32 v150, v150, v98, v99
v_swap_b32 v97, v98
ds_write_b128 v149, v[96:99] offset:4096
s_waitcnt vmcnt(5)
v_max3_u32 v150, v150, v100, v101
s_waitcnt vmcnt(4)
v_max3_u32 v150, v150, v102, v103
v_swap_b32 v101, v102
ds_write_b128 v149, v[100:103] offset:5120
s_waitcnt vmcnt(3)
v_max3_u32 v150, v150, v104, v105
s_waitcnt vmcnt(2)
v_max3_u32 v150, v150, v106, v107
v_swap_b32 v105, v106
ds_write_b128 v149, v[104:107] offset:6144
s_waitcnt vmcnt(1)
v_max3_u32 v150, v150, v108, v109
s_waitcnt vmcnt(0)
v_max3_u32 v150, v150, v110, v111
v_swap_b32 v109, v110
ds_write_b128 v149, v[108:111] offset:7168
v_cmp_eq_u32 vcc, -1, v150
s_cbranch_vccz S2_UH
s_waitcnt lgkmcnt(0)
s_sub_u32 s41, s41, 1
s_cbranch_scc0 S2_U
s_mov_b32 s41, 0
S2_UH:
s_waitcnt lgkmcnt(0)
s_barrier
s_add_u32 s36, s36, 1
s_cmp_lt_u32 s36, 63
s_cbranch_scc1 S2_H
S2_END:
s_endpgm
ROLE_PA:
s_mov_b64 s[48:49], %0
s_mov_b64 s[50:51], %1
s_mov_b64 s[52:53], %2
s_mov_b32 s54, %3
s_mov_b32 s40, %4
s_mov_b32 s39, %5
s_mov_b32 s41, 0x200000
s_mov_b32 s36, 0
s_lshl_b32 s55, s40, 10
v_mbcnt_lo_u32_b32 v31, -1, 0
v_mbcnt_hi_u32_b32 v31, -1, v31
s_lshl_b32 s42, s40, 8
s_add_u32 s42, s42, 147456
v_lshl_add_u32 v31, v31, 2, s42
ds_read_b32 v72, v31 offset:0
ds_read_b32 v73, v31 offset:1024
ds_read_b32 v74, v31 offset:2048
ds_read_b32 v75, v31 offset:3072
ds_read_b32 v76, v31 offset:4096
ds_read_b32 v77, v31 offset:5120
ds_read_b32 v78, v31 offset:6144
ds_read_b32 v84, v31 offset:7168
ds_read_b32 v85, v31 offset:8192
ds_read_b32 v86, v31 offset:9216
ds_read_b32 v87, v31 offset:10240
s_waitcnt lgkmcnt(0)
s_memrealtime s[94:95]
s_waitcnt lgkmcnt(0)
s_mov_b32 s92, s94
s_add_u32 s20, s48, 0x0
s_addc_u32 s21, s49, 0
s_and_b32 s21, s21, 0xffff
s_mov_b32 s22, 0x400000
s_mov_b32 s23, 0x20000
s_add_u32 s96, s48, 0x0
s_addc_u32 s97, s49, 0
s_and_b32 s97, s97, 0xffff
s_mov_b32 s98, 0xfe00000
s_mov_b32 s99, 0x20000
s_mov_b32 s44, 0x30000
s_mov_b32 s45, 0
s_add_u32 m0, s55, 0
s_nop 0
global_load_lds_dwordx4 v[84:85], off
v_lshl_add_u64 v[84:85], v[84:85], 0, s[44:45]
s_add_u32 m0, s55, 4096
s_nop 0
global_load_lds_dwordx4 v[84:85], off
v_lshl_add_u64 v[84:85], v[84:85], 0, s[44:45]
s_add_u32 m0, s55, 8192
s_nop 0
global_load_lds_dwordx4 v[84:85], off
v_lshl_add_u64 v[84:85], v[84:85], 0, s[44:45]
s_add_u32 m0, s55, 12288
s_nop 0
global_load_lds_dwordx4 v[84:85], off
v_lshl_add_u64 v[84:85], v[84:85], 0, s[44:45]
s_add_u32 m0, s55, 16384
s_nop 0
global_load_lds_dwordx4 v[86:87], off
v_lshl_add_u64 v[86:87], v[86:87], 0, s[44:45]
s_add_u32 m0, s55, 20480
s_nop 0
global_load_lds_dwordx4 v[86:87], off
v_lshl_add_u64 v[86:87], v[86:87], 0, s[44:45]
s_add_u32 m0, s55, 24576
s_nop 0
global_load_lds_dwordx4 v[86:87], off
v_lshl_add_u64 v[86:87], v[86:87], 0, s[44:45]
s_add_u32 m0, s55, 28672
s_nop 0
global_load_lds_dwordx4 v[86:87], off
v_lshl_add_u64 v[86:87], v[86:87], 0, s[44:45]
s_add_u32 m0, s55, 32768
s_nop 0
global_load_lds_dwordx4 v[84:85], off
v_lshl_add_u64 v[84:85], v[84:85], 0, s[44:45]
s_add_u32 m0, s55, 36864
s_nop 0
global_load_lds_dwordx4 v[84:85], off
v_lshl_add_u64 v[84:85], v[84:85], 0, s[44:45]
s_add_u32 m0, s55, 40960
s_nop 0
global_load_lds_dwordx4 v[84:85], off
v_lshl_add_u64 v[84:85], v[84:85], 0, s[44:45]
s_add_u32 m0, s55, 45056
s_nop 0
global_load_lds_dwordx4 v[84:85], off
v_lshl_add_u64 v[84:85], v[84:85], 0, s[44:45]
s_add_u32 m0, s55, 49152
s_nop 0
global_load_lds_dwordx4 v[86:87], off
v_lshl_add_u64 v[86:87], v[86:87], 0, s[44:45]
s_add_u32 m0, s55, 53248
s_nop 0
global_load_lds_dwordx4 v[86:87], off
v_lshl_add_u64 v[86:87], v[86:87], 0, s[44:45]
s_add_u32 m0, s55, 57344
s_nop 0
global_load_lds_dwordx4 v[86:87], off
v_lshl_add_u64 v[86:87], v[86:87], 0, s[44:45]
s_add_u32 m0, s55, 61440
s_nop 0
global_load_lds_dwordx4 v[86:87], off
v_lshl_add_u64 v[86:87], v[86:87], 0, s[44:45]
s_add_u32 m0, s55, 65536
s_nop 0
global_load_lds_dwordx4 v[84:85], off
v_lshl_add_u64 v[84:85], v[84:85], 0, s[44:45]
s_add_u32 m0, s55, 69632
s_nop 0
global_load_lds_dwordx4 v[84:85], off
v_lshl_add_u64 v[84:85], v[84:85], 0, s[44:45]
s_add_u32 m0, s55, 73728
s_nop 0
global_load_lds_dwordx4 v[84:85], off
v_lshl_add_u64 v[84:85], v[84:85], 0, s[44:45]
s_add_u32 m0, s55, 77824
s_nop 0
global_load_lds_dwordx4 v[84:85], off
v_lshl_add_u64 v[84:85], v[84:85], 0, s[44:45]
s_add_u32 m0, s55, 81920
s_nop 0
global_load_lds_dwordx4 v[86:87], off
v_lshl_add_u64 v[86:87], v[86:87], 0, s[44:45]
s_add_u32 m0, s55, 86016
s_nop 0
global_load_lds_dwordx4 v[86:87], off
v_lshl_add_u64 v[86:87], v[86:87], 0, s[44:45]
s_add_u32 m0, s55, 90112
s_nop 0
global_load_lds_dwordx4 v[86:87], off
v_lshl_add_u64 v[86:87], v[86:87], 0, s[44:45]
s_add_u32 m0, s55, 94208
s_nop 0
global_load_lds_dwordx4 v[86:87], off
v_lshl_add_u64 v[86:87], v[86:87], 0, s[44:45]
s_add_u32 m0, s55, 98304
s_nop 0
global_load_lds_dwordx4 v[84:85], off
v_lshl_add_u64 v[84:85], v[84:85], 0, s[44:45]
s_add_u32 m0, s55, 102400
s_nop 0
global_load_lds_dwordx4 v[84:85], off
v_lshl_add_u64 v[84:85], v[84:85], 0, s[44:45]
s_add_u32 m0, s55, 106496
s_nop 0
global_load_lds_dwordx4 v[84:85], off
v_lshl_add_u64 v[84:85], v[84:85], 0, s[44:45]
s_add_u32 m0, s55, 110592
s_nop 0
global_load_lds_dwordx4 v[84:85], off
v_lshl_add_u64 v[84:85], v[84:85], 0, s[44:45]
s_add_u32 m0, s55, 114688
s_nop 0
global_load_lds_dwordx4 v[86:87], off
v_lshl_add_u64 v[86:87], v[86:87], 0, s[44:45]
s_add_u32 m0, s55, 118784
s_nop 0
global_load_lds_dwordx4 v[86:87], off
v_lshl_add_u64 v[86:87], v[86:87], 0, s[44:45]
s_add_u32 m0, s55, 122880
s_nop 0
global_load_lds_dwordx4 v[86:87], off
v_lshl_add_u64 v[86:87], v[86:87], 0, s[44:45]
s_add_u32 m0, s55, 126976
s_nop 0
global_load_lds_dwordx4 v[86:87], off
v_lshl_add_u64 v[86:87], v[86:87], 0, s[44:45]
s_waitcnt vmcnt(28)
s_waitcnt lgkmcnt(0)
s_barrier
s_add_u32 m0, s55, 131072
s_nop 0
global_load_lds_dwordx4 v[84:85], off
v_lshl_add_u64 v[84:85], v[84:85], 0, s[44:45]
s_add_u32 m0, s55, 135168
s_nop 0
global_load_lds_dwordx4 v[84:85], off
v_lshl_add_u64 v[84:85], v[84:85], 0, s[44:45]
s_add_u32 m0, s55, 139264
s_nop 0
global_load_lds_dwordx4 v[84:85], off
v_lshl_add_u64 v[84:85], v[84:85], 0, s[44:45]
s_add_u32 m0, s55, 143360
s_nop 0
global_load_lds_dwordx4 v[84:85], off
v_lshl_add_u64 v[84:85], v[84:85], 0, s[44:45]
v_add_u32 v81, 0, v73
ds_read_b64_tr_b16 a[0:1], v81 offset:0
ds_read_b64_tr_b16 a[2:3], v81 offset:1024
ds_read_b64_tr_b16 a[4:5], v81 offset:4096
ds_read_b64_tr_b16 a[6:7], v81 offset:5120
ds_read_b64_tr_b16 a[8:9], v81 offset:8192
ds_read_b64_tr_b16 a[10:11], v81 offset:9216
ds_read_b64_tr_b16 a[12:13], v81 offset:12288
ds_read_b64_tr_b16 a[14:15], v81 offset:13312
s_waitcnt vmcnt(28)
s_waitcnt lgkmcnt(0)
s_barrier
s_add_u32 m0, s55, 0
s_nop 0
global_load_lds_dwordx4 v[86:87], off
v_lshl_add_u64 v[86:87], v[86:87], 0, s[44:45]
s_add_u32 m0, s55, 4096
s_nop 0
global_load_lds_dwordx4 v[86:87], off
v_lshl_add_u64 v[86:87], v[86:87], 0, s[44:45]
s_add_u32 m0, s55, 8192
s_nop 0
global_load_lds_dwordx4 v[86:87], off
v_lshl_add_u64 v[86:87], v[86:87], 0, s[44:45]
s_add_u32 m0, s55, 12288
s_nop 0
global_load_lds_dwordx4 v[86:87], off
v_lshl_add_u64 v[86:87], v[86:87], 0, s[44:45]
v_add_u32 v81, 16384, v73
ds_read_b64_tr_b16 a[160:161], v81 offset:0
ds_read_b64_tr_b16 a[162:163], v81 offset:1024
ds_read_b64_tr_b16 a[164:165], v81 offset:4096
ds_read_b64_tr_b16 a[166:167], v81 offset:5120
ds_read_b64_tr_b16 a[168:169], v81 offset:8192
ds_read_b64_tr_b16 a[170:171], v81 offset:9216
ds_read_b64_tr_b16 a[172:173], v81 offset:12288
ds_read_b64_tr_b16 a[174:175], v81 offset:13312
s_waitcnt vmcnt(28)
s_waitcnt lgkmcnt(0)
s_barrier
s_add_u32 m0, s55, 16384
s_nop 0
global_load_lds_dwordx4 v[84:85], off
v_lshl_add_u64 v[84:85], v[84:85], 0, s[44:45]
s_add_u32 m0, s55, 20480
s_nop 0
global_load_lds_dwordx4 v[84:85], off
v_lshl_add_u64 v[84:85], v[84:85], 0, s[44:45]
s_add_u32 m0, s55, 24576
s_nop 0
global_load_lds_dwordx4 v[84:85], off
v_lshl_add_u64 v[84:85], v[84:85], 0, s[44:45]
s_add_u32 m0, s55, 28672
s_nop 0
global_load_lds_dwordx4 v[84:85], off
v_lshl_add_u64 v[84:85], v[84:85], 0, s[44:45]
v_add_u32 v81, 32768, v73
ds_read_b64_tr_b16 a[16:17], v81 offset:0
ds_read_b64_tr_b16 a[18:19], v81 offset:1024
ds_read_b64_tr_b16 a[20:21], v81 offset:4096
ds_read_b64_tr_b16 a[22:23], v81 offset:5120
ds_read_b64_tr_b16 a[24:25], v81 offset:8192
ds_read_b64_tr_b16 a[26:27], v81 offset:9216
ds_read_b64_tr_b16 a[28:29], v81 offset:12288
ds_read_b64_tr_b16 a[30:31], v81 offset:13312
s_waitcnt vmcnt(28)
s_waitcnt lgkmcnt(0)
s_barrier
s_add_u32 m0, s55, 32768
s_nop 0
global_load_lds_dwordx4 v[86:87], off
v_lshl_add_u64 v[86:87], v[86:87], 0, s[44:45]
s_add_u32 m0, s55, 36864
s_nop 0
global_load_lds_dwordx4 v[86:87], off
v_lshl_add_u64 v[86:87], v[86:87], 0, s[44:45]
s_add_u32 m0, s55, 40960
s_nop 0
global_load_lds_dwordx4 v[86:87], off
v_lshl_add_u64 v[86:87], v[86:87], 0, s[44:45]
s_add_u32 m0, s55, 45056
s_nop 0
global_load_lds_dwordx4 v[86:87], off
v_lshl_add_u64 v[86:87], v[86:87], 0, s[44:45]
v_add_u32 v81, 49152, v73
ds_read_b64_tr_b16 a[176:177], v81 offset:0
ds_read_b64_tr_b16 a[178:179], v81 offset:1024
ds_read_b64_tr_b16 a[180:181], v81 offset:4096
ds_read_b64_tr_b16 a[182:183], v81 offset:5120
ds_read_b64_tr_b16 a[184:185], v81 offset:8192
ds_read_b64_tr_b16 a[186:187], v81 offset:9216
ds_read_b64_tr_b16 a[188:189], v81 offset:12288
ds_read_b64_tr_b16 a[190:191], v81 offset:13312
s_waitcnt vmcnt(28)
s_waitcnt lgkmcnt(0)
s_barrier
s_add_u32 m0, s55, 49152
s_nop 0
global_load_lds_dwordx4 v[84:85], off
v_lshl_add_u64 v[84:85], v[84:85], 0, s[44:45]
s_add_u32 m0, s55, 53248
s_nop 0
global_load_lds_dwordx4 v[84:85], off
v_lshl_add_u64 v[84:85], v[84:85], 0, s[44:45]
s_add_u32 m0, s55, 57344
s_nop 0
global_load_lds_dwordx4 v[84:85], off
v_lshl_add_u64 v[84:85], v[84:85], 0, s[44:45]
s_add_u32 m0, s55, 61440
s_nop 0
global_load_lds_dwordx4 v[84:85], off
v_lshl_add_u64 v[84:85], v[84:85], 0, s[44:45]
v_add_u32 v81, 65536, v73
ds_read_b64_tr_b16 a[32:33], v81 offset:0
ds_read_b64_tr_b16 a[34:35], v81 offset:1024
ds_read_b64_tr_b16 a[36:37], v81 offset:4096
ds_read_b64_tr_b16 a[38:39], v81 offset:5120
ds_read_b64_tr_b16 a[40:41], v81 offset:8192
ds_read_b64_tr_b16 a[42:43], v81 offset:9216
ds_read_b64_tr_b16 a[44:45], v81 offset:12288
ds_read_b64_tr_b16 a[46:47], v81 offset:13312
s_waitcnt vmcnt(28)
s_waitcnt lgkmcnt(0)
s_barrier
s_add_u32 m0, s55, 65536
s_nop 0
global_load_lds_dwordx4 v[86:87], off
v_lshl_add_u64 v[86:87], v[86:87], 0, s[44:45]
s_add_u32 m0, s55, 69632
s_nop 0
global_load_lds_dwordx4 v[86:87], off
v_lshl_add_u64 v[86:87], v[86:87], 0, s[44:45]
s_add_u32 m0, s55, 73728
s_nop 0
global_load_lds_dwordx4 v[86:87], off
v_lshl_add_u64 v[86:87], v[86:87], 0, s[44:45]
s_add_u32 m0, s55, 77824
s_nop 0
global_load_lds_dwordx4 v[86:87], off
v_lshl_add_u64 v[86:87], v[86:87], 0, s[44:45]
v_add_u32 v81, 81920, v73
ds_read_b64_tr_b16 a[192:193], v81 offset:0
ds_read_b64_tr_b16 a[194:195], v81 offset:1024
ds_read_b64_tr_b16 a[196:197], v81 offset:4096
ds_read_b64_tr_b16 a[198:199], v81 offset:5120
ds_read_b64_tr_b16 a[200:201], v81 offset:8192
ds_read_b64_tr_b16 a[202:203], v81 offset:9216
ds_read_b64_tr_b16 a[204:205], v81 offset:12288
ds_read_b64_tr_b16 a[206:207], v81 offset:13312
s_waitcnt vmcnt(28)
s_waitcnt lgkmcnt(0)
s_barrier
s_add_u32 m0, s55, 81920
s_nop 0
global_load_lds_dwordx4 v[84:85], off
v_lshl_add_u64 v[84:85], v[84:85], 0, s[44:45]
s_add_u32 m0, s55, 86016
s_nop 0
global_load_lds_dwordx4 v[84:85], off
v_lshl_add_u64 v[84:85], v[84:85], 0, s[44:45]
s_add_u32 m0, s55, 90112
s_nop 0
global_load_lds_dwordx4 v[84:85], off
v_lshl_add_u64 v[84:85], v[84:85], 0, s[44:45]
s_add_u32 m0, s55, 94208
s_nop 0
global_load_lds_dwordx4 v[84:85], off
v_lshl_add_u64 v[84:85], v[84:85], 0, s[44:45]
v_add_u32 v81, 98304, v73
ds_read_b64_tr_b16 a[48:49], v81 offset:0
ds_read_b64_tr_b16 a[50:51], v81 offset:1024
ds_read_b64_tr_b16 a[52:53], v81 offset:4096
ds_read_b64_tr_b16 a[54:55], v81 offset:5120
ds_read_b64_tr_b16 a[56:57], v81 offset:8192
ds_read_b64_tr_b16 a[58:59], v81 offset:9216
ds_read_b64_tr_b16 a[60:61], v81 offset:12288
ds_read_b64_tr_b16 a[62:63], v81 offset:13312
s_waitcnt vmcnt(28)
s_waitcnt lgkmcnt(0)
s_barrier
s_add_u32 m0, s55, 98304
s_nop 0
global_load_lds_dwordx4 v[86:87], off
v_lshl_add_u64 v[86:87], v[86:87], 0, s[44:45]
s_add_u32 m0, s55, 102400
s_nop 0
global_load_lds_dwordx4 v[86:87], off
v_lshl_add_u64 v[86:87], v[86:87], 0, s[44:45]
s_add_u32 m0, s55, 106496
s_nop 0
global_load_lds_dwordx4 v[86:87], off
v_lshl_add_u64 v[86:87], v[86:87], 0, s[44:45]
s_add_u32 m0, s55, 110592
s_nop 0
global_load_lds_dwordx4 v[86:87], off
v_lshl_add_u64 v[86:87], v[86:87], 0, s[44:45]
v_add_u32 v81, 114688, v73
ds_read_b64_tr_b16 a[208:209], v81 offset:0
ds_read_b64_tr_b16 a[210:211], v81 offset:1024
ds_read_b64_tr_b16 a[212:213], v81 offset:4096
ds_read_b64_tr_b16 a[214:215], v81 offset:5120
ds_read_b64_tr_b16 a[216:217], v81 offset:8192
ds_read_b64_tr_b16 a[218:219], v81 offset:9216
ds_read_b64_tr_b16 a[220:221], v81 offset:12288
ds_read_b64_tr_b16 a[222:223], v81 offset:13312
s_waitcnt vmcnt(28)
s_waitcnt lgkmcnt(0)
s_barrier
s_add_u32 m0, s55, 114688
s_nop 0
global_load_lds_dwordx4 v[84:85], off
v_lshl_add_u64 v[84:85], v[84:85], 0, s[44:45]
s_add_u32 m0, s55, 118784
s_nop 0
global_load_lds_dwordx4 v[84:85], off
v_lshl_add_u64 v[84:85], v[84:85], 0, s[44:45]
s_add_u32 m0, s55, 122880
s_nop 0
global_load_lds_dwordx4 v[84:85], off
v_lshl_add_u64 v[84:85], v[84:85], 0, s[44:45]
s_add_u32 m0, s55, 126976
s_nop 0
global_load_lds_dwordx4 v[84:85], off
v_lshl_add_u64 v[84:85], v[84:85], 0, s[44:45]
v_add_u32 v81, 131072, v73
ds_read_b64_tr_b16 a[64:65], v81 offset:0
ds_read_b64_tr_b16 a[66:67], v81 offset:1024
ds_read_b64_tr_b16 a[68:69], v81 offset:4096
ds_read_b64_tr_b16 a[70:71], v81 offset:5120
ds_read_b64_tr_b16 a[72:73], v81 offset:8192
ds_read_b64_tr_b16 a[74:75], v81 offset:9216
ds_read_b64_tr_b16 a[76:77], v81 offset:12288
ds_read_b64_tr_b16 a[78:79], v81 offset:13312
s_waitcnt vmcnt(28)
s_waitcnt lgkmcnt(0)
s_barrier
s_add_u32 m0, s55, 131072
s_nop 0
global_load_lds_dwordx4 v[86:87], off
v_lshl_add_u64 v[86:87], v[86:87], 0, s[44:45]
s_add_u32 m0, s55, 135168
s_nop 0
global_load_lds_dwordx4 v[86:87], off
v_lshl_add_u64 v[86:87], v[86:87], 0, s[44:45]
s_add_u32 m0, s55, 139264
s_nop 0
global_load_lds_dwordx4 v[86:87], off
v_lshl_add_u64 v[86:87], v[86:87], 0, s[44:45]
s_add_u32 m0, s55, 143360
s_nop 0
global_load_lds_dwordx4 v[86:87], off
v_lshl_add_u64 v[86:87], v[86:87], 0, s[44:45]
v_add_u32 v81, 0, v73
ds_read_b64_tr_b16 a[224:225], v81 offset:0
ds_read_b64_tr_b16 a[226:227], v81 offset:1024
ds_read_b64_tr_b16 a[228:229], v81 offset:4096
ds_read_b64_tr_b16 a[230:231], v81 offset:5120
ds_read_b64_tr_b16 a[232:233], v81 offset:8192
ds_read_b64_tr_b16 a[234:235], v81 offset:9216
ds_read_b64_tr_b16 a[236:237], v81 offset:12288
ds_read_b64_tr_b16 a[238:239], v81 offset:13312
s_waitcnt vmcnt(28)
s_waitcnt lgkmcnt(0)
s_barrier
s_add_u32 m0, s55, 0
s_nop 0
global_load_lds_dwordx4 v[84:85], off
v_lshl_add_u64 v[84:85], v[84:85], 0, s[44:45]
s_add_u32 m0, s55, 4096
s_nop 0
global_load_lds_dwordx4 v[84:85], off
v_lshl_add_u64 v[84:85], v[84:85], 0, s[44:45]
s_add_u32 m0, s55, 8192
s_nop 0
global_load_lds_dwordx4 v[84:85], off
v_lshl_add_u64 v[84:85], v[84:85], 0, s[44:45]
s_add_u32 m0, s55, 12288
s_nop 0
global_load_lds_dwordx4 v[84:85], off
v_lshl_add_u64 v[84:85], v[84:85], 0, s[44:45]
v_add_u32 v81, 16384, v73
ds_read_b64_tr_b16 a[80:81], v81 offset:0
ds_read_b64_tr_b16 a[82:83], v81 offset:1024
ds_read_b64_tr_b16 a[84:85], v81 offset:4096
ds_read_b64_tr_b16 a[86:87], v81 offset:5120
ds_read_b64_tr_b16 a[88:89], v81 offset:8192
ds_read_b64_tr_b16 a[90:91], v81 offset:9216
ds_read_b64_tr_b16 a[92:93], v81 offset:12288
ds_read_b64_tr_b16 a[94:95], v81 offset:13312
s_waitcnt vmcnt(28)
s_waitcnt lgkmcnt(0)
s_barrier
s_add_u32 m0, s55, 16384
s_nop 0
global_load_lds_dwordx4 v[86:87], off
v_lshl_add_u64 v[86:87], v[86:87], 0, s[44:45]
s_add_u32 m0, s55, 20480
s_nop 0
global_load_lds_dwordx4 v[86:87], off
v_lshl_add_u64 v[86:87], v[86:87], 0, s[44:45]
s_add_u32 m0, s55, 24576
s_nop 0
global_load_lds_dwordx4 v[86:87], off
v_lshl_add_u64 v[86:87], v[86:87], 0, s[44:45]
s_add_u32 m0, s55, 28672
s_nop 0
global_load_lds_dwordx4 v[86:87], off
v_lshl_add_u64 v[86:87], v[86:87], 0, s[44:45]
v_add_u32 v81, 32768, v73
ds_read_b64_tr_b16 a[240:241], v81 offset:0
ds_read_b64_tr_b16 a[242:243], v81 offset:1024
ds_read_b64_tr_b16 a[244:245], v81 offset:4096
ds_read_b64_tr_b16 a[246:247], v81 offset:5120
ds_read_b64_tr_b16 a[248:249], v81 offset:8192
ds_read_b64_tr_b16 a[250:251], v81 offset:9216
ds_read_b64_tr_b16 a[252:253], v81 offset:12288
ds_read_b64_tr_b16 a[254:255], v81 offset:13312
s_waitcnt vmcnt(28)
s_waitcnt lgkmcnt(0)
s_barrier
v_add_u32 v81, 49152, v73
ds_read_b64_tr_b16 a[96:97], v81 offset:0
ds_read_b64_tr_b16 a[98:99], v81 offset:1024
ds_read_b64_tr_b16 a[100:101], v81 offset:4096
ds_read_b64_tr_b16 a[102:103], v81 offset:5120
ds_read_b64_tr_b16 a[104:105], v81 offset:8192
ds_read_b64_tr_b16 a[106:107], v81 offset:9216
ds_read_b64_tr_b16 a[108:109], v81 offset:12288
ds_read_b64_tr_b16 a[110:111], v81 offset:13312
s_waitcnt vmcnt(24)
s_waitcnt lgkmcnt(0)
s_barrier
v_add_u32 v81, 65536, v73
ds_read_b64_tr_b16 v[192:193], v81 offset:0
ds_read_b64_tr_b16 v[194:195], v81 offset:1024
ds_read_b64_tr_b16 v[196:197], v81 offset:4096
ds_read_b64_tr_b16 v[198:199], v81 offset:5120
ds_read_b64_tr_b16 v[200:201], v81 offset:8192
ds_read_b64_tr_b16 v[202:203], v81 offset:9216
ds_read_b64_tr_b16 v[204:205], v81 offset:12288
ds_read_b64_tr_b16 v[206:207], v81 offset:13312
s_waitcnt vmcnt(20)
s_waitcnt lgkmcnt(0)
s_barrier
v_add_u32 v81, 81920, v73
ds_read_b64_tr_b16 a[112:113], v81 offset:0
ds_read_b64_tr_b16 a[114:115], v81 offset:1024
ds_read_b64_tr_b16 a[116:117], v81 offset:4096
ds_read_b64_tr_b16 a[118:119], v81 offset:5120
ds_read_b64_tr_b16 a[120:121], v81 offset:8192
ds_read_b64_tr_b16 a[122:123], v81 offset:9216
ds_read_b64_tr_b16 a[124:125], v81 offset:12288
ds_read_b64_tr_b16 a[126:127], v81 offset:13312
s_waitcnt vmcnt(16)
s_waitcnt lgkmcnt(0)
s_barrier
v_add_u32 v81, 98304, v73
ds_read_b64_tr_b16 v[208:209], v81 offset:0
ds_read_b64_tr_b16 v[210:211], v81 offset:1024
ds_read_b64_tr_b16 v[212:213], v81 offset:4096
ds_read_b64_tr_b16 v[214:215], v81 offset:5120
ds_read_b64_tr_b16 v[216:217], v81 offset:8192
ds_read_b64_tr_b16 v[218:219], v81 offset:9216
ds_read_b64_tr_b16 v[220:221], v81 offset:12288
ds_read_b64_tr_b16 v[222:223], v81 offset:13312
s_waitcnt vmcnt(12)
s_waitcnt lgkmcnt(0)
s_barrier
v_add_u32 v81, 114688, v73
ds_read_b64_tr_b16 a[128:129], v81 offset:0
ds_read_b64_tr_b16 a[130:131], v81 offset:1024
ds_read_b64_tr_b16 a[132:133], v81 offset:4096
ds_read_b64_tr_b16 a[134:135], v81 offset:5120
ds_read_b64_tr_b16 a[136:137], v81 offset:8192
ds_read_b64_tr_b16 a[138:139], v81 offset:9216
ds_read_b64_tr_b16 a[140:141], v81 offset:12288
ds_read_b64_tr_b16 a[142:143], v81 offset:13312
s_waitcnt vmcnt(8)
s_waitcnt lgkmcnt(0)
s_barrier
v_add_u32 v81, 131072, v73
ds_read_b64_tr_b16 v[224:225], v81 offset:0
ds_read_b64_tr_b16 v[226:227], v81 offset:1024
ds_read_b64_tr_b16 v[228:229], v81 offset:4096
ds_read_b64_tr_b16 v[230:231], v81 offset:5120
ds_read_b64_tr_b16 v[232:233], v81 offset:8192
ds_read_b64_tr_b16 v[234:235], v81 offset:9216
ds_read_b64_tr_b16 v[236:237], v81 offset:12288
ds_read_b64_tr_b16 v[238:239], v81 offset:13312
s_waitcnt vmcnt(4)
s_waitcnt lgkmcnt(0)
s_barrier
v_add_u32 v81, 0, v73
ds_read_b64_tr_b16 a[144:145], v81 offset:0
ds_read_b64_tr_b16 a[146:147], v81 offset:1024
ds_read_b64_tr_b16 a[148:149], v81 offset:4096
ds_read_b64_tr_b16 a[150:151], v81 offset:5120
ds_read_b64_tr_b16 a[152:153], v81 offset:8192
ds_read_b64_tr_b16 a[154:155], v81 offset:9216
ds_read_b64_tr_b16 a[156:157], v81 offset:12288
ds_read_b64_tr_b16 a[158:159], v81 offset:13312
s_waitcnt vmcnt(0)
s_waitcnt lgkmcnt(0)
s_barrier
v_add_u32 v81, 16384, v73
ds_read_b64_tr_b16 v[240:241], v81 offset:0
ds_read_b64_tr_b16 v[242:243], v81 offset:1024
ds_read_b64_tr_b16 v[244:245], v81 offset:4096
ds_read_b64_tr_b16 v[246:247], v81 offset:5120
ds_read_b64_tr_b16 v[248:249], v81 offset:8192
ds_read_b64_tr_b16 v[250:251], v81 offset:9216
ds_read_b64_tr_b16 v[252:253], v81 offset:12288
ds_read_b64_tr_b16 v[254:255], v81 offset:13312
s_waitcnt lgkmcnt(0)
s_barrier
s_mul_i32 s58, s40, 10240
s_add_u32 s50, s48, 5242880
s_addc_u32 s51, s49, 0
s_add_u32 s50, s50, s58
s_addc_u32 s51, s51, 0
s_add_u32 s42, s36, 0
s_and_b32 s43, s42, 1
s_min_u32 s42, s42, 63
s_mul_i32 s43, s43, 40960
s_add_u32 s57, s43, s58
s_mul_i32 s42, s42, 40960
s_add_u32 s46, s50, s42
s_addc_u32 s47, s51, 0
s_add_u32 m0, s57, 0
s_nop 0
global_load_lds_dwordx4 v72, s[46:47]
s_add_u32 s46, s46, 1024
s_addc_u32 s47, s47, 0
s_add_u32 m0, s57, 1024
s_nop 0
global_load_lds_dwordx4 v72, s[46:47]
s_add_u32 s46, s46, 1024
s_addc_u32 s47, s47, 0
s_add_u32 m0, s57, 2048
s_nop 0
global_load_lds_dwordx4 v72, s[46:47]
s_add_u32 s46, s46, 1024
s_addc_u32 s47, s47, 0
s_add_u32 m0, s57, 3072
s_nop 0
global_load_lds_dwordx4 v72, s[46:47]
s_add_u32 s46, s46, 1024
s_addc_u32 s47, s47, 0
s_add_u32 m0, s57, 4096
s_nop 0
global_load_lds_dwordx4 v72, s[46:47]
s_add_u32 s46, s46, 1024
s_addc_u32 s47, s47, 0
s_add_u32 m0, s57, 5120
s_nop 0
global_load_lds_dwordx4 v72, s[46:47]
s_add_u32 s46, s46, 1024
s_addc_u32 s47, s47, 0
s_add_u32 m0, s57, 6144
s_nop 0
global_load_lds_dwordx4 v72, s[46:47]
s_add_u32 s46, s46, 1024
s_addc_u32 s47, s47, 0
s_add_u32 m0, s57, 7168
s_nop 0
global_load_lds_dwordx4 v72, s[46:47]
s_add_u32 s46, s46, 1024
s_addc_u32 s47, s47, 0
s_add_u32 m0, s57, 8192
s_nop 0
global_load_lds_dwordx4 v72, s[46:47]
s_add_u32 s46, s46, 1024
s_addc_u32 s47, s47, 0
s_add_u32 m0, s57, 9216
s_nop 0
global_load_lds_dwordx4 v72, s[46:47]
s_waitcnt vmcnt(0)
PA_T:
s_and_b32 s42, s36, 1
s_mul_i32 s42, s42, 40960
v_add_u32 v79, s42, v72
s_lshl_b32 s37, s36, 5
s_add_u32 s37, s37, s54
s_mul_i32 s37, s37, 12288
s_waitcnt lgkmcnt(0)
s_barrier
ds_read_b128 v[32:35], v79 offset:0
ds_read_b128 v[36:39], v79 offset:1024
ds_read_b128 v[40:43], v79 offset:2048
ds_read_b128 v[44:47], v79 offset:3072
ds_read_b128 v[48:51], v79 offset:4096
s_waitcnt lgkmcnt(4)
v_mfma_f32_32x32x16_bf16 v[0:15], a[0:3], v[32:35], 0
s_add_u32 s42, s36, 1
s_and_b32 s43, s42, 1
s_min_u32 s42, s42, 63
v_mfma_f32_32x32x16_bf16 v[16:31], a[160:163], v[32:35], 0
s_waitcnt lgkmcnt(3)
ds_read_b128 v[52:55], v79 offset:5120
s_mul_i32 s43, s43, 40960
v_mfma_f32_32x32x16_bf16 v[0:15], a[4:7], v[36:39], v[0:15]
s_add_u32 s57, s43, s58
s_mul_i32 s42, s42, 40960
s_add_u32 s46, s50, s42
s_addc_u32 s47, s51, 0
v_mfma_f32_32x32x16_bf16 v[16:31], a[164:167], v[36:39], v[16:31]
s_waitcnt lgkmcnt(3)
ds_read_b128 v[32:35], v79 offset:6144
s_add_u32 m0, s57, 0
v_mfma_f32_32x32x16_bf16 v[0:15], a[8:11], v[40:43], v[0:15]
global_load_lds_dwordx4 v72, s[46:47]
s_add_u32 s46, s46, 1024
s_addc_u32 s47, s47, 0
s_add_u32 m0, s57, 1024
v_mfma_f32_32x32x16_bf16 v[16:31], a[168:171], v[40:43], v[16:31]
s_waitcnt lgkmcnt(3)
ds_read_b128 v[36:39], v79 offset:7168
global_load_lds_dwordx4 v72, s[46:47]
v_mfma_f32_32x32x16_bf16 v[0:15], a[12:15], v[44:47], v[0:15]
s_add_u32 s46, s46, 1024
s_addc_u32 s47, s47, 0
s_add_u32 m0, s57, 2048
s_nop 0
global_load_lds_dwordx4 v72, s[46:47]
v_mfma_f32_32x32x16_bf16 v[16:31], a[172:175], v[44:47], v[16:31]
s_waitcnt lgkmcnt(3)
ds_read_b128 v[40:43], v79 offset:8192
s_add_u32 s46, s46, 1024
s_addc_u32 s47, s47, 0
v_mfma_f32_32x32x16_bf16 v[0:15], a[16:19], v[48:51], v[0:15]
s_add_u32 m0, s57, 3072
s_nop 0
global_load_lds_dwordx4 v72, s[46:47]
s_add_u32 s46, s46, 1024
s_addc_u32 s47, s47, 0
v_mfma_f32_32x32x16_bf16 v[16:31], a[176:179], v[48:51], v[16:31]
s_waitcnt lgkmcnt(3)
ds_read_b128 v[44:47], v79 offset:9216
s_add_u32 m0, s57, 4096
v_mfma_f32_32x32x16_bf16 v[0:15], a[20:23], v[52:55], v[0:15]
global_load_lds_dwordx4 v72, s[46:47]
s_add_u32 s46, s46, 1024
s_addc_u32 s47, s47, 0
s_add_u32 m0, s57, 5120
v_mfma_f32_32x32x16_bf16 v[16:31], a[180:183], v[52:55], v[16:31]
s_waitcnt lgkmcnt(3)
ds_read_b128 v[48:51], v79 offset:10240
global_load_lds_dwordx4 v72, s[46:47]
v_mfma_f32_32x32x16_bf16 v[0:15], a[24:27], v[32:35], v[0:15]
s_add_u32 s46, s46, 1024
s_addc_u32 s47, s47, 0
s_add_u32 m0, s57, 6144
s_nop 0
global_load_lds_dwordx4 v72, s[46:47]
v_mfma_f32_32x32x16_bf16 v[16:31], a[184:187], v[32:35], v[16:31]
s_waitcnt lgkmcnt(3)
ds_read_b128 v[52:55], v79 offset:11264
s_add_u32 s46, s46, 1024
s_addc_u32 s47, s47, 0
v_mfma_f32_32x32x16_bf16 v[0:15], a[28:31], v[36:39], v[0:15]
s_add_u32 m0, s57, 7168
s_nop 0
global_load_lds_dwordx4 v72, s[46:47]
s_add_u32 s46, s46, 1024
s_addc_u32 s47, s47, 0
v_mfma_f32_32x32x16_bf16 v[16:31], a[188:191], v[36:39], v[16:31]
s_waitcnt lgkmcnt(3)
ds_read_b128 v[32:35], v79 offset:12288
s_add_u32 m0, s57, 8192
v_mfma_f32_32x32x16_bf16 v[0:15], a[32:35], v[40:43], v[0:15]
global_load_lds_dwordx4 v72, s[46:47]
s_add_u32 s46, s46, 1024
s_addc_u32 s47, s47, 0
s_add_u32 m0, s57, 9216
v_mfma_f32_32x32x16_bf16 v[16:31], a[192:195], v[40:43], v[16:31]
s_waitcnt lgkmcnt(3)
ds_read_b128 v[36:39], v79 offset:13312
global_load_lds_dwordx4 v72, s[46:47]
v_mfma_f32_32x32x16_bf16 v[0:15], a[36:39], v[44:47], v[0:15]
v_mfma_f32_32x32x16_bf16 v[16:31], a[196:199], v[44:47], v[16:31]
s_waitcnt lgkmcnt(3)
ds_read_b128 v[40:43], v79 offset:14336
v_mfma_f32_32x32x16_bf16 v[0:15], a[40:43], v[48:51], v[0:15]
v_mfma_f32_32x32x16_bf16 v[16:31], a[200:203], v[48:51], v[16:31]
s_waitcnt lgkmcnt(3)
ds_read_b128 v[44:47], v79 offset:15360
v_mfma_f32_32x32x16_bf16 v[0:15], a[44:47], v[52:55], v[0:15]
v_mfma_f32_32x32x16_bf16 v[16:31], a[204:207], v[52:55], v[16:31]
s_waitcnt lgkmcnt(3)
ds_read_b128 v[48:51], v79 offset:16384
v_mfma_f32_32x32x16_bf16 v[0:15], a[48:51], v[32:35], v[0:15]
v_mfma_f32_32x32x16_bf16 v[16:31], a[208:211], v[32:35], v[16:31]
s_waitcnt lgkmcnt(3)
ds_read_b128 v[52:55], v79 offset:17408
v_mfma_f32_32x32x16_bf16 v[0:15], a[52:55], v[36:39], v[0:15]
v_mfma_f32_32x32x16_bf16 v[16:31], a[212:215], v[36:39], v[16:31]
s_waitcnt lgkmcnt(3)
ds_read_b128 v[32:35], v79 offset:18432
v_mfma_f32_32x32x16_bf16 v[0:15], a[56:59], v[40:43], v[0:15]
v_mfma_f32_32x32x16_bf16 v[16:31], a[216:219], v[40:43], v[16:31]
s_waitcnt lgkmcnt(3)
ds_read_b128 v[36:39], v79 offset:19456
v_mfma_f32_32x32x16_bf16 v[0:15], a[60:63], v[44:47], v[0:15]
s_waitcnt vmcnt(10)
v_mov_b32 v80, s36
buffer_store_dword v80, v78, s[96:99], 0 offen offset:0 sc1
v_mfma_f32_32x32x16_bf16 v[16:31], a[220:223], v[44:47], v[16:31]
s_waitcnt lgkmcnt(3)
ds_read_b128 v[40:43], v79 offset:20480
v_mfma_f32_32x32x16_bf16 v[0:15], a[64:67], v[48:51], v[0:15]
v_mfma_f32_32x32x16_bf16 v[16:31], a[224:227], v[48:51], v[16:31]
s_waitcnt lgkmcnt(3)
ds_read_b128 v[44:47], v79 offset:21504
v_mfma_f32_32x32x16_bf16 v[0:15], a[68:71], v[52:55], v[0:15]
v_mfma_f32_32x32x16_bf16 v[16:31], a[228:231], v[52:55], v[16:31]
s_waitcnt lgkmcnt(3)
ds_read_b128 v[48:51], v79 offset:22528
v_mfma_f32_32x32x16_bf16 v[0:15], a[72:75], v[32:35], v[0:15]
v_mfma_f32_32x32x16_bf16 v[16:31], a[232:235], v[32:35], v[16:31]
s_waitcnt lgkmcnt(3)
ds_read_b128 v[52:55], v79 offset:23552
v_mfma_f32_32x32x16_bf16 v[0:15], a[76:79], v[36:39], v[0:15]
v_mfma_f32_32x32x16_bf16 v[16:31], a[236:239], v[36:39], v[16:31]
s_waitcnt lgkmcnt(3)
ds_read_b128 v[32:35], v79 offset:24576
v_mfma_f32_32x32x16_bf16 v[0:15], a[80:83], v[40:43], v[0:15]
v_mfma_f32_32x32x16_bf16 v[16:31], a[240:243], v[40:43], v[16:31]
s_waitcnt lgkmcnt(3)
ds_read_b128 v[36:39], v79 offset:25600
v_mfma_f32_32x32x16_bf16 v[0:15], a[84:87], v[44:47], v[0:15]
v_mfma_f32_32x32x16_bf16 v[16:31], a[244:247], v[44:47], v[16:31]
s_waitcnt lgkmcnt(3)
ds_read_b128 v[40:43], v79 offset:26624
v_mfma_f32_32x32x16_bf16 v[0:15], a[88:91], v[48:51], v[0:15]
v_mfma_f32_32x32x16_bf16 v[16:31], a[248:251], v[48:51], v[16:31]
s_waitcnt lgkmcnt(3)
ds_read_b128 v[44:47], v79 offset:27648
v_mfma_f32_32x32x16_bf16 v[0:15], a[92:95], v[52:55], v[0:15]
v_mfma_f32_32x32x16_bf16 v[16:31], a[252:255], v[52:55], v[16:31]
s_waitcnt lgkmcnt(3)
ds_read_b128 v[48:51], v79 offset:28672
v_mfma_f32_32x32x16_bf16 v[0:15], a[96:99], v[32:35], v[0:15]
v_mfma_f32_32x32x16_bf16 v[16:31], v[192:195], v[32:35], v[16:31]
s_waitcnt lgkmcnt(3)
ds_read_b128 v[52:55], v79 offset:29696
v_mfma_f32_32x32x16_bf16 v[0:15], a[100:103], v[36:39], v[0:15]
v_mfma_f32_32x32x16_bf16 v[16:31], v[196:199], v[36:39], v[16:31]
s_waitcnt lgkmcnt(3)
ds_read_b128 v[32:35], v79 offset:30720
v_mfma_f32_32x32x16_bf16 v[0:15], a[104:107], v[40:43], v[0:15]
v_mfma_f32_32x32x16_bf16 v[16:31], v[200:203], v[40:43], v[16:31]
s_waitcnt lgkmcnt(3)
ds_read_b128 v[36:39], v79 offset:31744
v_mfma_f32_32x32x16_bf16 v[0:15], a[108:111], v[44:47], v[0:15]
v_mfma_f32_32x32x16_bf16 v[16:31], v[204:207], v[44:47], v[16:31]
s_waitcnt lgkmcnt(3)
ds_read_b128 v[40:43], v79 offset:32768
v_mfma_f32_32x32x16_bf16 v[0:15], a[112:115], v[48:51], v[0:15]
v_mfma_f32_32x32x16_bf16 v[16:31], v[208:211], v[48:51], v[16:31]
s_waitcnt lgkmcnt(3)
ds_read_b128 v[44:47], v79 offset:33792
v_mfma_f32_32x32x16_bf16 v[0:15], a[116:119], v[52:55], v[0:15]
v_mfma_f32_32x32x16_bf16 v[16:31], v[212:215], v[52:55], v[16:31]
s_waitcnt lgkmcnt(3)
ds_read_b128 v[48:51], v79 offset:34816
v_mfma_f32_32x32x16_bf16 v[0:15], a[120:123], v[32:35], v[0:15]
v_mfma_f32_32x32x16_bf16 v[16:31], v[216:219], v[32:35], v[16:31]
s_waitcnt lgkmcnt(3)
ds_read_b128 v[52:55], v79 offset:35840
v_mfma_f32_32x32x16_bf16 v[0:15], a[124:127], v[36:39], v[0:15]
v_mfma_f32_32x32x16_bf16 v[16:31], v[220:223], v[36:39], v[16:31]
s_waitcnt lgkmcnt(3)
ds_read_b128 v[32:35], v79 offset:36864
v_mfma_f32_32x32x16_bf16 v[0:15], a[128:131], v[40:43], v[0:15]
v_mfma_f32_32x32x16_bf16 v[16:31], v[224:227], v[40:43], v[16:31]
s_waitcnt lgkmcnt(3)
ds_read_b128 v[36:39], v79 offset:37888
v_mfma_f32_32x32x16_bf16 v[0:15], a[132:135], v[44:47], v[0:15]
v_mfma_f32_32x32x16_bf16 v[16:31], v[228:231], v[44:47], v[16:31]
s_waitcnt lgkmcnt(3)
ds_read_b128 v[40:43], v79 offset:38912
v_mfma_f32_32x32x16_bf16 v[0:15], a[136:139], v[48:51], v[0:15]
v_mfma_f32_32x32x16_bf16 v[16:31], v[232:235], v[48:51], v[16:31]
s_waitcnt lgkmcnt(3)
ds_read_b128 v[44:47], v79 offset:39936
v_mfma_f32_32x32x16_bf16 v[0:15], a[140:143], v[52:55], v[0:15]
v_mfma_f32_32x32x16_bf16 v[16:31], v[236:239], v[52:55], v[16:31]
s_waitcnt lgkmcnt(3)
v_mfma_f32_32x32x16_bf16 v[0:15], a[144:147], v[32:35], v[0:15]
v_mfma_f32_32x32x16_bf16 v[16:31], v[240:243], v[32:35], v[16:31]
s_waitcnt lgkmcnt(2)
v_mfma_f32_32x32x16_bf16 v[0:15], a[148:151], v[36:39], v[0:15]
v_mfma_f32_32x32x16_bf16 v[16:31], v[244:247], v[36:39], v[16:31]
s_waitcnt lgkmcnt(1)
v_mfma_f32_32x32x16_bf16 v[0:15], a[152:155], v[40:43], v[0:15]
v_mfma_f32_32x32x16_bf16 v[16:31], v[248:251], v[40:43], v[16:31]
s_waitcnt lgkmcnt(0)
v_mfma_f32_32x32x16_bf16 v[0:15], a[156:159], v[44:47], v[0:15]
s_waitcnt vmcnt(1)
v_mfma_f32_32x32x16_bf16 v[16:31], v[252:255], v[44:47], v[16:31]
s_nop 9
v_cvt_pk_bf16_f32 v56, v0, v1
v_cvt_pk_bf16_f32 v57, v2, v3
v_cvt_pk_bf16_f32 v58, v4, v5
v_cvt_pk_bf16_f32 v59, v6, v7
buffer_store_dwordx4 v[56:59], v74, s[96:99], s37 offen offset:0 sc1
v_cvt_pk_bf16_f32 v60, v8, v9
v_cvt_pk_bf16_f32 v61, v10, v11
v_cvt_pk_bf16_f32 v62, v12, v13
v_cvt_pk_bf16_f32 v63, v14, v15
buffer_store_dwordx4 v[60:63], v75, s[96:99], s37 offen offset:0 sc1
v_cvt_pk_bf16_f32 v64, v16, v17
v_cvt_pk_bf16_f32 v65, v18, v19
v_cvt_pk_bf16_f32 v66, v20, v21
v_cvt_pk_bf16_f32 v67, v22, v23
buffer_store_dwordx4 v[64:67], v76, s[96:99], s37 offen offset:0 sc1
v_cvt_pk_bf16_f32 v68, v24, v25
v_cvt_pk_bf16_f32 v69, v26, v27
v_cvt_pk_bf16_f32 v70, v28, v29
v_cvt_pk_bf16_f32 v71, v30, v31
buffer_store_dwordx4 v[68:71], v77, s[96:99], s37 offen offset:0 sc1
s_add_u32 s36, s36, 1
s_sub_u32 s42, s36, 3
s_cmp_lt_u32 s42, 61
s_cbranch_scc1 PA_T
s_waitcnt vmcnt(0)
v_mov_b32 v80, s36
buffer_store_dword v80, v78, s[96:99], 0 offen offset:0 sc1
s_cmp_lt_u32 s36, 64
s_cbranch_scc1 PA_T
s_endpgm
ROLE_PC:
s_mov_b64 s[48:49], %0
s_mov_b64 s[50:51], %1
s_mov_b64 s[52:53], %2
s_mov_b32 s54, %3
s_mov_b32 s40, %4
s_mov_b32 s39, %5
s_mov_b32 s41, 0x200000
s_mov_b32 s36, 0
s_lshl_b32 s55, s40, 10
v_mbcnt_lo_u32_b32 v31, -1, 0
v_mbcnt_hi_u32_b32 v31, -1, v31
s_lshl_b32 s42, s40, 8
s_add_u32 s42, s42, 147456
v_lshl_add_u32 v31, v31, 2, s42
ds_read_b32 v152, v31 offset:0
ds_read_b32 v153, v31 offset:1024
ds_read_b32 v154, v31 offset:2048
ds_read_b32 v155, v31 offset:3072
ds_read_b32 v156, v31 offset:4096
ds_read_b32 v157, v31 offset:5120
ds_read_b32 v158, v31 offset:6144
ds_read_b32 v159, v31 offset:7168
ds_read_b32 v160, v31 offset:8192
ds_read_b32 v161, v31 offset:9216
ds_read_b32 v168, v31 offset:10240
ds_read_b32 v169, v31 offset:11264
ds_read_b32 v170, v31 offset:12288
ds_read_b32 v171, v31 offset:13312
s_waitcnt lgkmcnt(0)
s_memrealtime s[94:95]
s_waitcnt lgkmcnt(0)
s_mov_b32 s92, s94
s_add_u32 s20, s48, 0x0
s_addc_u32 s21, s49, 0
s_and_b32 s21, s21, 0xffff
s_mov_b32 s22, 0x400000
s_mov_b32 s23, 0x20000
s_add_u32 s96, s48, 0x0
s_addc_u32 s97, s49, 0
s_and_b32 s97, s97, 0xffff
s_mov_b32 s98, 0xfe00000
s_mov_b32 s99, 0x20000
s_mov_b32 s44, 0x30000
s_mov_b32 s45, 0
s_mov_b32 s46, 0xf0000
s_mov_b32 s47, 0
s_mov_b32 s76, 0x5000
s_mov_b32 s77, 0
s_mov_b32 s80, 0xfffc9100
s_mov_b32 s81, -1
s_add_u32 m0, s55, 0
s_nop 0
global_load_lds_dwordx4 v[168:169], off
v_lshl_add_u64 v[168:169], v[168:169], 0, s[44:45]
s_add_u32 m0, s55, 4096
s_nop 0
global_load_lds_dwordx4 v[168:169], off
v_lshl_add_u64 v[168:169], v[168:169], 0, s[44:45]
s_add_u32 m0, s55, 8192
s_nop 0
global_load_lds_dwordx4 v[168:169], off
v_lshl_add_u64 v[168:169], v[168:169], 0, s[44:45]
s_add_u32 m0, s55, 12288
s_nop 0
global_load_lds_dwordx4 v[168:169], off
v_lshl_add_u64 v[168:169], v[168:169], 0, s[46:47]
s_add_u32 m0, s55, 16384
s_nop 0
global_load_lds_dwordx4 v[168:169], off
v_lshl_add_u64 v[168:169], v[168:169], 0, s[44:45]
s_add_u32 m0, s55, 20480
s_nop 0
global_load_lds_dwordx4 v[168:169], off
v_lshl_add_u64 v[168:169], v[168:169], 0, s[44:45]
s_add_u32 m0, s55, 24576
s_nop 0
global_load_lds_dwordx4 v[168:169], off
v_lshl_add_u64 v[168:169], v[168:169], 0, s[44:45]
s_add_u32 m0, s55, 28672
s_nop 0
global_load_lds_dwordx4 v[168:169], off
v_lshl_add_u64 v[168:169], v[168:169], 0, s[46:47]
s_add_u32 m0, s55, 32768
s_nop 0
global_load_lds_dwordx4 v[168:169], off
v_lshl_add_u64 v[168:169], v[168:169], 0, s[44:45]
s_add_u32 m0, s55, 36864
s_nop 0
global_load_lds_dwordx4 v[168:169], off
v_lshl_add_u64 v[168:169], v[168:169], 0, s[44:45]
s_add_u32 m0, s55, 40960
s_nop 0
global_load_lds_dwordx4 v[168:169], off
v_lshl_add_u64 v[168:169], v[168:169], 0, s[44:45]
s_add_u32 m0, s55, 45056
s_nop 0
global_load_lds_dwordx4 v[168:169], off
v_lshl_add_u64 v[168:169], v[168:169], 0, s[46:47]
s_add_u32 m0, s55, 49152
s_nop 0
global_load_lds_dwordx4 v[168:169], off
v_lshl_add_u64 v[168:169], v[168:169], 0, s[44:45]
s_add_u32 m0, s55, 53248
s_nop 0
global_load_lds_dwordx4 v[168:169], off
v_lshl_add_u64 v[168:169], v[168:169], 0, s[44:45]
s_add_u32 m0, s55, 57344
s_nop 0
global_load_lds_dwordx4 v[168:169], off
v_lshl_add_u64 v[168:169], v[168:169], 0, s[44:45]
s_add_u32 m0, s55, 61440
s_nop 0
global_load_lds_dwordx4 v[168:169], off
v_lshl_add_u64 v[168:169], v[168:169], 0, s[46:47]
s_add_u32 m0, s55, 65536
s_nop 0
global_load_lds_dwordx4 v[168:169], off
v_lshl_add_u64 v[168:169], v[168:169], 0, s[44:45]
s_add_u32 m0, s55, 69632
s_nop 0
global_load_lds_dwordx4 v[168:169], off
v_lshl_add_u64 v[168:169], v[168:169], 0, s[44:45]
s_add_u32 m0, s55, 73728
s_nop 0
global_load_lds_dwordx4 v[168:169], off
v_lshl_add_u64 v[168:169], v[168:169], 0, s[44:45]
s_add_u32 m0, s55, 77824
s_nop 0
global_load_lds_dwordx4 v[168:169], off
v_lshl_add_u64 v[168:169], v[168:169], 0, s[46:47]
s_add_u32 m0, s55, 81920
s_nop 0
global_load_lds_dwordx4 v[170:171], off
v_lshl_add_u64 v[170:171], v[170:171], 0, s[76:77]
s_add_u32 m0, s55, 86016
s_nop 0
global_load_lds_dwordx4 v[170:171], off
v_lshl_add_u64 v[170:171], v[170:171], 0, s[76:77]
s_add_u32 m0, s55, 90112
s_nop 0
global_load_lds_dwordx4 v[170:171], off
v_lshl_add_u64 v[170:171], v[170:171], 0, s[76:77]
s_add_u32 m0, s55, 94208
s_nop 0
global_load_lds_dwordx4 v[170:171], off
v_lshl_add_u64 v[170:171], v[170:171], 0, s[76:77]
s_add_u32 m0, s55, 98304
s_nop 0
global_load_lds_dwordx4 v[170:171], off
v_lshl_add_u64 v[170:171], v[170:171], 0, s[76:77]
s_add_u32 m0, s55, 102400
s_nop 0
global_load_lds_dwordx4 v[170:171], off
v_lshl_add_u64 v[170:171], v[170:171], 0, s[76:77]
s_add_u32 m0, s55, 106496
s_nop 0
global_load_lds_dwordx4 v[170:171], off
v_lshl_add_u64 v[170:171], v[170:171], 0, s[76:77]
s_add_u32 m0, s55, 110592
s_nop 0
global_load_lds_dwordx4 v[170:171], off
v_lshl_add_u64 v[170:171], v[170:171], 0, s[76:77]
s_add_u32 m0, s55, 114688
s_nop 0
global_load_lds_dwordx4 v[170:171], off
v_lshl_add_u64 v[170:171], v[170:171], 0, s[76:77]
s_add_u32 m0, s55, 118784
s_nop 0
global_load_lds_dwordx4 v[170:171], off
v_lshl_add_u64 v[170:171], v[170:171], 0, s[76:77]
s_add_u32 m0, s55, 122880
s_nop 0
global_load_lds_dwordx4 v[170:171], off
v_lshl_add_u64 v[170:171], v[170:171], 0, s[76:77]
s_add_u32 m0, s55, 126976
s_nop 0
global_load_lds_dwordx4 v[170:171], off
v_lshl_add_u64 v[170:171], v[170:171], 0, s[80:81]
s_waitcnt vmcnt(28)
s_waitcnt lgkmcnt(0)
s_barrier
s_add_u32 m0, s55, 131072
s_nop 0
global_load_lds_dwordx4 v[170:171], off
v_lshl_add_u64 v[170:171], v[170:171], 0, s[76:77]
s_add_u32 m0, s55, 135168
s_nop 0
global_load_lds_dwordx4 v[170:171], off
v_lshl_add_u64 v[170:171], v[170:171], 0, s[76:77]
s_add_u32 m0, s55, 139264
s_nop 0
global_load_lds_dwordx4 v[170:171], off
v_lshl_add_u64 v[170:171], v[170:171], 0, s[76:77]
s_add_u32 m0, s55, 143360
s_nop 0
global_load_lds_dwordx4 v[170:171], off
v_lshl_add_u64 v[170:171], v[170:171], 0, s[76:77]
v_add_u32 v164, 0, v153
ds_read_b64_tr_b16 a[240:241], v164 offset:0
ds_read_b64_tr_b16 a[242:243], v164 offset:1024
ds_read_b64_tr_b16 a[244:245], v164 offset:8192
ds_read_b64_tr_b16 a[246:247], v164 offset:9216
v_add_u32 v163, 0, v154
ds_read_b64_tr_b16 a[248:249], v163 offset:0
ds_read_b64_tr_b16 a[250:251], v163 offset:1024
ds_read_b64_tr_b16 a[252:253], v163 offset:8192
ds_read_b64_tr_b16 a[254:255], v163 offset:9216
s_waitcnt vmcnt(28)
s_waitcnt lgkmcnt(0)
s_barrier
s_add_u32 m0, s55, 0
s_nop 0
global_load_lds_dwordx4 v[170:171], off
v_lshl_add_u64 v[170:171], v[170:171], 0, s[76:77]
s_add_u32 m0, s55, 4096
s_nop 0
global_load_lds_dwordx4 v[170:171], off
v_lshl_add_u64 v[170:171], v[170:171], 0, s[76:77]
s_add_u32 m0, s55, 8192
s_nop 0
global_load_lds_dwordx4 v[170:171], off
v_lshl_add_u64 v[170:171], v[170:171], 0, s[76:77]
s_add_u32 m0, s55, 12288
s_nop 0
global_load_lds_dwordx4 v[170:171], off
v_lshl_add_u64 v[170:171], v[170:171], 0, s[76:77]
v_add_u32 v164, 16384, v153
ds_read_b64_tr_b16 v[192:193], v164 offset:0
ds_read_b64_tr_b16 v[194:195], v164 offset:1024
ds_read_b64_tr_b16 v[196:197], v164 offset:8192
ds_read_b64_tr_b16 v[198:199], v164 offset:9216
v_add_u32 v163, 16384, v154
ds_read_b64_tr_b16 v[200:201], v163 offset:0
ds_read_b64_tr_b16 v[202:203], v163 offset:1024
ds_read_b64_tr_b16 v[204:205], v163 offset:8192
ds_read_b64_tr_b16 v[206:207], v163 offset:9216
s_waitcnt vmcnt(28)
s_waitcnt lgkmcnt(0)
s_barrier
s_add_u32 m0, s55, 16384
s_nop 0
global_load_lds_dwordx4 v[170:171], off
v_lshl_add_u64 v[170:171], v[170:171], 0, s[76:77]
s_add_u32 m0, s55, 20480
s_nop 0
global_load_lds_dwordx4 v[170:171], off
v_lshl_add_u64 v[170:171], v[170:171], 0, s[76:77]
s_add_u32 m0, s55, 24576
s_nop 0
global_load_lds_dwordx4 v[170:171], off
v_lshl_add_u64 v[170:171], v[170:171], 0, s[76:77]
s_add_u32 m0, s55, 28672
s_nop 0
global_load_lds_dwordx4 v[170:171], off
v_lshl_add_u64 v[170:171], v[170:171], 0, s[80:81]
v_add_u32 v164, 32768, v153
ds_read_b64_tr_b16 v[208:209], v164 offset:0
ds_read_b64_tr_b16 v[210:211], v164 offset:1024
ds_read_b64_tr_b16 v[212:213], v164 offset:8192
ds_read_b64_tr_b16 v[214:215], v164 offset:9216
v_add_u32 v163, 32768, v154
ds_read_b64_tr_b16 v[216:217], v163 offset:0
ds_read_b64_tr_b16 v[218:219], v163 offset:1024
ds_read_b64_tr_b16 v[220:221], v163 offset:8192
ds_read_b64_tr_b16 v[222:223], v163 offset:9216
s_waitcnt vmcnt(28)
s_waitcnt lgkmcnt(0)
s_barrier
s_add_u32 m0, s55, 32768
s_nop 0
global_load_lds_dwordx4 v[170:171], off
v_lshl_add_u64 v[170:171], v[170:171], 0, s[76:77]
s_add_u32 m0, s55, 36864
s_nop 0
global_load_lds_dwordx4 v[170:171], off
v_lshl_add_u64 v[170:171], v[170:171], 0, s[76:77]
s_add_u32 m0, s55, 40960
s_nop 0
global_load_lds_dwordx4 v[170:171], off
v_lshl_add_u64 v[170:171], v[170:171], 0, s[76:77]
s_add_u32 m0, s55, 45056
s_nop 0
global_load_lds_dwordx4 v[170:171], off
v_lshl_add_u64 v[170:171], v[170:171], 0, s[76:77]
v_add_u32 v164, 49152, v153
ds_read_b64_tr_b16 v[224:225], v164 offset:0
ds_read_b64_tr_b16 v[226:227], v164 offset:1024
ds_read_b64_tr_b16 v[228:229], v164 offset:8192
ds_read_b64_tr_b16 v[230:231], v164 offset:9216
v_add_u32 v163, 49152, v154
ds_read_b64_tr_b16 v[232:233], v163 offset:0
ds_read_b64_tr_b16 v[234:235], v163 offset:1024
ds_read_b64_tr_b16 v[236:237], v163 offset:8192
ds_read_b64_tr_b16 v[238:239], v163 offset:9216
s_waitcnt vmcnt(28)
s_waitcnt lgkmcnt(0)
s_barrier
s_add_u32 m0, s55, 49152
s_nop 0
global_load_lds_dwordx4 v[170:171], off
v_lshl_add_u64 v[170:171], v[170:171], 0, s[76:77]
s_add_u32 m0, s55, 53248
s_nop 0
global_load_lds_dwordx4 v[170:171], off
v_lshl_add_u64 v[170:171], v[170:171], 0, s[76:77]
s_add_u32 m0, s55, 57344
s_nop 0
global_load_lds_dwordx4 v[170:171], off
v_lshl_add_u64 v[170:171], v[170:171], 0, s[76:77]
s_add_u32 m0, s55, 61440
s_nop 0
global_load_lds_dwordx4 v[170:171], off
v_lshl_add_u64 v[170:171], v[170:171], 0, s[76:77]
v_add_u32 v164, 65536, v153
ds_read_b64_tr_b16 v[240:241], v164 offset:0
ds_read_b64_tr_b16 v[242:243], v164 offset:1024
ds_read_b64_tr_b16 v[244:245], v164 offset:8192
ds_read_b64_tr_b16 v[246:247], v164 offset:9216
v_add_u32 v163, 65536, v154
ds_read_b64_tr_b16 v[248:249], v163 offset:0
ds_read_b64_tr_b16 v[250:251], v163 offset:1024
ds_read_b64_tr_b16 v[252:253], v163 offset:8192
ds_read_b64_tr_b16 v[254:255], v163 offset:9216
s_waitcnt vmcnt(28)
s_waitcnt lgkmcnt(0)
s_barrier
s_add_u32 m0, s55, 65536
s_nop 0
global_load_lds_dwordx4 v[170:171], off
v_lshl_add_u64 v[170:171], v[170:171], 0, s[76:77]
s_add_u32 m0, s55, 69632
s_nop 0
global_load_lds_dwordx4 v[170:171], off
v_lshl_add_u64 v[170:171], v[170:171], 0, s[76:77]
s_add_u32 m0, s55, 73728
s_nop 0
global_load_lds_dwordx4 v[170:171], off
v_lshl_add_u64 v[170:171], v[170:171], 0, s[76:77]
s_add_u32 m0, s55, 77824
s_nop 0
global_load_lds_dwordx4 v[170:171], off
v_lshl_add_u64 v[170:171], v[170:171], 0, s[80:81]
v_add_u32 v164, 81920, v155
ds_read_b64_tr_b16 a[0:1], v164 offset:0
ds_read_b64_tr_b16 a[2:3], v164 offset:1024
ds_read_b64_tr_b16 a[4:5], v164 offset:4096
ds_read_b64_tr_b16 a[6:7], v164 offset:5120
ds_read_b64_tr_b16 a[8:9], v164 offset:8192
ds_read_b64_tr_b16 a[10:11], v164 offset:9216
ds_read_b64_tr_b16 a[12:13], v164 offset:12288
ds_read_b64_tr_b16 a[14:15], v164 offset:13312
s_waitcnt vmcnt(28)
s_waitcnt lgkmcnt(0)
s_barrier
s_add_u32 m0, s55, 81920
s_nop 0
global_load_lds_dwordx4 v[170:171], off
v_lshl_add_u64 v[170:171], v[170:171], 0, s[76:77]
s_add_u32 m0, s55, 86016
s_nop 0
global_load_lds_dwordx4 v[170:171], off
v_lshl_add_u64 v[170:171], v[170:171], 0, s[76:77]
s_add_u32 m0, s55, 90112
s_nop 0
global_load_lds_dwordx4 v[170:171], off
v_lshl_add_u64 v[170:171], v[170:171], 0, s[76:77]
s_add_u32 m0, s55, 94208
s_nop 0
global_load_lds_dwordx4 v[170:171], off
v_lshl_add_u64 v[170:171], v[170:171], 0, s[76:77]
v_add_u32 v164, 98304, v155
ds_read_b64_tr_b16 a[16:17], v164 offset:0
ds_read_b64_tr_b16 a[18:19], v164 offset:1024
ds_read_b64_tr_b16 a[20:21], v164 offset:4096
ds_read_b64_tr_b16 a[22:23], v164 offset:5120
ds_read_b64_tr_b16 a[24:25], v164 offset:8192
ds_read_b64_tr_b16 a[26:27], v164 offset:9216
ds_read_b64_tr_b16 a[28:29], v164 offset:12288
ds_read_b64_tr_b16 a[30:31], v164 offset:13312
s_waitcnt vmcnt(28)
s_waitcnt lgkmcnt(0)
s_barrier
s_add_u32 m0, s55, 98304
s_nop 0
global_load_lds_dwordx4 v[170:171], off
v_lshl_add_u64 v[170:171], v[170:171], 0, s[76:77]
s_add_u32 m0, s55, 102400
s_nop 0
global_load_lds_dwordx4 v[170:171], off
v_lshl_add_u64 v[170:171], v[170:171], 0, s[76:77]
s_add_u32 m0, s55, 106496
s_nop 0
global_load_lds_dwordx4 v[170:171], off
v_lshl_add_u64 v[170:171], v[170:171], 0, s[76:77]
s_add_u32 m0, s55, 110592
s_nop 0
global_load_lds_dwordx4 v[170:171], off
v_lshl_add_u64 v[170:171], v[170:171], 0, s[76:77]
v_add_u32 v164, 114688, v155
ds_read_b64_tr_b16 a[32:33], v164 offset:0
ds_read_b64_tr_b16 a[34:35], v164 offset:1024
ds_read_b64_tr_b16 a[36:37], v164 offset:4096
ds_read_b64_tr_b16 a[38:39], v164 offset:5120
ds_read_b64_tr_b16 a[40:41], v164 offset:8192
ds_read_b64_tr_b16 a[42:43], v164 offset:9216
ds_read_b64_tr_b16 a[44:45], v164 offset:12288
ds_read_b64_tr_b16 a[46:47], v164 offset:13312
s_waitcnt vmcnt(28)
s_waitcnt lgkmcnt(0)
s_barrier
s_add_u32 m0, s55, 114688
s_nop 0
global_load_lds_dwordx4 v[170:171], off
v_lshl_add_u64 v[170:171], v[170:171], 0, s[76:77]
s_add_u32 m0, s55, 118784
s_nop 0
global_load_lds_dwordx4 v[170:171], off
v_lshl_add_u64 v[170:171], v[170:171], 0, s[76:77]
s_add_u32 m0, s55, 122880
s_nop 0
global_load_lds_dwordx4 v[170:171], off
v_lshl_add_u64 v[170:171], v[170:171], 0, s[76:77]
s_add_u32 m0, s55, 126976
s_nop 0
global_load_lds_dwordx4 v[170:171], off
v_lshl_add_u64 v[170:171], v[170:171], 0, s[80:81]
v_add_u32 v164, 131072, v155
ds_read_b64_tr_b16 a[48:49], v164 offset:0
ds_read_b64_tr_b16 a[50:51], v164 offset:1024
ds_read_b64_tr_b16 a[52:53], v164 offset:4096
ds_read_b64_tr_b16 a[54:55], v164 offset:5120
ds_read_b64_tr_b16 a[56:57], v164 offset:8192
ds_read_b64_tr_b16 a[58:59], v164 offset:9216
ds_read_b64_tr_b16 a[60:61], v164 offset:12288
ds_read_b64_tr_b16 a[62:63], v164 offset:13312
s_waitcnt vmcnt(28)
s_waitcnt lgkmcnt(0)
s_barrier
s_add_u32 m0, s55, 131072
s_nop 0
global_load_lds_dwordx4 v[170:171], off
v_lshl_add_u64 v[170:171], v[170:171], 0, s[76:77]
s_add_u32 m0, s55, 135168
s_nop 0
global_load_lds_dwordx4 v[170:171], off
v_lshl_add_u64 v[170:171], v[170:171], 0, s[76:77]
s_add_u32 m0, s55, 139264
s_nop 0
global_load_lds_dwordx4 v[170:171], off
v_lshl_add_u64 v[170:171], v[170:171], 0, s[76:77]
s_add_u32 m0, s55, 143360
s_nop 0
global_load_lds_dwordx4 v[170:171], off
v_lshl_add_u64 v[170:171], v[170:171], 0, s[76:77]
v_add_u32 v164, 0, v155
ds_read_b64_tr_b16 a[64:65], v164 offset:0
ds_read_b64_tr_b16 a[66:67], v164 offset:1024
ds_read_b64_tr_b16 a[68:69], v164 offset:4096
ds_read_b64_tr_b16 a[70:71], v164 offset:5120
ds_read_b64_tr_b16 a[72:73], v164 offset:8192
ds_read_b64_tr_b16 a[74:75], v164 offset:9216
ds_read_b64_tr_b16 a[76:77], v164 offset:12288
ds_read_b64_tr_b16 a[78:79], v164 offset:13312
s_waitcnt vmcnt(28)
s_waitcnt lgkmcnt(0)
s_barrier
s_add_u32 m0, s55, 0
s_nop 0
global_load_lds_dwordx4 v[170:171], off
v_lshl_add_u64 v[170:171], v[170:171], 0, s[76:77]
s_add_u32 m0, s55, 4096
s_nop 0
global_load_lds_dwordx4 v[170:171], off
v_lshl_add_u64 v[170:171], v[170:171], 0, s[76:77]
s_add_u32 m0, s55, 8192
s_nop 0
global_load_lds_dwordx4 v[170:171], off
v_lshl_add_u64 v[170:171], v[170:171], 0, s[76:77]
s_add_u32 m0, s55, 12288
s_nop 0
global_load_lds_dwordx4 v[170:171], off
v_lshl_add_u64 v[170:171], v[170:171], 0, s[76:77]
v_add_u32 v164, 16384, v155
ds_read_b64_tr_b16 a[80:81], v164 offset:0
ds_read_b64_tr_b16 a[82:83], v164 offset:1024
ds_read_b64_tr_b16 a[84:85], v164 offset:4096
ds_read_b64_tr_b16 a[86:87], v164 offset:5120
ds_read_b64_tr_b16 a[88:89], v164 offset:8192
ds_read_b64_tr_b16 a[90:91], v164 offset:9216
ds_read_b64_tr_b16 a[92:93], v164 offset:12288
ds_read_b64_tr_b16 a[94:95], v164 offset:13312
s_waitcnt vmcnt(28)
s_waitcnt lgkmcnt(0)
s_barrier
s_add_u32 m0, s55, 16384
s_nop 0
global_load_lds_dwordx4 v[170:171], off
v_lshl_add_u64 v[170:171], v[170:171], 0, s[76:77]
s_add_u32 m0, s55, 20480
s_nop 0
global_load_lds_dwordx4 v[170:171], off
v_lshl_add_u64 v[170:171], v[170:171], 0, s[76:77]
s_add_u32 m0, s55, 24576
s_nop 0
global_load_lds_dwordx4 v[170:171], off
v_lshl_add_u64 v[170:171], v[170:171], 0, s[76:77]
s_add_u32 m0, s55, 28672
s_nop 0
global_load_lds_dwordx4 v[170:171], off
v_lshl_add_u64 v[170:171], v[170:171], 0, s[80:81]
v_add_u32 v164, 32768, v155
ds_read_b64_tr_b16 a[96:97], v164 offset:0
ds_read_b64_tr_b16 a[98:99], v164 offset:1024
ds_read_b64_tr_b16 a[100:101], v164 offset:4096
ds_read_b64_tr_b16 a[102:103], v164 offset:5120
ds_read_b64_tr_b16 a[104:105], v164 offset:8192
ds_read_b64_tr_b16 a[106:107], v164 offset:9216
ds_read_b64_tr_b16 a[108:109], v164 offset:12288
ds_read_b64_tr_b16 a[110:111], v164 offset:13312
s_waitcnt vmcnt(28)
s_waitcnt lgkmcnt(0)
s_barrier
v_add_u32 v164, 49152, v155
ds_read_b64_tr_b16 a[112:113], v164 offset:0
ds_read_b64_tr_b16 a[114:115], v164 offset:1024
ds_read_b64_tr_b16 a[116:117], v164 offset:4096
ds_read_b64_tr_b16 a[118:119], v164 offset:5120
ds_read_b64_tr_b16 a[120:121], v164 offset:8192
ds_read_b64_tr_b16 a[122:123], v164 offset:9216
ds_read_b64_tr_b16 a[124:125], v164 offset:12288
ds_read_b64_tr_b16 a[126:127], v164 offset:13312
s_waitcnt vmcnt(24)
s_waitcnt lgkmcnt(0)
s_barrier
v_add_u32 v164, 65536, v155
ds_read_b64_tr_b16 a[128:129], v164 offset:0
ds_read_b64_tr_b16 a[130:131], v164 offset:1024
ds_read_b64_tr_b16 a[132:133], v164 offset:4096
ds_read_b64_tr_b16 a[134:135], v164 offset:5120
ds_read_b64_tr_b16 a[136:137], v164 offset:8192
ds_read_b64_tr_b16 a[138:139], v164 offset:9216
ds_read_b64_tr_b16 a[140:141], v164 offset:12288
ds_read_b64_tr_b16 a[142:143], v164 offset:13312
s_waitcnt vmcnt(20)
s_waitcnt lgkmcnt(0)
s_barrier
v_add_u32 v164, 81920, v155
ds_read_b64_tr_b16 a[144:145], v164 offset:0
ds_read_b64_tr_b16 a[146:147], v164 offset:1024
ds_read_b64_tr_b16 a[148:149], v164 offset:4096
ds_read_b64_tr_b16 a[150:151], v164 offset:5120
ds_read_b64_tr_b16 a[152:153], v164 offset:8192
ds_read_b64_tr_b16 a[154:155], v164 offset:9216
ds_read_b64_tr_b16 a[156:157], v164 offset:12288
ds_read_b64_tr_b16 a[158:159], v164 offset:13312
s_waitcnt vmcnt(16)
s_waitcnt lgkmcnt(0)
s_barrier
v_add_u32 v164, 98304, v155
ds_read_b64_tr_b16 a[160:161], v164 offset:0
ds_read_b64_tr_b16 a[162:163], v164 offset:1024
ds_read_b64_tr_b16 a[164:165], v164 offset:4096
ds_read_b64_tr_b16 a[166:167], v164 offset:5120
ds_read_b64_tr_b16 a[168:169], v164 offset:8192
ds_read_b64_tr_b16 a[170:171], v164 offset:9216
ds_read_b64_tr_b16 a[172:173], v164 offset:12288
ds_read_b64_tr_b16 a[174:175], v164 offset:13312
s_waitcnt vmcnt(12)
s_waitcnt lgkmcnt(0)
s_barrier
v_add_u32 v164, 114688, v155
ds_read_b64_tr_b16 a[176:177], v164 offset:0
ds_read_b64_tr_b16 a[178:179], v164 offset:1024
ds_read_b64_tr_b16 a[180:181], v164 offset:4096
ds_read_b64_tr_b16 a[182:183], v164 offset:5120
ds_read_b64_tr_b16 a[184:185], v164 offset:8192
ds_read_b64_tr_b16 a[186:187], v164 offset:9216
ds_read_b64_tr_b16 a[188:189], v164 offset:12288
ds_read_b64_tr_b16 a[190:191], v164 offset:13312
s_waitcnt vmcnt(8)
s_waitcnt lgkmcnt(0)
s_barrier
v_add_u32 v164, 131072, v155
ds_read_b64_tr_b16 a[192:193], v164 offset:0
ds_read_b64_tr_b16 a[194:195], v164 offset:1024
ds_read_b64_tr_b16 a[196:197], v164 offset:4096
ds_read_b64_tr_b16 a[198:199], v164 offset:5120
ds_read_b64_tr_b16 a[200:201], v164 offset:8192
ds_read_b64_tr_b16 a[202:203], v164 offset:9216
ds_read_b64_tr_b16 a[204:205], v164 offset:12288
ds_read_b64_tr_b16 a[206:207], v164 offset:13312
s_waitcnt vmcnt(4)
s_waitcnt lgkmcnt(0)
s_barrier
v_add_u32 v164, 0, v155
ds_read_b64_tr_b16 a[208:209], v164 offset:0
ds_read_b64_tr_b16 a[210:211], v164 offset:1024
ds_read_b64_tr_b16 a[212:213], v164 offset:4096
ds_read_b64_tr_b16 a[214:215], v164 offset:5120
ds_read_b64_tr_b16 a[216:217], v164 offset:8192
ds_read_b64_tr_b16 a[218:219], v164 offset:9216
ds_read_b64_tr_b16 a[220:221], v164 offset:12288
ds_read_b64_tr_b16 a[222:223], v164 offset:13312
s_waitcnt vmcnt(0)
s_waitcnt lgkmcnt(0)
s_barrier
v_add_u32 v164, 16384, v155
ds_read_b64_tr_b16 a[224:225], v164 offset:0
ds_read_b64_tr_b16 a[226:227], v164 offset:1024
ds_read_b64_tr_b16 a[228:229], v164 offset:4096
ds_read_b64_tr_b16 a[230:231], v164 offset:5120
ds_read_b64_tr_b16 a[232:233], v164 offset:8192
ds_read_b64_tr_b16 a[234:235], v164 offset:9216
ds_read_b64_tr_b16 a[236:237], v164 offset:12288
ds_read_b64_tr_b16 a[238:239], v164 offset:13312
s_waitcnt lgkmcnt(0)
s_barrier
s_mul_i32 s58, s40, 10240
s_add_u32 s50, s48, 5242880
s_addc_u32 s51, s49, 0
s_add_u32 s50, s50, s58
s_addc_u32 s51, s51, 0
s_add_u32 s42, s36, 0
s_and_b32 s43, s42, 1
s_min_u32 s42, s42, 63
s_mul_i32 s43, s43, 40960
s_add_u32 s57, s43, s58
s_mul_i32 s42, s42, 40960
s_add_u32 s46, s50, s42
s_addc_u32 s47, s51, 0
s_add_u32 m0, s57, 0
s_nop 0
global_load_lds_dwordx4 v152, s[46:47]
s_add_u32 s46, s46, 1024
s_addc_u32 s47, s47, 0
s_add_u32 m0, s57, 1024
s_nop 0
global_load_lds_dwordx4 v152, s[46:47]
s_add_u32 s46, s46, 1024
s_addc_u32 s47, s47, 0
s_add_u32 m0, s57, 2048
s_nop 0
global_load_lds_dwordx4 v152, s[46:47]
s_add_u32 s46, s46, 1024
s_addc_u32 s47, s47, 0
s_add_u32 m0, s57, 3072
s_nop 0
global_load_lds_dwordx4 v152, s[46:47]
s_add_u32 s46, s46, 1024
s_addc_u32 s47, s47, 0
s_add_u32 m0, s57, 4096
s_nop 0
global_load_lds_dwordx4 v152, s[46:47]
s_add_u32 s46, s46, 1024
s_addc_u32 s47, s47, 0
s_add_u32 m0, s57, 5120
s_nop 0
global_load_lds_dwordx4 v152, s[46:47]
s_add_u32 s46, s46, 1024
s_addc_u32 s47, s47, 0
s_add_u32 m0, s57, 6144
s_nop 0
global_load_lds_dwordx4 v152, s[46:47]
s_add_u32 s46, s46, 1024
s_addc_u32 s47, s47, 0
s_add_u32 m0, s57, 7168
s_nop 0
global_load_lds_dwordx4 v152, s[46:47]
s_add_u32 s46, s46, 1024
s_addc_u32 s47, s47, 0
s_add_u32 m0, s57, 8192
s_nop 0
global_load_lds_dwordx4 v152, s[46:47]
s_add_u32 s46, s46, 1024
s_addc_u32 s47, s47, 0
s_add_u32 m0, s57, 9216
s_nop 0
global_load_lds_dwordx4 v152, s[46:47]
s_lshl_b32 s42, s40, 11
v_add_u32 v166, s42, v152
v_add_u32 v167, 81920, v166
v_add_u32 v158, 81920, v158
s_nop 1
PC_F0:
buffer_load_dword v165, v161, s[96:99], 0 offen offset:0 sc1
s_waitcnt vmcnt(0)
v_cmp_gt_u32 vcc, 1, v165
s_cbranch_vccz PC_F0K
s_sleep 3
s_sub_u32 s41, s41, 1
s_cbranch_scc0 PC_F0
s_mov_b32 s41, 0
PC_F0K:
s_add_u32 s38, s36, 0
s_min_u32 s38, s38, 63
s_lshl_b32 s38, s38, 5
s_add_u32 s38, s38, s54
s_mul_i32 s38, s38, 12288
s_add_u32 s38, s38, 58720256
buffer_load_dwordx4 v[80:83], v166, s[96:99], s38 offen offset:0 sc1
buffer_load_dwordx4 v[84:87], v166, s[96:99], s38 offen offset:1024 sc1
s_waitcnt vmcnt(0)
PC_T:
s_and_b32 s42, s36, 1
s_mul_i32 s42, s42, 40960
v_add_u32 v162, s42, v156
s_mul_i32 s37, s36, 1310720
s_add_u32 s59, s36, 2
s_min_u32 s59, s59, 64
buffer_load_dword v165, v161, s[96:99], 0 offen offset:0 sc1
s_waitcnt lgkmcnt(0)
s_barrier
ds_read_b128 v[120:123], v162 offset:0
ds_read_b128 v[124:127], v162 offset:256
ds_read_b128 v[128:131], v162 offset:2048
ds_read_b128 v[132:135], v162 offset:2304
ds_read_b128 v[136:139], v162 offset:4096
s_waitcnt lgkmcnt(4)
v_mfma_f32_16x16x32_bf16 v[112:115], a[240:243], v[120:123], 0
s_waitcnt lgkmcnt(3)
ds_read_b128 v[140:143], v162 offset:4352
s_add_u32 s42, s36, 1
v_mfma_f32_16x16x32_bf16 v[116:119], a[240:243], v[124:127], 0
s_waitcnt lgkmcnt(3)
ds_read_b128 v[120:123], v162 offset:6144
s_and_b32 s43, s42, 1
v_mfma_f32_16x16x32_bf16 v[112:115], a[244:247], v[128:131], v[112:115]
s_waitcnt lgkmcnt(3)
ds_read_b128 v[124:127], v162 offset:6400
s_min_u32 s42, s42, 63
v_mfma_f32_16x16x32_bf16 v[116:119], a[244:247], v[132:135], v[116:119]
s_waitcnt lgkmcnt(3)
ds_read_b128 v[128:131], v162 offset:8192
s_mul_i32 s43, s43, 40960
v_mfma_f32_16x16x32_bf16 v[112:115], a[248:251], v[136:139], v[112:115]
s_waitcnt lgkmcnt(3)
ds_read_b128 v[132:135], v162 offset:8448
s_add_u32 s57, s43, s58
v_mfma_f32_16x16x32_bf16 v[116:119], a[248:251], v[140:143], v[116:119]
s_waitcnt lgkmcnt(3)
ds_read_b128 v[136:139], v162 offset:10240
s_mul_i32 s42, s42, 40960
v_mfma_f32_16x16x32_bf16 v[112:115], a[252:255], v[120:123], v[112:115]
s_waitcnt lgkmcnt(3)
ds_read_b128 v[140:143], v162 offset:10496
s_add_u32 s46, s50, s42
s_addc_u32 s47, s51, 0
v_mfma_f32_16x16x32_bf16 v[116:119], a[252:255], v[124:127], v[116:119]
s_waitcnt lgkmcnt(3)
ds_read_b128 v[120:123], v162 offset:12288
s_add_u32 m0, s57, 0
v_mfma_f32_16x16x32_bf16 v[112:115], v[192:195], v[128:131], v[112:115]
s_waitcnt lgkmcnt(3)
ds_read_b128 v[124:127], v162 offset:12544
global_load_lds_dwordx4 v152, s[46:47]
v_mfma_f32_16x16x32_bf16 v[116:119], v[192:195], v[132:135], v[116:119]
s_waitcnt lgkmcnt(3)
ds_read_b128 v[128:131], v162 offset:14336
s_add_u32 s46, s46, 1024
s_addc_u32 s47, s47, 0
v_mfma_f32_16x16x32_bf16 v[112:115], v[196:199], v[136:139], v[112:115]
s_waitcnt lgkmcnt(3)
ds_read_b128 v[132:135], v162 offset:14592
s_add_u32 m0, s57, 1024
v_mfma_f32_16x16x32_bf16 v[116:119], v[196:199], v[140:143], v[116:119]
s_waitcnt lgkmcnt(3)
ds_read_b128 v[136:139], v162 offset:16384
global_load_lds_dwordx4 v152, s[46:47]
v_mfma_f32_16x16x32_bf16 v[112:115], v[200:203], v[120:123], v[112:115]
s_waitcnt lgkmcnt(3)
ds_read_b128 v[140:143], v162 offset:16640
s_add_u32 s46, s46, 1024
s_addc_u32 s47, s47, 0
v_mfma_f32_16x16x32_bf16 v[116:119], v[200:203], v[124:127], v[116:119]
s_waitcnt lgkmcnt(3)
ds_read_b128 v[120:123], v162 offset:18432
s_add_u32 m0, s57, 2048
v_mfma_f32_16x16x32_bf16 v[112:115], v[204:207], v[128:131], v[112:115]
s_waitcnt lgkmcnt(3)
ds_read_b128 v[124:127], v162 offset:18688
global_load_lds_dwordx4 v152, s[46:47]
v_mfma_f32_16x16x32_bf16 v[116:119], v[204:207], v[132:135], v[116:119]
s_waitcnt lgkmcnt(3)
ds_read_b128 v[128:131], v162 offset:20480
s_add_u32 s46, s46, 1024
s_addc_u32 s47, s47, 0
v_mfma_f32_16x16x32_bf16 v[112:115], v[208:211], v[136:139], v[112:115]
s_waitcnt lgkmcnt(3)
ds_read_b128 v[132:135], v162 offset:20736
s_add_u32 m0, s57, 3072
v_mfma_f32_16x16x32_bf16 v[116:119], v[208:211], v[140:143], v[116:119]
s_waitcnt lgkmcnt(3)
ds_read_b128 v[136:139], v162 offset:22528
global_load_lds_dwordx4 v152, s[46:47]
v_mfma_f32_16x16x32_bf16 v[112:115], v[212:215], v[120:123], v[112:115]
s_waitcnt lgkmcnt(3)
ds_read_b128 v[140:143], v162 offset:22784
s_add_u32 s46, s46, 1024
s_addc_u32 s47, s47, 0
v_mfma_f32_16x16x32_bf16 v[116:119], v[212:215], v[124:127], v[116:119]
s_waitcnt lgkmcnt(3)
ds_read_b128 v[120:123], v162 offset:24576
s_add_u32 m0, s57, 4096
v_mfma_f32_16x16x32_bf16 v[112:115], v[216:219], v[128:131], v[112:115]
s_waitcnt lgkmcnt(3)
ds_read_b128 v[124:127], v162 offset:24832
global_load_lds_dwordx4 v152, s[46:47]
v_mfma_f32_16x16x32_bf16 v[116:119], v[216:219], v[132:135], v[116:119]
s_waitcnt lgkmcnt(3)
ds_read_b128 v[128:131], v162 offset:26624
s_add_u32 s46, s46, 1024
s_addc_u32 s47, s47, 0
v_mfma_f32_16x16x32_bf16 v[112:115], v[220:223], v[136:139], v[112:115]
s_waitcnt lgkmcnt(3)
ds_read_b128 v[132:135], v162 offset:26880
s_add_u32 m0, s57, 5120
v_mfma_f32_16x16x32_bf16 v[116:119], v[220:223], v[140:143], v[116:119]
s_waitcnt lgkmcnt(3)
ds_read_b128 v[136:139], v162 offset:28672
global_load_lds_dwordx4 v152, s[46:47]
v_mfma_f32_16x16x32_bf16 v[112:115], v[224:227], v[120:123], v[112:115]
s_waitcnt lgkmcnt(3)
ds_read_b128 v[140:143], v162 offset:28928
s_add_u32 s46, s46, 1024
s_addc_u32 s47, s47, 0
v_mfma_f32_16x16x32_bf16 v[116:119], v[224:227], v[124:127], v[116:119]
s_waitcnt lgkmcnt(3)
ds_read_b128 v[120:123], v162 offset:30720
s_add_u32 m0, s57, 6144
v_mfma_f32_16x16x32_bf16 v[112:115], v[228:231], v[128:131], v[112:115]
s_waitcnt lgkmcnt(3)
ds_read_b128 v[124:127], v162 offset:30976
global_load_lds_dwordx4 v152, s[46:47]
v_mfma_f32_16x16x32_bf16 v[116:119], v[228:231], v[132:135], v[116:119]
s_waitcnt lgkmcnt(3)
ds_read_b128 v[128:131], v162 offset:32768
s_add_u32 s46, s46, 1024
s_addc_u32 s47, s47, 0
v_mfma_f32_16x16x32_bf16 v[112:115], v[232:235], v[136:139], v[112:115]
s_waitcnt lgkmcnt(3)
ds_read_b128 v[132:135], v162 offset:33024
s_add_u32 m0, s57, 7168
v_mfma_f32_16x16x32_bf16 v[116:119], v[232:235], v[140:143], v[116:119]
s_waitcnt lgkmcnt(3)
ds_read_b128 v[136:139], v162 offset:34816
global_load_lds_dwordx4 v152, s[46:47]
v_mfma_f32_16x16x32_bf16 v[112:115], v[236:239], v[120:123], v[112:115]
s_waitcnt lgkmcnt(3)
ds_read_b128 v[140:143], v162 offset:35072
s_add_u32 s46, s46, 1024
s_addc_u32 s47, s47, 0
v_mfma_f32_16x16x32_bf16 v[116:119], v[236:239], v[124:127], v[116:119]
s_waitcnt lgkmcnt(3)
ds_read_b128 v[120:123], v162 offset:36864
s_add_u32 m0, s57, 8192
v_mfma_f32_16x16x32_bf16 v[112:115], v[240:243], v[128:131], v[112:115]
s_waitcnt lgkmcnt(3)
ds_read_b128 v[124:127], v162 offset:37120
global_load_lds_dwordx4 v152, s[46:47]
v_mfma_f32_16x16x32_bf16 v[116:119], v[240:243], v[132:135], v[116:119]
s_waitcnt lgkmcnt(3)
ds_read_b128 v[128:131], v162 offset:38912
s_add_u32 s46, s46, 1024
s_addc_u32 s47, s47, 0
v_mfma_f32_16x16x32_bf16 v[112:115], v[244:247], v[136:139], v[112:115]
s_waitcnt lgkmcnt(3)
ds_read_b128 v[132:135], v162 offset:39168
s_add_u32 m0, s57, 9216
v_mfma_f32_16x16x32_bf16 v[116:119], v[244:247], v[140:143], v[116:119]
s_waitcnt lgkmcnt(3)
global_load_lds_dwordx4 v152, s[46:47]
v_mfma_f32_16x16x32_bf16 v[112:115], v[248:251], v[120:123], v[112:115]
s_waitcnt lgkmcnt(2)
v_mfma_f32_16x16x32_bf16 v[116:119], v[248:251], v[124:127], v[116:119]
s_waitcnt lgkmcnt(1)
v_mfma_f32_16x16x32_bf16 v[112:115], v[252:255], v[128:131], v[112:115]
s_waitcnt lgkmcnt(0)
v_mfma_f32_16x16x32_bf16 v[116:119], v[252:255], v[132:135], v[116:119]
ds_write_b128 v167, v[80:83] offset:0
ds_write_b128 v167, v[84:87] offset:1024
s_nop 3
v_cvt_pk_bf16_f32 v112, v112, v113
v_cvt_pk_bf16_f32 v113, v114, v115
ds_write_b64 v157, v[112:113] offset:0
v_cvt_pk_bf16_f32 v116, v116, v117
v_cvt_pk_bf16_f32 v117, v118, v119
ds_write_b64 v157, v[116:117] offset:256
s_waitcnt lgkmcnt(0)
s_barrier
ds_read_b128 v[32:35], v158 offset:0
ds_read_b128 v[36:39], v158 offset:1024
ds_read_b128 v[40:43], v158 offset:2048
ds_read_b128 v[44:47], v158 offset:3072
ds_read_b128 v[48:51], v158 offset:4096
ds_read_b128 v[52:55], v158 offset:5120
ds_read_b128 v[56:59], v158 offset:6144
ds_read_b128 v[60:63], v158 offset:7168
ds_read_b128 v[64:67], v158 offset:8192
ds_read_b128 v[68:71], v158 offset:9216
ds_read_b128 v[72:75], v158 offset:10240
ds_read_b128 v[76:79], v158 offset:11264
s_waitcnt lgkmcnt(11)
v_mfma_f32_32x32x16_bf16 v[0:15], v[32:35], a[0:3], 0
s_waitcnt lgkmcnt(10)
v_mfma_f32_32x32x16_bf16 v[0:15], v[36:39], a[4:7], v[0:15]
s_waitcnt lgkmcnt(9)
v_mfma_f32_32x32x16_bf16 v[0:15], v[40:43], a[8:11], v[0:15]
s_waitcnt lgkmcnt(8)
v_mfma_f32_32x32x16_bf16 v[0:15], v[44:47], a[12:15], v[0:15]
s_waitcnt lgkmcnt(7)
s_waitcnt vmcnt(10)
v_cmp_gt_u32 s[78:79], s59, v165
v_mov_b32 v163, s36
buffer_store_dword v163, v160, s[96:99], 0 offen offset:0 sc1
s_add_u32 s38, s36, 1
v_mfma_f32_32x32x16_bf16 v[0:15], v[48:51], a[16:19], v[0:15]
s_waitcnt lgkmcnt(6)
s_min_u32 s38, s38, 63
s_lshl_b32 s38, s38, 5
v_mfma_f32_32x32x16_bf16 v[0:15], v[52:55], a[20:23], v[0:15]
s_waitcnt lgkmcnt(5)
s_add_u32 s38, s38, s54
s_mul_i32 s38, s38, 12288
v_mfma_f32_32x32x16_bf16 v[0:15], v[56:59], a[24:27], v[0:15]
s_waitcnt lgkmcnt(4)
s_add_u32 s38, s38, 58720256
s_cmp_eq_u64 s[78:79], 0
s_cselect_b32 s38, s38, 0x800000
v_mfma_f32_32x32x16_bf16 v[0:15], v[60:63], a[28:31], v[0:15]
s_waitcnt lgkmcnt(3)
buffer_load_dwordx4 v[80:83], v166, s[96:99], s38 offen offset:0 sc1
buffer_load_dwordx4 v[84:87], v166, s[96:99], s38 offen offset:1024 sc1
v_mfma_f32_32x32x16_bf16 v[0:15], v[64:67], a[32:35], v[0:15]
s_waitcnt lgkmcnt(2)
v_mfma_f32_32x32x16_bf16 v[0:15], v[68:71], a[36:39], v[0:15]
s_waitcnt lgkmcnt(1)
v_mfma_f32_32x32x16_bf16 v[0:15], v[72:75], a[40:43], v[0:15]
s_waitcnt lgkmcnt(0)
v_mfma_f32_32x32x16_bf16 v[0:15], v[76:79], a[44:47], v[0:15]
v_mfma_f32_32x32x16_bf16 v[16:31], v[32:35], a[48:51], 0
v_mfma_f32_32x32x16_bf16 v[16:31], v[36:39], a[52:55], v[16:31]
s_nop 9
v_cvt_pk_bf16_f32 v144, v0, v1
v_cvt_pk_bf16_f32 v145, v2, v3
v_mfma_f32_32x32x16_bf16 v[16:31], v[40:43], a[56:59], v[16:31]
v_cvt_pk_bf16_f32 v146, v4, v5
v_cvt_pk_bf16_f32 v147, v6, v7
v_mfma_f32_32x32x16_bf16 v[16:31], v[44:47], a[60:63], v[16:31]
s_add_u32 s56, s37, 0
buffer_store_dwordx4 v[144:147], v159, s[96:99], s56 offen offset:0 sc1
v_cvt_pk_bf16_f32 v148, v8, v9
v_cvt_pk_bf16_f32 v149, v10, v11
v_mfma_f32_32x32x16_bf16 v[16:31], v[48:51], a[64:67], v[16:31]
v_cvt_pk_bf16_f32 v150, v12, v13
v_cvt_pk_bf16_f32 v151, v14, v15
v_mfma_f32_32x32x16_bf16 v[16:31], v[52:55], a[68:71], v[16:31]
buffer_store_dwordx4 v[148:151], v159, s[96:99], s56 offen offset:512 sc1
v_mfma_f32_32x32x16_bf16 v[16:31], v[56:59], a[72:75], v[16:31]
v_mfma_f32_32x32x16_bf16 v[16:31], v[60:63], a[76:79], v[16:31]
v_mfma_f32_32x32x16_bf16 v[16:31], v[64:67], a[80:83], v[16:31]
v_mfma_f32_32x32x16_bf16 v[16:31], v[68:71], a[84:87], v[16:31]
v_mfma_f32_32x32x16_bf16 v[16:31], v[72:75], a[88:91], v[16:31]
v_mfma_f32_32x32x16_bf16 v[16:31], v[76:79], a[92:95], v[16:31]
v_mfma_f32_32x32x16_bf16 v[0:15], v[32:35], a[96:99], 0
v_mfma_f32_32x32x16_bf16 v[0:15], v[36:39], a[100:103], v[0:15]
s_nop 9
v_cvt_pk_bf16_f32 v144, v16, v17
v_cvt_pk_bf16_f32 v145, v18, v19
v_mfma_f32_32x32x16_bf16 v[0:15], v[40:43], a[104:107], v[0:15]
v_cvt_pk_bf16_f32 v146, v20, v21
v_cvt_pk_bf16_f32 v147, v22, v23
v_mfma_f32_32x32x16_bf16 v[0:15], v[44:47], a[108:111], v[0:15]
s_add_u32 s56, s37, 262144
buffer_store_dwordx4 v[144:147], v159, s[96:99], s56 offen offset:0 sc1
v_cvt_pk_bf16_f32 v148, v24, v25
v_cvt_pk_bf16_f32 v149, v26, v27
v_mfma_f32_32x32x16_bf16 v[0:15], v[48:51], a[112:115], v[0:15]
v_cvt_pk_bf16_f32 v150, v28, v29
v_cvt_pk_bf16_f32 v151, v30, v31
v_mfma_f32_32x32x16_bf16 v[0:15], v[52:55], a[116:119], v[0:15]
buffer_store_dwordx4 v[148:151], v159, s[96:99], s56 offen offset:512 sc1
v_mfma_f32_32x32x16_bf16 v[0:15], v[56:59], a[120:123], v[0:15]
v_mfma_f32_32x32x16_bf16 v[0:15], v[60:63], a[124:127], v[0:15]
v_mfma_f32_32x32x16_bf16 v[0:15], v[64:67], a[128:131], v[0:15]
v_mfma_f32_32x32x16_bf16 v[0:15], v[68:71], a[132:135], v[0:15]
v_mfma_f32_32x32x16_bf16 v[0:15], v[72:75], a[136:139], v[0:15]
v_mfma_f32_32x32x16_bf16 v[0:15], v[76:79], a[140:143], v[0:15]
v_mfma_f32_32x32x16_bf16 v[16:31], v[32:35], a[144:147], 0
v_mfma_f32_32x32x16_bf16 v[16:31], v[36:39], a[148:151], v[16:31]
s_nop 9
v_cvt_pk_bf16_f32 v144, v0, v1
v_cvt_pk_bf16_f32 v145, v2, v3
v_mfma_f32_32x32x16_bf16 v[16:31], v[40:43], a[152:155], v[16:31]
v_cvt_pk_bf16_f32 v146, v4, v5
v_cvt_pk_bf16_f32 v147, v6, v7
v_mfma_f32_32x32x16_bf16 v[16:31], v[44:47], a[156:159], v[16:31]
s_add_u32 s56, s37, 524288
buffer_store_dwordx4 v[144:147], v159, s[96:99], s56 offen offset:0 sc1
v_cvt_pk_bf16_f32 v148, v8, v9
v_cvt_pk_bf16_f32 v149, v10, v11
v_mfma_f32_32x32x16_bf16 v[16:31], v[48:51], a[160:163], v[16:31]
v_cvt_pk_bf16_f32 v150, v12, v13
v_cvt_pk_bf16_f32 v151, v14, v15
v_mfma_f32_32x32x16_bf16 v[16:31], v[52:55], a[164:167], v[16:31]
buffer_store_dwordx4 v[148:151], v159, s[96:99], s56 offen offset:512 sc1
v_mfma_f32_32x32x16_bf16 v[16:31], v[56:59], a[168:171], v[16:31]
v_mfma_f32_32x32x16_bf16 v[16:31], v[60:63], a[172:175], v[16:31]
v_mfma_f32_32x32x16_bf16 v[16:31], v[64:67], a[176:179], v[16:31]
v_mfma_f32_32x32x16_bf16 v[16:31], v[68:71], a[180:183], v[16:31]
v_mfma_f32_32x32x16_bf16 v[16:31], v[72:75], a[184:187], v[16:31]
v_mfma_f32_32x32x16_bf16 v[16:31], v[76:79], a[188:191], v[16:31]
v_mfma_f32_32x32x16_bf16 v[0:15], v[32:35], a[192:195], 0
v_mfma_f32_32x32x16_bf16 v[0:15], v[36:39], a[196:199], v[0:15]
s_nop 9
v_cvt_pk_bf16_f32 v144, v16, v17
v_cvt_pk_bf16_f32 v145, v18, v19
v_mfma_f32_32x32x16_bf16 v[0:15], v[40:43], a[200:203], v[0:15]
v_cvt_pk_bf16_f32 v146, v20, v21
v_cvt_pk_bf16_f32 v147, v22, v23
v_mfma_f32_32x32x16_bf16 v[0:15], v[44:47], a[204:207], v[0:15]
s_add_u32 s56, s37, 786432
buffer_store_dwordx4 v[144:147], v159, s[96:99], s56 offen offset:0 sc1
v_cvt_pk_bf16_f32 v148, v24, v25
v_cvt_pk_bf16_f32 v149, v26, v27
v_mfma_f32_32x32x16_bf16 v[0:15], v[48:51], a[208:211], v[0:15]
v_cvt_pk_bf16_f32 v150, v28, v29
v_cvt_pk_bf16_f32 v151, v30, v31
v_mfma_f32_32x32x16_bf16 v[0:15], v[52:55], a[212:215], v[0:15]
buffer_store_dwordx4 v[148:151], v159, s[96:99], s56 offen offset:512 sc1
v_mfma_f32_32x32x16_bf16 v[0:15], v[56:59], a[216:219], v[0:15]
v_mfma_f32_32x32x16_bf16 v[0:15], v[60:63], a[220:223], v[0:15]
v_mfma_f32_32x32x16_bf16 v[0:15], v[64:67], a[224:227], v[0:15]
v_mfma_f32_32x32x16_bf16 v[0:15], v[68:71], a[228:231], v[0:15]
v_mfma_f32_32x32x16_bf16 v[0:15], v[72:75], a[232:235], v[0:15]
v_mfma_f32_32x32x16_bf16 v[0:15], v[76:79], a[236:239], v[0:15]
s_nop 11
v_cvt_pk_bf16_f32 v144, v0, v1
v_cvt_pk_bf16_f32 v145, v2, v3
v_cvt_pk_bf16_f32 v146, v4, v5
v_cvt_pk_bf16_f32 v147, v6, v7
s_add_u32 s56, s37, 1048576
buffer_store_dwordx4 v[144:147], v159, s[96:99], s56 offen offset:0 sc1
v_cvt_pk_bf16_f32 v148, v8, v9
v_cvt_pk_bf16_f32 v149, v10, v11
v_cvt_pk_bf16_f32 v150, v12, v13
v_cvt_pk_bf16_f32 v151, v14, v15
buffer_store_dwordx4 v[148:151], v159, s[96:99], s56 offen offset:512 sc1
s_waitcnt vmcnt(10)
s_cmp_eq_u64 s[78:79], 0
s_cbranch_scc1 PC_E
s_waitcnt vmcnt(0)
s_add_u32 s42, s36, 1
v_mov_b32 v163, s42
buffer_store_dword v163, v160, s[96:99], 0 offen offset:0 sc1
s_nop 0
PC_F:
buffer_load_dword v165, v161, s[96:99], 0 offen offset:0 sc1
s_waitcnt vmcnt(0)
v_cmp_gt_u32 vcc, s59, v165
s_cbranch_vccz PC_FK
s_sleep 3
s_sub_u32 s41, s41, 1
s_cbranch_scc0 PC_F
s_mov_b32 s41, 0
PC_FK:
s_add_u32 s38, s36, 1
s_min_u32 s38, s38, 63
s_lshl_b32 s38, s38, 5
s_add_u32 s38, s38, s54
s_mul_i32 s38, s38, 12288
s_add_u32 s38, s38, 58720256
buffer_load_dwordx4 v[80:83], v166, s[96:99], s38 offen offset:0 sc1
buffer_load_dwordx4 v[84:87], v166, s[96:99], s38 offen offset:1024 sc1
s_waitcnt vmcnt(0)
PC_E:
s_add_u32 s36, s36, 1
s_sub_u32 s42, s36, 3
s_cmp_lt_u32 s42, 61
s_cbranch_scc1 PC_T
s_waitcnt vmcnt(0)
v_mov_b32 v163, s36
buffer_store_dword v163, v160, s[96:99], 0 offen offset:0 sc1
s_cmp_lt_u32 s36, 64
s_cbranch_scc1 PC_T
s_endpgm
ROLE_S3:
s_mov_b64 s[48:49], %0
s_mov_b64 s[50:51], %1
s_mov_b64 s[52:53], %2
s_mov_b32 s54, %3
s_mov_b32 s40, %4
s_mov_b32 s39, %5
s_mov_b32 s41, 0x200000
s_mov_b32 s36, 0
s_lshl_b32 s55, s40, 10
v_mbcnt_lo_u32_b32 v31, -1, 0
v_mbcnt_hi_u32_b32 v31, -1, v31
s_lshl_b32 s42, s40, 8
s_add_u32 s42, s42, 147456
v_lshl_add_u32 v31, v31, 2, s42
ds_read_b32 v196, v31 offset:0
ds_read_b32 v197, v31 offset:1024
ds_read_b32 v198, v31 offset:2048
ds_read_b32 v199, v31 offset:3072
ds_read_b32 v200, v31 offset:4096
ds_read_b32 v201, v31 offset:5120
ds_read_b32 v202, v31 offset:6144
ds_read_b32 v203, v31 offset:7168
s_waitcnt lgkmcnt(0)
s_memrealtime s[94:95]
s_waitcnt lgkmcnt(0)
s_mov_b32 s92, s94
s_add_u32 s20, s48, 0x0
s_addc_u32 s21, s49, 0
s_and_b32 s21, s21, 0xffff
s_mov_b32 s22, 0x400000
s_mov_b32 s23, 0x20000
s_add_u32 s96, s48, 0x0
s_addc_u32 s97, s49, 0
s_and_b32 s97, s97, 0xffff
s_mov_b32 s98, 0xfe00000
s_mov_b32 s99, 0x20000
s_add_u32 s24, s48, 0xa000000
s_addc_u32 s25, s49, 0
s_and_b32 s25, s25, 0xffff
s_mov_b32 s26, 0x4000000
s_mov_b32 s27, 0x20000
s_mov_b64 s[28:29], s[50:51]
s_and_b32 s29, s29, 0xffff
s_mov_b32 s30, 2621440
s_mov_b32 s31, 0x20000
s_lshl_b32 s42, s40, 12
v_add_u32 v208, s42, v196
v_add_u32 v209, s55, v196
s_nop 1
S3_V0:
buffer_load_dword v206, v201, s[96:99], 0 offen offset:0 sc1
s_waitcnt vmcnt(0)
v_cmp_gt_u32 vcc, 1, v206
s_cbranch_vccz S3_V0K
s_sleep 3
s_sub_u32 s41, s41, 1
s_cbranch_scc0 S3_V0
s_mov_b32 s41, 0
S3_V0K:
s_add_u32 s42, s36, 0
s_mul_i32 s42, s42, 1310720
buffer_load_dwordx4 v[80:83], v197, s[96:99], s42 offen offset:0 sc1
buffer_load_dwordx4 v[84:87], v197, s[96:99], s42 offen offset:512 sc1
buffer_load_dwordx4 v[88:91], v197, s[96:99], s42 offen offset:1024 sc1
buffer_load_dwordx4 v[92:95], v197, s[96:99], s42 offen offset:1536 sc1
buffer_load_dwordx4 v[96:99], v197, s[96:99], s42 offen offset:2048 sc1
buffer_load_dwordx4 v[100:103], v197, s[96:99], s42 offen offset:2560 sc1
buffer_load_dwordx4 v[104:107], v197, s[96:99], s42 offen offset:3072 sc1
buffer_load_dwordx4 v[108:111], v197, s[96:99], s42 offen offset:3584 sc1
s_add_u32 s42, s42, 4096
buffer_load_dwordx4 v[112:115], v197, s[96:99], s42 offen offset:0 sc1
buffer_load_dwordx4 v[116:119], v197, s[96:99], s42 offen offset:512 sc1
buffer_load_dwordx4 v[120:123], v197, s[96:99], s42 offen offset:1024 sc1
buffer_load_dwordx4 v[124:127], v197, s[96:99], s42 offen offset:1536 sc1
buffer_load_dwordx4 v[128:131], v197, s[96:99], s42 offen offset:2048 sc1
buffer_load_dwordx4 v[132:135], v197, s[96:99], s42 offen offset:2560 sc1
buffer_load_dwordx4 v[136:139], v197, s[96:99], s42 offen offset:3072 sc1
buffer_load_dwordx4 v[140:143], v197, s[96:99], s42 offen offset:3584 sc1
buffer_load_dword v207, v202, s[96:99], 0 offen offset:0 sc1
S3_T:
s_add_u32 s59, s36, 1
s_waitcnt vmcnt(0)
v_cmp_gt_u32 vcc, s59, v207
s_cbranch_vccz S3_PK
s_nop 3
S3_P:
buffer_load_dword v207, v202, s[96:99], 0 offen offset:0 sc1
s_waitcnt vmcnt(0)
v_cmp_gt_u32 vcc, s59, v207
s_cbranch_vccz S3_PK
s_sleep 3
s_sub_u32 s41, s41, 1
s_cbranch_scc0 S3_P
s_mov_b32 s41, 0
S3_PK:
s_mul_i32 s42, s36, 81920
s_lshl_b32 s43, s40, 14
s_add_u32 s57, s42, s43
s_add_u32 s57, s57, 234881024
s_lshl_b32 s43, s40, 12
s_add_u32 s58, s42, s43
s_add_u32 s58, s58, 234946560
buffer_load_dwordx4 v[0:3], v196, s[96:99], s57 offen offset:0 sc1
buffer_load_dwordx4 v[4:7], v196, s[96:99], s57 offen offset:1024 sc1
buffer_load_dwordx4 v[8:11], v196, s[96:99], s57 offen offset:2048 sc1
buffer_load_dwordx4 v[12:15], v196, s[96:99], s57 offen offset:3072 sc1
s_add_u32 s57, s57, 4096
buffer_load_dwordx4 v[16:19], v196, s[96:99], s57 offen offset:0 sc1
buffer_load_dwordx4 v[20:23], v196, s[96:99], s57 offen offset:1024 sc1
buffer_load_dwordx4 v[24:27], v196, s[96:99], s57 offen offset:2048 sc1
buffer_load_dwordx4 v[28:31], v196, s[96:99], s57 offen offset:3072 sc1
s_add_u32 s57, s57, 4096
buffer_load_dwordx4 v[32:35], v196, s[96:99], s57 offen offset:0 sc1
buffer_load_dwordx4 v[36:39], v196, s[96:99], s57 offen offset:1024 sc1
buffer_load_dwordx4 v[40:43], v196, s[96:99], s57 offen offset:2048 sc1
buffer_load_dwordx4 v[44:47], v196, s[96:99], s57 offen offset:3072 sc1
s_add_u32 s57, s57, 4096
buffer_load_dwordx4 v[48:51], v196, s[96:99], s57 offen offset:0 sc1
buffer_load_dwordx4 v[52:55], v196, s[96:99], s57 offen offset:1024 sc1
buffer_load_dwordx4 v[56:59], v196, s[96:99], s57 offen offset:2048 sc1
buffer_load_dwordx4 v[60:63], v196, s[96:99], s57 offen offset:3072 sc1
buffer_load_dwordx4 v[64:67], v196, s[96:99], s58 offen offset:0 sc1
buffer_load_dwordx4 v[68:71], v196, s[96:99], s58 offen offset:1024 sc1
buffer_load_dwordx4 v[72:75], v196, s[96:99], s58 offen offset:2048 sc1
buffer_load_dwordx4 v[76:79], v196, s[96:99], s58 offen offset:3072 sc1
s_add_u32 s42, s36, 0
s_lshl_b32 s37, s42, 20
s_lshr_b32 s43, s37, 8
s_xor_b32 s43, s43, s39
s_and_b32 s43, s43, 8192
s_or_b32 s37, s37, s43
buffer_load_dwordx4 v[144:147], v198, s[24:27], s37 offen offset:0 sc1
buffer_load_dwordx4 v[148:151], v198, s[24:27], s37 offen offset:512 sc1
buffer_load_dwordx4 v[152:155], v198, s[24:27], s37 offen offset:1024 sc1
buffer_load_dwordx4 v[156:159], v198, s[24:27], s37 offen offset:1536 sc1
s_mul_i32 s42, s36, 40960
buffer_load_dwordx2 v[192:193], v199, s[28:31], s42 offen offset:0
buffer_load_dword v206, v201, s[96:99], 0 offen offset:0 sc1
s_and_b32 s42, s36, 1
s_lshl_b32 s42, s42, 14
v_add_u32 v210, s42, v208
v_add_u32 v211, s42, v209
s_waitcnt vmcnt(25)
v_mfma_f32_32x32x16_bf16 v[160:175], v[80:83], v[0:3], 0
s_waitcnt vmcnt(24)
v_mfma_f32_32x32x16_bf16 v[160:175], v[84:87], v[4:7], v[160:175]
s_waitcnt vmcnt(23)
v_mfma_f32_32x32x16_bf16 v[160:175], v[88:91], v[8:11], v[160:175]
s_waitcnt vmcnt(22)
v_mfma_f32_32x32x16_bf16 v[160:175], v[92:95], v[12:15], v[160:175]
s_waitcnt vmcnt(21)
v_mfma_f32_32x32x16_bf16 v[160:175], v[96:99], v[16:19], v[160:175]
s_waitcnt vmcnt(20)
v_mfma_f32_32x32x16_bf16 v[160:175], v[100:103], v[20:23], v[160:175]
s_waitcnt vmcnt(19)
v_mfma_f32_32x32x16_bf16 v[160:175], v[104:107], v[24:27], v[160:175]
s_waitcnt vmcnt(18)
v_mfma_f32_32x32x16_bf16 v[160:175], v[108:111], v[28:31], v[160:175]
s_waitcnt vmcnt(17)
v_mfma_f32_32x32x16_bf16 v[160:175], v[112:115], v[32:35], v[160:175]
s_waitcnt vmcnt(16)
v_mfma_f32_32x32x16_bf16 v[160:175], v[116:119], v[36:39], v[160:175]
s_waitcnt vmcnt(15)
v_mfma_f32_32x32x16_bf16 v[160:175], v[120:123], v[40:43], v[160:175]
s_waitcnt vmcnt(14)
v_mfma_f32_32x32x16_bf16 v[160:175], v[124:127], v[44:47], v[160:175]
s_waitcnt vmcnt(13)
v_mfma_f32_32x32x16_bf16 v[160:175], v[128:131], v[48:51], v[160:175]
s_waitcnt vmcnt(12)
v_mfma_f32_32x32x16_bf16 v[160:175], v[132:135], v[52:55], v[160:175]
s_waitcnt vmcnt(11)
v_mfma_f32_32x32x16_bf16 v[160:175], v[136:139], v[56:59], v[160:175]
s_waitcnt vmcnt(10)
v_mfma_f32_32x32x16_bf16 v[160:175], v[140:143], v[60:63], v[160:175]
s_waitcnt vmcnt(5)
v_max3_u32 v204, v144, v145, v146
s_waitcnt vmcnt(4)
v_max3_u32 v204, v204, v147, v148
v_max3_u32 v204, v204, v149, v150
s_waitcnt vmcnt(3)
v_max3_u32 v204, v204, v151, v152
v_max3_u32 v204, v204, v153, v154
s_waitcnt vmcnt(2)
v_max3_u32 v204, v204, v155, v156
v_max3_u32 v204, v204, v157, v158
v_max_u32 v204, v204, v159
v_cmp_eq_u32 vcc, -1, v204
s_cbranch_vccz S3_UK
S3_U:
buffer_load_dwordx4 v[144:147], v198, s[24:27], s37 offen offset:0 sc1
buffer_load_dwordx4 v[148:151], v198, s[24:27], s37 offen offset:512 sc1
buffer_load_dwordx4 v[152:155], v198, s[24:27], s37 offen offset:1024 sc1
buffer_load_dwordx4 v[156:159], v198, s[24:27], s37 offen offset:1536 sc1
s_waitcnt vmcnt(3)
v_max3_u32 v204, v144, v145, v146
s_waitcnt vmcnt(2)
v_max3_u32 v204, v204, v147, v148
v_max3_u32 v204, v204, v149, v150
s_waitcnt vmcnt(1)
v_max3_u32 v204, v204, v151, v152
v_max3_u32 v204, v204, v153, v154
s_waitcnt vmcnt(0)
v_max3_u32 v204, v204, v155, v156
v_max3_u32 v204, v204, v157, v158
v_max_u32 v204, v204, v159
v_cmp_eq_u32 vcc, -1, v204
s_cbranch_vccz S3_UK
s_sub_u32 s41, s41, 1
s_cbranch_scc0 S3_U
s_mov_b32 s41, 0
S3_UK:
v_mfma_f32_32x32x16_bf16 v[160:175], v[144:147], v[64:67], v[160:175]
v_mfma_f32_32x32x16_bf16 v[160:175], v[148:151], v[68:71], v[160:175]
v_mfma_f32_32x32x16_bf16 v[160:175], v[152:155], v[72:75], v[160:175]
v_mfma_f32_32x32x16_bf16 v[160:175], v[156:159], v[76:79], v[160:175]
s_waitcnt vmcnt(1)
s_cmp_eq_u32 s36, 63
s_cbranch_scc1 S3_NV
s_add_u32 s59, s36, 2
s_waitcnt vmcnt(0)
v_cmp_gt_u32 vcc, s59, v206
s_cbranch_vccz S3_VK
s_nop 4
S3_V:
buffer_load_dword v206, v201, s[96:99], 0 offen offset:0 sc1
s_waitcnt vmcnt(0)
v_cmp_gt_u32 vcc, s59, v206
s_cbranch_vccz S3_VK
s_sleep 3
s_sub_u32 s41, s41, 1
s_cbranch_scc0 S3_V
s_mov_b32 s41, 0
S3_VK:
s_add_u32 s42, s36, 1
s_mul_i32 s42, s42, 1310720
buffer_load_dwordx4 v[80:83], v197, s[96:99], s42 offen offset:0 sc1
buffer_load_dwordx4 v[84:87], v197, s[96:99], s42 offen offset:512 sc1
buffer_load_dwordx4 v[88:91], v197, s[96:99], s42 offen offset:1024 sc1
buffer_load_dwordx4 v[92:95], v197, s[96:99], s42 offen offset:1536 sc1
buffer_load_dwordx4 v[96:99], v197, s[96:99], s42 offen offset:2048 sc1
buffer_load_dwordx4 v[100:103], v197, s[96:99], s42 offen offset:2560 sc1
buffer_load_dwordx4 v[104:107], v197, s[96:99], s42 offen offset:3072 sc1
buffer_load_dwordx4 v[108:111], v197, s[96:99], s42 offen offset:3584 sc1
s_add_u32 s42, s42, 4096
buffer_load_dwordx4 v[112:115], v197, s[96:99], s42 offen offset:0 sc1
buffer_load_dwordx4 v[116:119], v197, s[96:99], s42 offen offset:512 sc1
buffer_load_dwordx4 v[120:123], v197, s[96:99], s42 offen offset:1024 sc1
buffer_load_dwordx4 v[124:127], v197, s[96:99], s42 offen offset:1536 sc1
buffer_load_dwordx4 v[128:131], v197, s[96:99], s42 offen offset:2048 sc1
buffer_load_dwordx4 v[132:135], v197, s[96:99], s42 offen offset:2560 sc1
buffer_load_dwordx4 v[136:139], v197, s[96:99], s42 offen offset:3072 sc1
buffer_load_dwordx4 v[140:143], v197, s[96:99], s42 offen offset:3584 sc1
S3_NV:
s_nop 8
ds_write_b128 v210, v[160:163] offset:0
ds_write_b128 v210, v[164:167] offset:1024
ds_write_b128 v210, v[168:171] offset:2048
ds_write_b128 v210, v[172:175] offset:3072
s_waitcnt lgkmcnt(0)
s_barrier
ds_read_b128 v[176:179], v211 offset:0
ds_read_b128 v[180:183], v211 offset:4096
ds_read_b128 v[184:187], v211 offset:8192
ds_read_b128 v[188:191], v211 offset:12288
s_waitcnt lgkmcnt(2)
v_add_f32 v176, v176, v180
s_waitcnt lgkmcnt(0)
v_add_f32 v184, v184, v188
v_add_f32 v176, v176, v184
v_add_f32 v177, v177, v181
v_add_f32 v185, v185, v189
v_add_f32 v177, v177, v185
v_add_f32 v178, v178, v182
v_add_f32 v186, v186, v190
v_add_f32 v178, v178, v186
v_add_f32 v179, v179, v183
v_add_f32 v187, v187, v191
v_add_f32 v179, v179, v187
v_lshlrev_b32 v204, 16, v192
v_add_f32 v176, v176, v204
v_and_b32 v204, 0xffff0000, v192
v_add_f32 v177, v177, v204
v_lshlrev_b32 v204, 16, v193
v_add_f32 v178, v178, v204
v_and_b32 v204, 0xffff0000, v193
v_add_f32 v179, v179, v204
v_cvt_pk_bf16_f32 v176, v176, v177
v_cvt_pk_bf16_f32 v177, v178, v179
s_mul_i32 s42, s36, 40960
buffer_store_dwordx2 v[176:177], v200, s[96:99], s42 offen offset:0 sc1
s_add_u32 s36, s36, 1
s_waitcnt vmcnt(0)
v_mov_b32 v204, s36
buffer_store_dword v204, v203, s[96:99], 0 offen offset:0 sc1
buffer_load_dword v207, v202, s[96:99], 0 offen offset:0 sc1
s_cmp_lt_u32 s36, 64
s_cbranch_scc1 S3_T
s_endpgm
ROLE_F1:
s_mov_b64 s[48:49], %0
s_mov_b64 s[50:51], %1
s_mov_b64 s[52:53], %2
s_mov_b32 s54, %3
s_mov_b32 s40, %4
s_mov_b32 s39, %5
s_mov_b32 s41, 0x200000
s_mov_b32 s36, 0
s_lshl_b32 s55, s40, 10
v_mbcnt_lo_u32_b32 v31, -1, 0
v_mbcnt_hi_u32_b32 v31, -1, v31
s_lshl_b32 s42, s40, 8
s_add_u32 s42, s42, 147456
v_lshl_add_u32 v31, v31, 2, s42
ds_read_b32 v124, v31 offset:0
ds_read_b32 v125, v31 offset:1024
ds_read_b32 v126, v31 offset:2048
ds_read_b32 v127, v31 offset:3072
ds_read_b32 v128, v31 offset:4096
ds_read_b32 v129, v31 offset:5120
ds_read_b32 v138, v31 offset:6144
ds_read_b32 v139, v31 offset:7168
s_waitcnt lgkmcnt(0)
s_memrealtime s[94:95]
s_waitcnt lgkmcnt(0)
s_mov_b32 s92, s94
s_add_u32 s20, s48, 0x0
s_addc_u32 s21, s49, 0
s_and_b32 s21, s21, 0xffff
s_mov_b32 s22, 0x400000
s_mov_b32 s23, 0x20000
s_add_u32 s96, s48, 0x0
s_addc_u32 s97, s49, 0
s_and_b32 s97, s97, 0xffff
s_mov_b32 s98, 0xfe00000
s_mov_b32 s99, 0x20000
s_mov_b32 s44, 0xc000
s_mov_b32 s45, 0
s_add_u32 m0, s55, 0
s_nop 0
global_load_lds_dwordx4 v[138:139], off
v_lshl_add_u64 v[138:139], v[138:139], 0, s[44:45]
s_add_u32 m0, s55, 4096
s_nop 0
global_load_lds_dwordx4 v[138:139], off
v_lshl_add_u64 v[138:139], v[138:139], 0, s[44:45]
s_add_u32 m0, s55, 8192
s_nop 0
global_load_lds_dwordx4 v[138:139], off
v_lshl_add_u64 v[138:139], v[138:139], 0, s[44:45]
s_add_u32 m0, s55, 12288
s_nop 0
global_load_lds_dwordx4 v[138:139], off
v_lshl_add_u64 v[138:139], v[138:139], 0, s[44:45]
s_add_u32 m0, s55, 16384
s_nop 0
global_load_lds_dwordx4 v[138:139], off
v_lshl_add_u64 v[138:139], v[138:139], 0, s[44:45]
s_add_u32 m0, s55, 20480
s_nop 0
global_load_lds_dwordx4 v[138:139], off
v_lshl_add_u64 v[138:139], v[138:139], 0, s[44:45]
s_add_u32 m0, s55, 24576
s_nop 0
global_load_lds_dwordx4 v[138:139], off
v_lshl_add_u64 v[138:139], v[138:139], 0, s[44:45]
s_add_u32 m0, s55, 28672
s_nop 0
global_load_lds_dwordx4 v[138:139], off
v_lshl_add_u64 v[138:139], v[138:139], 0, s[44:45]
s_add_u32 m0, s55, 32768
s_nop 0
global_load_lds_dwordx4 v[138:139], off
v_lshl_add_u64 v[138:139], v[138:139], 0, s[44:45]
s_add_u32 m0, s55, 36864
s_nop 0
global_load_lds_dwordx4 v[138:139], off
v_lshl_add_u64 v[138:139], v[138:139], 0, s[44:45]
s_add_u32 m0, s55, 40960
s_nop 0
global_load_lds_dwordx4 v[138:139], off
v_lshl_add_u64 v[138:139], v[138:139], 0, s[44:45]
s_add_u32 m0, s55, 45056
s_nop 0
global_load_lds_dwordx4 v[138:139], off
v_lshl_add_u64 v[138:139], v[138:139], 0, s[44:45]
s_add_u32 m0, s55, 49152
s_nop 0
global_load_lds_dwordx4 v[138:139], off
v_lshl_add_u64 v[138:139], v[138:139], 0, s[44:45]
s_add_u32 m0, s55, 53248
s_nop 0
global_load_lds_dwordx4 v[138:139], off
v_lshl_add_u64 v[138:139], v[138:139], 0, s[44:45]
s_add_u32 m0, s55, 57344
s_nop 0
global_load_lds_dwordx4 v[138:139], off
v_lshl_add_u64 v[138:139], v[138:139], 0, s[44:45]
s_add_u32 m0, s55, 61440
s_nop 0
global_load_lds_dwordx4 v[138:139], off
v_lshl_add_u64 v[138:139], v[138:139], 0, s[44:45]
s_add_u32 m0, s55, 65536
s_nop 0
global_load_lds_dwordx4 v[138:139], off
v_lshl_add_u64 v[138:139], v[138:139], 0, s[44:45]
s_add_u32 m0, s55, 69632
s_nop 0
global_load_lds_dwordx4 v[138:139], off
v_lshl_add_u64 v[138:139], v[138:139], 0, s[44:45]
s_add_u32 m0, s55, 73728
s_nop 0
global_load_lds_dwordx4 v[138:139], off
v_lshl_add_u64 v[138:139], v[138:139], 0, s[44:45]
s_add_u32 m0, s55, 77824
s_nop 0
global_load_lds_dwordx4 v[138:139], off
v_lshl_add_u64 v[138:139], v[138:139], 0, s[44:45]
s_add_u32 m0, s55, 81920
s_nop 0
global_load_lds_dwordx4 v[138:139], off
v_lshl_add_u64 v[138:139], v[138:139], 0, s[44:45]
s_add_u32 m0, s55, 86016
s_nop 0
global_load_lds_dwordx4 v[138:139], off
v_lshl_add_u64 v[138:139], v[138:139], 0, s[44:45]
s_add_u32 m0, s55, 90112
s_nop 0
global_load_lds_dwordx4 v[138:139], off
v_lshl_add_u64 v[138:139], v[138:139], 0, s[44:45]
s_add_u32 m0, s55, 94208
s_nop 0
global_load_lds_dwordx4 v[138:139], off
v_lshl_add_u64 v[138:139], v[138:139], 0, s[44:45]
s_add_u32 m0, s55, 98304
s_nop 0
global_load_lds_dwordx4 v[138:139], off
v_lshl_add_u64 v[138:139], v[138:139], 0, s[44:45]
s_add_u32 m0, s55, 102400
s_nop 0
global_load_lds_dwordx4 v[138:139], off
v_lshl_add_u64 v[138:139], v[138:139], 0, s[44:45]
s_add_u32 m0, s55, 106496
s_nop 0
global_load_lds_dwordx4 v[138:139], off
v_lshl_add_u64 v[138:139], v[138:139], 0, s[44:45]
s_add_u32 m0, s55, 110592
s_nop 0
global_load_lds_dwordx4 v[138:139], off
v_lshl_add_u64 v[138:139], v[138:139], 0, s[44:45]
s_add_u32 m0, s55, 114688
s_nop 0
global_load_lds_dwordx4 v[138:139], off
v_lshl_add_u64 v[138:139], v[138:139], 0, s[44:45]
s_add_u32 m0, s55, 118784
s_nop 0
global_load_lds_dwordx4 v[138:139], off
v_lshl_add_u64 v[138:139], v[138:139], 0, s[44:45]
s_add_u32 m0, s55, 122880
s_nop 0
global_load_lds_dwordx4 v[138:139], off
v_lshl_add_u64 v[138:139], v[138:139], 0, s[44:45]
s_add_u32 m0, s55, 126976
s_nop 0
global_load_lds_dwordx4 v[138:139], off
v_lshl_add_u64 v[138:139], v[138:139], 0, s[44:45]
s_waitcnt vmcnt(28)
s_waitcnt lgkmcnt(0)
s_barrier
s_add_u32 m0, s55, 131072
s_nop 0
global_load_lds_dwordx4 v[138:139], off
v_lshl_add_u64 v[138:139], v[138:139], 0, s[44:45]
s_add_u32 m0, s55, 135168
s_nop 0
global_load_lds_dwordx4 v[138:139], off
v_lshl_add_u64 v[138:139], v[138:139], 0, s[44:45]
s_add_u32 m0, s55, 139264
s_nop 0
global_load_lds_dwordx4 v[138:139], off
v_lshl_add_u64 v[138:139], v[138:139], 0, s[44:45]
s_add_u32 m0, s55, 143360
s_nop 0
global_load_lds_dwordx4 v[138:139], off
v_lshl_add_u64 v[138:139], v[138:139], 0, s[44:45]
v_add_u32 v133, 0, v125
ds_read_b64_tr_b16 a[0:1], v133 offset:0
ds_read_b64_tr_b16 a[2:3], v133 offset:1024
ds_read_b64_tr_b16 a[4:5], v133 offset:4096
ds_read_b64_tr_b16 a[6:7], v133 offset:5120
ds_read_b64_tr_b16 a[8:9], v133 offset:8192
ds_read_b64_tr_b16 a[10:11], v133 offset:9216
ds_read_b64_tr_b16 a[12:13], v133 offset:12288
ds_read_b64_tr_b16 a[14:15], v133 offset:13312
s_waitcnt vmcnt(28)
s_waitcnt lgkmcnt(0)
s_barrier
s_add_u32 m0, s55, 0
s_nop 0
global_load_lds_dwordx4 v[138:139], off
v_lshl_add_u64 v[138:139], v[138:139], 0, s[44:45]
s_add_u32 m0, s55, 4096
s_nop 0
global_load_lds_dwordx4 v[138:139], off
v_lshl_add_u64 v[138:139], v[138:139], 0, s[44:45]
s_add_u32 m0, s55, 8192
s_nop 0
global_load_lds_dwordx4 v[138:139], off
v_lshl_add_u64 v[138:139], v[138:139], 0, s[44:45]
s_add_u32 m0, s55, 12288
s_nop 0
global_load_lds_dwordx4 v[138:139], off
v_lshl_add_u64 v[138:139], v[138:139], 0, s[44:45]
v_add_u32 v133, 16384, v125
ds_read_b64_tr_b16 a[16:17], v133 offset:0
ds_read_b64_tr_b16 a[18:19], v133 offset:1024
ds_read_b64_tr_b16 a[20:21], v133 offset:4096
ds_read_b64_tr_b16 a[22:23], v133 offset:5120
ds_read_b64_tr_b16 a[24:25], v133 offset:8192
ds_read_b64_tr_b16 a[26:27], v133 offset:9216
ds_read_b64_tr_b16 a[28:29], v133 offset:12288
ds_read_b64_tr_b16 a[30:31], v133 offset:13312
s_waitcnt vmcnt(28)
s_waitcnt lgkmcnt(0)
s_barrier
v_add_u32 v133, 32768, v125
ds_read_b64_tr_b16 a[32:33], v133 offset:0
ds_read_b64_tr_b16 a[34:35], v133 offset:1024
ds_read_b64_tr_b16 a[36:37], v133 offset:4096
ds_read_b64_tr_b16 a[38:39], v133 offset:5120
ds_read_b64_tr_b16 a[40:41], v133 offset:8192
ds_read_b64_tr_b16 a[42:43], v133 offset:9216
ds_read_b64_tr_b16 a[44:45], v133 offset:12288
ds_read_b64_tr_b16 a[46:47], v133 offset:13312
s_waitcnt vmcnt(24)
s_waitcnt lgkmcnt(0)
s_barrier
v_add_u32 v133, 49152, v125
ds_read_b64_tr_b16 a[48:49], v133 offset:0
ds_read_b64_tr_b16 a[50:51], v133 offset:1024
ds_read_b64_tr_b16 a[52:53], v133 offset:4096
ds_read_b64_tr_b16 a[54:55], v133 offset:5120
ds_read_b64_tr_b16 a[56:57], v133 offset:8192
ds_read_b64_tr_b16 a[58:59], v133 offset:9216
ds_read_b64_tr_b16 a[60:61], v133 offset:12288
ds_read_b64_tr_b16 a[62:63], v133 offset:13312
s_waitcnt vmcnt(20)
s_waitcnt lgkmcnt(0)
s_barrier
v_add_u32 v133, 65536, v125
ds_read_b64_tr_b16 a[64:65], v133 offset:0
ds_read_b64_tr_b16 a[66:67], v133 offset:1024
ds_read_b64_tr_b16 a[68:69], v133 offset:4096
ds_read_b64_tr_b16 a[70:71], v133 offset:5120
ds_read_b64_tr_b16 a[72:73], v133 offset:8192
ds_read_b64_tr_b16 a[74:75], v133 offset:9216
ds_read_b64_tr_b16 a[76:77], v133 offset:12288
ds_read_b64_tr_b16 a[78:79], v133 offset:13312
s_waitcnt vmcnt(16)
s_waitcnt lgkmcnt(0)
s_barrier
v_add_u32 v133, 81920, v125
ds_read_b64_tr_b16 a[80:81], v133 offset:0
ds_read_b64_tr_b16 a[82:83], v133 offset:1024
ds_read_b64_tr_b16 a[84:85], v133 offset:4096
ds_read_b64_tr_b16 a[86:87], v133 offset:5120
ds_read_b64_tr_b16 a[88:89], v133 offset:8192
ds_read_b64_tr_b16 a[90:91], v133 offset:9216
ds_read_b64_tr_b16 a[92:93], v133 offset:12288
ds_read_b64_tr_b16 a[94:95], v133 offset:13312
s_waitcnt vmcnt(12)
s_waitcnt lgkmcnt(0)
s_barrier
v_add_u32 v133, 98304, v125
ds_read_b64_tr_b16 a[96:97], v133 offset:0
ds_read_b64_tr_b16 a[98:99], v133 offset:1024
ds_read_b64_tr_b16 a[100:101], v133 offset:4096
ds_read_b64_tr_b16 a[102:103], v133 offset:5120
ds_read_b64_tr_b16 a[104:105], v133 offset:8192
ds_read_b64_tr_b16 a[106:107], v133 offset:9216
ds_read_b64_tr_b16 a[108:109], v133 offset:12288
ds_read_b64_tr_b16 a[110:111], v133 offset:13312
s_waitcnt vmcnt(8)
s_waitcnt lgkmcnt(0)
s_barrier
v_add_u32 v133, 114688, v125
ds_read_b64_tr_b16 a[112:113], v133 offset:0
ds_read_b64_tr_b16 a[114:115], v133 offset:1024
ds_read_b64_tr_b16 a[116:117], v133 offset:4096
ds_read_b64_tr_b16 a[118:119], v133 offset:5120
ds_read_b64_tr_b16 a[120:121], v133 offset:8192
ds_read_b64_tr_b16 a[122:123], v133 offset:9216
ds_read_b64_tr_b16 a[124:125], v133 offset:12288
ds_read_b64_tr_b16 a[126:127], v133 offset:13312
s_waitcnt vmcnt(4)
s_waitcnt lgkmcnt(0)
s_barrier
v_add_u32 v133, 131072, v125
ds_read_b64_tr_b16 a[128:129], v133 offset:0
ds_read_b64_tr_b16 a[130:131], v133 offset:1024
ds_read_b64_tr_b16 a[132:133], v133 offset:4096
ds_read_b64_tr_b16 a[134:135], v133 offset:5120
ds_read_b64_tr_b16 a[136:137], v133 offset:8192
ds_read_b64_tr_b16 a[138:139], v133 offset:9216
ds_read_b64_tr_b16 a[140:141], v133 offset:12288
ds_read_b64_tr_b16 a[142:143], v133 offset:13312
s_waitcnt vmcnt(0)
s_waitcnt lgkmcnt(0)
s_barrier
v_add_u32 v133, 0, v125
ds_read_b64_tr_b16 a[144:145], v133 offset:0
ds_read_b64_tr_b16 a[146:147], v133 offset:1024
ds_read_b64_tr_b16 a[148:149], v133 offset:4096
ds_read_b64_tr_b16 a[150:151], v133 offset:5120
ds_read_b64_tr_b16 a[152:153], v133 offset:8192
ds_read_b64_tr_b16 a[154:155], v133 offset:9216
ds_read_b64_tr_b16 a[156:157], v133 offset:12288
ds_read_b64_tr_b16 a[158:159], v133 offset:13312
s_waitcnt lgkmcnt(0)
s_barrier
s_mov_b64 s[28:29], s[52:53]
s_and_b32 s29, s29, 0xffff
s_mov_b32 s30, 3072
s_mov_b32 s31, 0x20000
buffer_load_dwordx4 v[112:115], v129, s[28:31], 0 offen offset:0
buffer_load_dwordx4 v[116:119], v129, s[28:31], 0 offen offset:32
v_mov_b32 v48, 0x3f803f80
v_mov_b32 v49, 0x3f803f80
v_mov_b32 v50, 0x3f803f80
v_mov_b32 v51, 0x3f803f80
s_nop 1
v_mfma_f32_32x32x16_bf16 v[16:31], a[0:3], v[48:51], 0
v_mfma_f32_32x32x16_bf16 v[16:31], a[4:7], v[48:51], v[16:31]
v_mfma_f32_32x32x16_bf16 v[16:31], a[8:11], v[48:51], v[16:31]
v_mfma_f32_32x32x16_bf16 v[16:31], a[12:15], v[48:51], v[16:31]
v_mfma_f32_32x32x16_bf16 v[16:31], a[16:19], v[48:51], v[16:31]
v_mfma_f32_32x32x16_bf16 v[16:31], a[20:23], v[48:51], v[16:31]
v_mfma_f32_32x32x16_bf16 v[16:31], a[24:27], v[48:51], v[16:31]
v_mfma_f32_32x32x16_bf16 v[16:31], a[28:31], v[48:51], v[16:31]
v_mfma_f32_32x32x16_bf16 v[16:31], a[32:35], v[48:51], v[16:31]
v_mfma_f32_32x32x16_bf16 v[16:31], a[36:39], v[48:51], v[16:31]
v_mfma_f32_32x32x16_bf16 v[16:31], a[40:43], v[48:51], v[16:31]
v_mfma_f32_32x32x16_bf16 v[16:31], a[44:47], v[48:51], v[16:31]
v_mfma_f32_32x32x16_bf16 v[16:31], a[48:51], v[48:51], v[16:31]
v_mfma_f32_32x32x16_bf16 v[16:31], a[52:55], v[48:51], v[16:31]
v_mfma_f32_32x32x16_bf16 v[16:31], a[56:59], v[48:51], v[16:31]
v_mfma_f32_32x32x16_bf16 v[16:31], a[60:63], v[48:51], v[16:31]
v_mfma_f32_32x32x16_bf16 v[16:31], a[64:67], v[48:51], v[16:31]
v_mfma_f32_32x32x16_bf16 v[16:31], a[68:71], v[48:51], v[16:31]
v_mfma_f32_32x32x16_bf16 v[16:31], a[72:75], v[48:51], v[16:31]
v_mfma_f32_32x32x16_bf16 v[16:31], a[76:79], v[48:51], v[16:31]
v_mfma_f32_32x32x16_bf16 v[16:31], a[80:83], v[48:51], v[16:31]
v_mfma_f32_32x32x16_bf16 v[16:31], a[84:87], v[48:51], v[16:31]
v_mfma_f32_32x32x16_bf16 v[16:31], a[88:91], v[48:51], v[16:31]
v_mfma_f32_32x32x16_bf16 v[16:31], a[92:95], v[48:51], v[16:31]
v_mfma_f32_32x32x16_bf16 v[16:31], a[96:99], v[48:51], v[16:31]
v_mfma_f32_32x32x16_bf16 v[16:31], a[100:103], v[48:51], v[16:31]
v_mfma_f32_32x32x16_bf16 v[16:31], a[104:107], v[48:51], v[16:31]
v_mfma_f32_32x32x16_bf16 v[16:31], a[108:111], v[48:51], v[16:31]
v_mfma_f32_32x32x16_bf16 v[16:31], a[112:115], v[48:51], v[16:31]
v_mfma_f32_32x32x16_bf16 v[16:31], a[116:119], v[48:51], v[16:31]
v_mfma_f32_32x32x16_bf16 v[16:31], a[120:123], v[48:51], v[16:31]
v_mfma_f32_32x32x16_bf16 v[16:31], a[124:127], v[48:51], v[16:31]
v_mfma_f32_32x32x16_bf16 v[16:31], a[128:131], v[48:51], v[16:31]
v_mfma_f32_32x32x16_bf16 v[16:31], a[132:135], v[48:51], v[16:31]
v_mfma_f32_32x32x16_bf16 v[16:31], a[136:139], v[48:51], v[16:31]
v_mfma_f32_32x32x16_bf16 v[16:31], a[140:143], v[48:51], v[16:31]
v_mfma_f32_32x32x16_bf16 v[16:31], a[144:147], v[48:51], v[16:31]
v_mfma_f32_32x32x16_bf16 v[16:31], a[148:151], v[48:51], v[16:31]
v_mfma_f32_32x32x16_bf16 v[16:31], a[152:155], v[48:51], v[16:31]
v_mfma_f32_32x32x16_bf16 v[16:31], a[156:159], v[48:51], v[16:31]
s_waitcnt vmcnt(1)
v_lshlrev_b32 v32, 16, v112
v_and_b32 v33, 0xffff0000, v112
v_lshlrev_b32 v34, 16, v113
v_and_b32 v35, 0xffff0000, v113
v_lshlrev_b32 v36, 16, v114
v_and_b32 v37, 0xffff0000, v114
v_lshlrev_b32 v38, 16, v115
v_and_b32 v39, 0xffff0000, v115
s_waitcnt vmcnt(0)
v_lshlrev_b32 v40, 16, v116
v_and_b32 v41, 0xffff0000, v116
v_lshlrev_b32 v42, 16, v117
v_and_b32 v43, 0xffff0000, v117
v_lshlrev_b32 v44, 16, v118
v_and_b32 v45, 0xffff0000, v118
v_lshlrev_b32 v46, 16, v119
v_and_b32 v47, 0xffff0000, v119
s_mul_i32 s42, s40, 10240
v_add_u32 v130, s42, v124
v_lshrrev_b32 v131, 1, v124
v_add_u32 v131, 40960, v131
v_mov_b32 v133, 0x3f803f80
s_mov_b32 s56, 0xbfb8aa3b
s_nop 0
F1_F0:
buffer_load_dwordx2 v[136:137], v128, s[96:99], 0 offen offset:0 sc1
s_waitcnt vmcnt(0)
v_min_u32 v136, v136, v137
v_cmp_gt_u32 vcc, 1, v136
s_cbranch_vccz F1_F0K
s_sleep 3
s_sub_u32 s41, s41, 1
s_cbranch_scc0 F1_F0
s_mov_b32 s41, 0
F1_F0K:
s_add_u32 s38, s36, 0
s_min_u32 s38, s38, 63
s_mul_i32 s38, s38, 40960
s_add_u32 s38, s38, 241172480
buffer_load_dwordx4 v[72:75], v130, s[96:99], s38 offen offset:0 sc1
buffer_load_dwordx4 v[76:79], v130, s[96:99], s38 offen offset:1024 sc1
buffer_load_dwordx4 v[80:83], v130, s[96:99], s38 offen offset:2048 sc1
buffer_load_dwordx4 v[84:87], v130, s[96:99], s38 offen offset:3072 sc1
s_add_u32 s38, s38, 4096
buffer_load_dwordx4 v[88:91], v130, s[96:99], s38 offen offset:0 sc1
buffer_load_dwordx4 v[92:95], v130, s[96:99], s38 offen offset:1024 sc1
buffer_load_dwordx4 v[96:99], v130, s[96:99], s38 offen offset:2048 sc1
buffer_load_dwordx4 v[100:103], v130, s[96:99], s38 offen offset:3072 sc1
s_add_u32 s38, s38, 4096
buffer_load_dwordx4 v[104:107], v130, s[96:99], s38 offen offset:0 sc1
buffer_load_dwordx4 v[108:111], v130, s[96:99], s38 offen offset:1024 sc1
s_waitcnt vmcnt(0)
F1_T:
s_add_u32 s59, s36, 2
s_min_u32 s59, s59, 64
v_mov_b32 v120, 0
v_mov_b32 v121, 0
ds_write_b128 v130, v[72:75] offset:0
v_dot2c_f32_bf16 v120, v72, v133
v_dot2c_f32_bf16 v121, v72, v72
v_dot2c_f32_bf16 v120, v73, v133
v_dot2c_f32_bf16 v121, v73, v73
v_dot2c_f32_bf16 v120, v74, v133
v_dot2c_f32_bf16 v121, v74, v74
v_dot2c_f32_bf16 v120, v75, v133
v_dot2c_f32_bf16 v121, v75, v75
ds_write_b128 v130, v[76:79] offset:1024
v_dot2c_f32_bf16 v120, v76, v133
v_dot2c_f32_bf16 v121, v76, v76
v_dot2c_f32_bf16 v120, v77, v133
v_dot2c_f32_bf16 v121, v77, v77
v_dot2c_f32_bf16 v120, v78, v133
v_dot2c_f32_bf16 v121, v78, v78
v_dot2c_f32_bf16 v120, v79, v133
v_dot2c_f32_bf16 v121, v79, v79
ds_write_b128 v130, v[80:83] offset:2048
v_dot2c_f32_bf16 v120, v80, v133
v_dot2c_f32_bf16 v121, v80, v80
v_dot2c_f32_bf16 v120, v81, v133
v_dot2c_f32_bf16 v121, v81, v81
v_dot2c_f32_bf16 v120, v82, v133
v_dot2c_f32_bf16 v121, v82, v82
v_dot2c_f32_bf16 v120, v83, v133
v_dot2c_f32_bf16 v121, v83, v83
ds_write_b128 v130, v[84:87] offset:3072
v_dot2c_f32_bf16 v120, v84, v133
v_dot2c_f32_bf16 v121, v84, v84
v_dot2c_f32_bf16 v120, v85, v133
v_dot2c_f32_bf16 v121, v85, v85
v_dot2c_f32_bf16 v120, v86, v133
v_dot2c_f32_bf16 v121, v86, v86
v_dot2c_f32_bf16 v120, v87, v133
v_dot2c_f32_bf16 v121, v87, v87
ds_write_b128 v130, v[88:91] offset:4096
v_dot2c_f32_bf16 v120, v88, v133
v_dot2c_f32_bf16 v121, v88, v88
v_dot2c_f32_bf16 v120, v89, v133
v_dot2c_f32_bf16 v121, v89, v89
v_dot2c_f32_bf16 v120, v90, v133
v_dot2c_f32_bf16 v121, v90, v90
v_dot2c_f32_bf16 v120, v91, v133
v_dot2c_f32_bf16 v121, v91, v91
ds_write_b128 v130, v[92:95] offset:5120
v_dot2c_f32_bf16 v120, v92, v133
v_dot2c_f32_bf16 v121, v92, v92
v_dot2c_f32_bf16 v120, v93, v133
v_dot2c_f32_bf16 v121, v93, v93
v_dot2c_f32_bf16 v120, v94, v133
v_dot2c_f32_bf16 v121, v94, v94
v_dot2c_f32_bf16 v120, v95, v133
v_dot2c_f32_bf16 v121, v95, v95
ds_write_b128 v130, v[96:99] offset:6144
v_dot2c_f32_bf16 v120, v96, v133
v_dot2c_f32_bf16 v121, v96, v96
v_dot2c_f32_bf16 v120, v97, v133
v_dot2c_f32_bf16 v121, v97, v97
v_dot2c_f32_bf16 v120, v98, v133
v_dot2c_f32_bf16 v121, v98, v98
v_dot2c_f32_bf16 v120, v99, v133
v_dot2c_f32_bf16 v121, v99, v99
ds_write_b128 v130, v[100:103] offset:7168
v_dot2c_f32_bf16 v120, v100, v133
v_dot2c_f32_bf16 v121, v100, v100
v_dot2c_f32_bf16 v120, v101, v133
v_dot2c_f32_bf16 v121, v101, v101
v_dot2c_f32_bf16 v120, v102, v133
v_dot2c_f32_bf16 v121, v102, v102
v_dot2c_f32_bf16 v120, v103, v133
v_dot2c_f32_bf16 v121, v103, v103
ds_write_b128 v130, v[104:107] offset:8192
v_dot2c_f32_bf16 v120, v104, v133
v_dot2c_f32_bf16 v121, v104, v104
v_dot2c_f32_bf16 v120, v105, v133
v_dot2c_f32_bf16 v121, v105, v105
v_dot2c_f32_bf16 v120, v106, v133
v_dot2c_f32_bf16 v121, v106, v106
v_dot2c_f32_bf16 v120, v107, v133
v_dot2c_f32_bf16 v121, v107, v107
ds_write_b128 v130, v[108:111] offset:9216
v_dot2c_f32_bf16 v120, v108, v133
v_dot2c_f32_bf16 v121, v108, v108
v_dot2c_f32_bf16 v120, v109, v133
v_dot2c_f32_bf16 v121, v109, v109
v_dot2c_f32_bf16 v120, v110, v133
v_dot2c_f32_bf16 v121, v110, v110
v_dot2c_f32_bf16 v120, v111, v133
v_dot2c_f32_bf16 v121, v111, v111
s_lshl_b32 s42, s40, 9
v_add_u32 v132, s42, v131
s_nop 0
ds_write_b64 v132, v[120:121] offset:0
buffer_load_dwordx2 v[136:137], v128, s[96:99], 0 offen offset:0 sc1
s_waitcnt lgkmcnt(0)
s_barrier
ds_read_b128 v[48:51], v124 offset:0
ds_read_b128 v[52:55], v124 offset:1024
ds_read_b128 v[56:59], v124 offset:2048
ds_read_b128 v[60:63], v124 offset:3072
ds_read_b128 v[64:67], v124 offset:4096
s_waitcnt lgkmcnt(4)
v_mfma_f32_32x32x16_bf16 v[0:15], a[0:3], v[48:51], 0
s_waitcnt lgkmcnt(3)
ds_read_b128 v[68:71], v124 offset:5120
v_and_b32 v132, 255, v131
v_add_u32 v132, 40960, v132
v_mfma_f32_32x32x16_bf16 v[0:15], a[4:7], v[52:55], v[0:15]
s_waitcnt lgkmcnt(3)
ds_read_b128 v[48:51], v124 offset:6144
v_mfma_f32_32x32x16_bf16 v[0:15], a[8:11], v[56:59], v[0:15]
s_waitcnt lgkmcnt(3)
ds_read_b128 v[52:55], v124 offset:7168
ds_read_b64 v[140:141], v132 offset:0
v_mfma_f32_32x32x16_bf16 v[0:15], a[12:15], v[60:63], v[0:15]
s_waitcnt lgkmcnt(4)
ds_read_b128 v[56:59], v124 offset:8192
ds_read_b64 v[142:143], v132 offset:256
v_mfma_f32_32x32x16_bf16 v[0:15], a[16:19], v[64:67], v[0:15]
s_waitcnt lgkmcnt(5)
ds_read_b128 v[60:63], v124 offset:9216
ds_read_b64 v[144:145], v132 offset:512
v_mfma_f32_32x32x16_bf16 v[0:15], a[20:23], v[68:71], v[0:15]
s_waitcnt lgkmcnt(6)
ds_read_b128 v[64:67], v124 offset:10240
ds_read_b64 v[146:147], v132 offset:768
v_mfma_f32_32x32x16_bf16 v[0:15], a[24:27], v[48:51], v[0:15]
s_waitcnt lgkmcnt(7)
ds_read_b128 v[68:71], v124 offset:11264
ds_read_b64 v[148:149], v132 offset:1024
v_mfma_f32_32x32x16_bf16 v[0:15], a[28:31], v[52:55], v[0:15]
s_waitcnt lgkmcnt(7)
ds_read_b128 v[48:51], v124 offset:12288
ds_read_b64 v[150:151], v132 offset:1280
v_mfma_f32_32x32x16_bf16 v[0:15], a[32:35], v[56:59], v[0:15]
s_waitcnt lgkmcnt(7)
ds_read_b128 v[52:55], v124 offset:13312
ds_read_b64 v[152:153], v132 offset:1536
v_mfma_f32_32x32x16_bf16 v[0:15], a[36:39], v[60:63], v[0:15]
s_waitcnt lgkmcnt(7)
ds_read_b128 v[56:59], v124 offset:14336
ds_read_b64 v[154:155], v132 offset:1792
v_mfma_f32_32x32x16_bf16 v[0:15], a[40:43], v[64:67], v[0:15]
s_waitcnt lgkmcnt(7)
ds_read_b128 v[60:63], v124 offset:15360
v_mfma_f32_32x32x16_bf16 v[0:15], a[44:47], v[68:71], v[0:15]
s_waitcnt lgkmcnt(6)
ds_read_b128 v[64:67], v124 offset:16384
v_mfma_f32_32x32x16_bf16 v[0:15], a[48:51], v[48:51], v[0:15]
s_waitcnt lgkmcnt(5)
ds_read_b128 v[68:71], v124 offset:17408
v_mfma_f32_32x32x16_bf16 v[0:15], a[52:55], v[52:55], v[0:15]
s_waitcnt lgkmcnt(4)
ds_read_b128 v[48:51], v124 offset:18432
v_mfma_f32_32x32x16_bf16 v[0:15], a[56:59], v[56:59], v[0:15]
s_waitcnt lgkmcnt(3)
ds_read_b128 v[52:55], v124 offset:19456
v_add_f32 v140, v140, v142
v_add_f32 v141, v141, v143
v_mfma_f32_32x32x16_bf16 v[0:15], a[60:63], v[60:63], v[0:15]
s_waitcnt lgkmcnt(3)
ds_read_b128 v[56:59], v124 offset:20480
v_add_f32 v140, v140, v144
v_add_f32 v141, v141, v145
v_mfma_f32_32x32x16_bf16 v[0:15], a[64:67], v[64:67], v[0:15]
s_waitcnt lgkmcnt(3)
ds_read_b128 v[60:63], v124 offset:21504
s_waitcnt vmcnt(0)
v_min_u32 v136, v136, v137
v_cmp_gt_u32 s[78:79], s59, v136
v_mov_b32 v120, s36
buffer_store_dword v120, v127, s[96:99], 0 offen offset:0 sc1
v_add_f32 v140, v140, v146
v_add_f32 v141, v141, v147
v_mfma_f32_32x32x16_bf16 v[0:15], a[68:71], v[68:71], v[0:15]
s_waitcnt lgkmcnt(3)
ds_read_b128 v[64:67], v124 offset:22528
s_add_u32 s38, s36, 1
v_add_f32 v140, v140, v148
v_add_f32 v141, v141, v149
v_mfma_f32_32x32x16_bf16 v[0:15], a[72:75], v[48:51], v[0:15]
s_waitcnt lgkmcnt(3)
ds_read_b128 v[68:71], v124 offset:23552
s_min_u32 s38, s38, 63
v_add_f32 v140, v140, v150
v_add_f32 v141, v141, v151
v_mfma_f32_32x32x16_bf16 v[0:15], a[76:79], v[52:55], v[0:15]
s_waitcnt lgkmcnt(3)
ds_read_b128 v[48:51], v124 offset:24576
s_mul_i32 s38, s38, 40960
v_add_f32 v140, v140, v152
v_add_f32 v141, v141, v153
v_mfma_f32_32x32x16_bf16 v[0:15], a[80:83], v[56:59], v[0:15]
s_waitcnt lgkmcnt(3)
ds_read_b128 v[52:55], v124 offset:25600
s_add_u32 s38, s38, 241172480
v_add_f32 v140, v140, v154
v_add_f32 v141, v141, v155
v_mfma_f32_32x32x16_bf16 v[0:15], a[84:87], v[60:63], v[0:15]
s_waitcnt lgkmcnt(3)
ds_read_b128 v[56:59], v124 offset:26624
s_cmp_eq_u64 s[78:79], 0
s_cselect_b32 s38, s38, 0x500000
v_mul_f32 v134, 0x3acccccd, v140
v_mul_f32 v135, 0x3acccccd, v141
v_mfma_f32_32x32x16_bf16 v[0:15], a[88:91], v[64:67], v[0:15]
s_waitcnt lgkmcnt(3)
ds_read_b128 v[60:63], v124 offset:27648
buffer_load_dwordx4 v[72:75], v130, s[96:99], s38 offen offset:0 sc1
v_fma_f32 v135, -v134, v134, v135
v_max_f32 v135, 0, v135
v_mfma_f32_32x32x16_bf16 v[0:15], a[92:95], v[68:71], v[0:15]
s_waitcnt lgkmcnt(3)
ds_read_b128 v[64:67], v124 offset:28672
buffer_load_dwordx4 v[76:79], v130, s[96:99], s38 offen offset:1024 sc1
v_add_f32 v135, 0x3727c5ac, v135
v_rsq_f32 v135, v135
v_mfma_f32_32x32x16_bf16 v[0:15], a[96:99], v[48:51], v[0:15]
s_waitcnt lgkmcnt(3)
ds_read_b128 v[68:71], v124 offset:29696
buffer_load_dwordx4 v[80:83], v130, s[96:99], s38 offen offset:2048 sc1
v_mfma_f32_32x32x16_bf16 v[0:15], a[100:103], v[52:55], v[0:15]
s_waitcnt lgkmcnt(3)
ds_read_b128 v[48:51], v124 offset:30720
buffer_load_dwordx4 v[84:87], v130, s[96:99], s38 offen offset:3072 sc1
v_mfma_f32_32x32x16_bf16 v[0:15], a[104:107], v[56:59], v[0:15]
s_waitcnt lgkmcnt(3)
ds_read_b128 v[52:55], v124 offset:31744
s_add_u32 s38, s38, 4096
v_mfma_f32_32x32x16_bf16 v[0:15], a[108:111], v[60:63], v[0:15]
s_waitcnt lgkmcnt(3)
ds_read_b128 v[56:59], v124 offset:32768
buffer_load_dwordx4 v[88:91], v130, s[96:99], s38 offen offset:0 sc1
v_mfma_f32_32x32x16_bf16 v[0:15], a[112:115], v[64:67], v[0:15]
s_waitcnt lgkmcnt(3)
ds_read_b128 v[60:63], v124 offset:33792
buffer_load_dwordx4 v[92:95], v130, s[96:99], s38 offen offset:1024 sc1
v_mfma_f32_32x32x16_bf16 v[0:15], a[116:119], v[68:71], v[0:15]
s_waitcnt lgkmcnt(3)
ds_read_b128 v[64:67], v124 offset:34816
buffer_load_dwordx4 v[96:99], v130, s[96:99], s38 offen offset:2048 sc1
v_mfma_f32_32x32x16_bf16 v[0:15], a[120:123], v[48:51], v[0:15]
s_waitcnt lgkmcnt(3)
ds_read_b128 v[68:71], v124 offset:35840
buffer_load_dwordx4 v[100:103], v130, s[96:99], s38 offen offset:3072 sc1
v_mfma_f32_32x32x16_bf16 v[0:15], a[124:127], v[52:55], v[0:15]
s_waitcnt lgkmcnt(3)
ds_read_b128 v[48:51], v124 offset:36864
s_add_u32 s38, s38, 4096
v_mfma_f32_32x32x16_bf16 v[0:15], a[128:131], v[56:59], v[0:15]
s_waitcnt lgkmcnt(3)
ds_read_b128 v[52:55], v124 offset:37888
buffer_load_dwordx4 v[104:107], v130, s[96:99], s38 offen offset:0 sc1
v_mfma_f32_32x32x16_bf16 v[0:15], a[132:135], v[60:63], v[0:15]
s_waitcnt lgkmcnt(3)
ds_read_b128 v[56:59], v124 offset:38912
buffer_load_dwordx4 v[108:111], v130, s[96:99], s38 offen offset:1024 sc1
v_mfma_f32_32x32x16_bf16 v[0:15], a[136:139], v[64:67], v[0:15]
s_waitcnt lgkmcnt(3)
ds_read_b128 v[60:63], v124 offset:39936
v_mfma_f32_32x32x16_bf16 v[0:15], a[140:143], v[68:71], v[0:15]
s_waitcnt lgkmcnt(3)
v_mfma_f32_32x32x16_bf16 v[0:15], a[144:147], v[48:51], v[0:15]
s_waitcnt lgkmcnt(2)
v_mfma_f32_32x32x16_bf16 v[0:15], a[148:151], v[52:55], v[0:15]
s_waitcnt lgkmcnt(1)
v_mfma_f32_32x32x16_bf16 v[0:15], a[152:155], v[56:59], v[0:15]
s_waitcnt lgkmcnt(0)
v_mfma_f32_32x32x16_bf16 v[0:15], a[156:159], v[60:63], v[0:15]
s_waitcnt lgkmcnt(0)
s_barrier
s_nop 9
v_fma_f32 v0, -v134, v16, v0
v_fma_f32 v1, -v134, v17, v1
v_fma_f32 v2, -v134, v18, v2
v_fma_f32 v3, -v134, v19, v3
v_fma_f32 v4, -v134, v20, v4
v_fma_f32 v5, -v134, v21, v5
v_fma_f32 v6, -v134, v22, v6
v_fma_f32 v7, -v134, v23, v7
v_fma_f32 v8, -v134, v24, v8
v_fma_f32 v9, -v134, v25, v9
v_fma_f32 v10, -v134, v26, v10
v_fma_f32 v11, -v134, v27, v11
v_fma_f32 v12, -v134, v28, v12
v_fma_f32 v13, -v134, v29, v13
v_fma_f32 v14, -v134, v30, v14
v_fma_f32 v15, -v134, v31, v15
v_fma_f32 v0, v0, v135, v32
v_fma_f32 v1, v1, v135, v33
v_fma_f32 v2, v2, v135, v34
v_fma_f32 v3, v3, v135, v35
v_fma_f32 v4, v4, v135, v36
v_fma_f32 v5, v5, v135, v37
v_fma_f32 v6, v6, v135, v38
v_fma_f32 v7, v7, v135, v39
v_fma_f32 v8, v8, v135, v40
v_fma_f32 v9, v9, v135, v41
v_fma_f32 v10, v10, v135, v42
v_fma_f32 v11, v11, v135, v43
v_fma_f32 v12, v12, v135, v44
v_fma_f32 v13, v13, v135, v45
v_fma_f32 v14, v14, v135, v46
v_fma_f32 v15, v15, v135, v47
v_mul_f32_e64 v140, |v0|, s56
v_mul_f32_e64 v141, |v1|, s56
v_mul_f32_e64 v142, |v2|, s56
v_mul_f32_e64 v143, |v3|, s56
v_exp_f32 v140, v140
v_exp_f32 v141, v141
v_exp_f32 v142, v142
v_exp_f32 v143, v143
v_mul_f32 v152, v140, v140
v_mul_f32 v153, v141, v141
v_mul_f32 v154, v142, v142
v_mul_f32 v155, v143, v143
v_cmp_le_f32 vcc, 0, v0
v_add_f32 v140, v140, v140
s_nop 0
v_cndmask_b32 v144, v152, 1.0, vcc
v_cndmask_b32 v148, 1.0, v152, vcc
v_cmp_le_f32 vcc, 0, v1
v_add_f32 v141, v141, v141
s_nop 0
v_cndmask_b32 v145, v153, 1.0, vcc
v_cndmask_b32 v149, 1.0, v153, vcc
v_cmp_le_f32 vcc, 0, v2
v_add_f32 v142, v142, v142
s_nop 0
v_cndmask_b32 v146, v154, 1.0, vcc
v_cndmask_b32 v150, 1.0, v154, vcc
v_cmp_le_f32 vcc, 0, v3
v_add_f32 v143, v143, v143
s_nop 0
v_cndmask_b32 v147, v155, 1.0, vcc
v_cndmask_b32 v151, 1.0, v155, vcc
v_add_f32 v144, v144, v140
v_add_f32 v145, v145, v141
v_add_f32 v146, v146, v142
v_add_f32 v147, v147, v143
v_fma_f32 v148, v148, 2.0, v144
v_fma_f32 v149, v149, 2.0, v145
v_fma_f32 v150, v150, 2.0, v146
v_fma_f32 v151, v151, 2.0, v147
v_rcp_f32 v148, v148
v_rcp_f32 v149, v149
v_rcp_f32 v150, v150
v_rcp_f32 v151, v151
v_mul_f32 v144, v144, v0
v_mul_f32 v145, v145, v1
v_mul_f32 v146, v146, v2
v_mul_f32 v147, v147, v3
v_mul_f32 v144, v144, v148
v_mul_f32 v145, v145, v149
v_mul_f32 v146, v146, v150
v_mul_f32 v147, v147, v151
v_cvt_pk_bf16_f32 v112, v144, v145
v_cvt_pk_bf16_f32 v113, v146, v147
v_mul_f32_e64 v140, |v4|, s56
v_mul_f32_e64 v141, |v5|, s56
v_mul_f32_e64 v142, |v6|, s56
v_mul_f32_e64 v143, |v7|, s56
v_exp_f32 v140, v140
v_exp_f32 v141, v141
v_exp_f32 v142, v142
v_exp_f32 v143, v143
v_mul_f32 v152, v140, v140
v_mul_f32 v153, v141, v141
v_mul_f32 v154, v142, v142
v_mul_f32 v155, v143, v143
v_cmp_le_f32 vcc, 0, v4
v_add_f32 v140, v140, v140
s_nop 0
v_cndmask_b32 v144, v152, 1.0, vcc
v_cndmask_b32 v148, 1.0, v152, vcc
v_cmp_le_f32 vcc, 0, v5
v_add_f32 v141, v141, v141
s_nop 0
v_cndmask_b32 v145, v153, 1.0, vcc
v_cndmask_b32 v149, 1.0, v153, vcc
v_cmp_le_f32 vcc, 0, v6
v_add_f32 v142, v142, v142
s_nop 0
v_cndmask_b32 v146, v154, 1.0, vcc
v_cndmask_b32 v150, 1.0, v154, vcc
v_cmp_le_f32 vcc, 0, v7
v_add_f32 v143, v143, v143
s_nop 0
v_cndmask_b32 v147, v155, 1.0, vcc
v_cndmask_b32 v151, 1.0, v155, vcc
v_add_f32 v144, v144, v140
v_add_f32 v145, v145, v141
v_add_f32 v146, v146, v142
v_add_f32 v147, v147, v143
v_fma_f32 v148, v148, 2.0, v144
v_fma_f32 v149, v149, 2.0, v145
v_fma_f32 v150, v150, 2.0, v146
v_fma_f32 v151, v151, 2.0, v147
v_rcp_f32 v148, v148
v_rcp_f32 v149, v149
v_rcp_f32 v150, v150
v_rcp_f32 v151, v151
v_mul_f32 v144, v144, v4
v_mul_f32 v145, v145, v5
v_mul_f32 v146, v146, v6
v_mul_f32 v147, v147, v7
v_mul_f32 v144, v144, v148
v_mul_f32 v145, v145, v149
v_mul_f32 v146, v146, v150
v_mul_f32 v147, v147, v151
v_cvt_pk_bf16_f32 v114, v144, v145
v_cvt_pk_bf16_f32 v115, v146, v147
s_mul_i32 s37, s36, 98304
buffer_store_dwordx4 v[112:115], v126, s[96:99], s37 offen offset:0 sc1
v_mul_f32_e64 v140, |v8|, s56
v_mul_f32_e64 v141, |v9|, s56
v_mul_f32_e64 v142, |v10|, s56
v_mul_f32_e64 v143, |v11|, s56
v_exp_f32 v140, v140
v_exp_f32 v141, v141
v_exp_f32 v142, v142
v_exp_f32 v143, v143
v_mul_f32 v152, v140, v140
v_mul_f32 v153, v141, v141
v_mul_f32 v154, v142, v142
v_mul_f32 v155, v143, v143
v_cmp_le_f32 vcc, 0, v8
v_add_f32 v140, v140, v140
s_nop 0
v_cndmask_b32 v144, v152, 1.0, vcc
v_cndmask_b32 v148, 1.0, v152, vcc
v_cmp_le_f32 vcc, 0, v9
v_add_f32 v141, v141, v141
s_nop 0
v_cndmask_b32 v145, v153, 1.0, vcc
v_cndmask_b32 v149, 1.0, v153, vcc
v_cmp_le_f32 vcc, 0, v10
v_add_f32 v142, v142, v142
s_nop 0
v_cndmask_b32 v146, v154, 1.0, vcc
v_cndmask_b32 v150, 1.0, v154, vcc
v_cmp_le_f32 vcc, 0, v11
v_add_f32 v143, v143, v143
s_nop 0
v_cndmask_b32 v147, v155, 1.0, vcc
v_cndmask_b32 v151, 1.0, v155, vcc
v_add_f32 v144, v144, v140
v_add_f32 v145, v145, v141
v_add_f32 v146, v146, v142
v_add_f32 v147, v147, v143
v_fma_f32 v148, v148, 2.0, v144
v_fma_f32 v149, v149, 2.0, v145
v_fma_f32 v150, v150, 2.0, v146
v_fma_f32 v151, v151, 2.0, v147
v_rcp_f32 v148, v148
v_rcp_f32 v149, v149
v_rcp_f32 v150, v150
v_rcp_f32 v151, v151
v_mul_f32 v144, v144, v8
v_mul_f32 v145, v145, v9
v_mul_f32 v146, v146, v10
v_mul_f32 v147, v147, v11
v_mul_f32 v144, v144, v148
v_mul_f32 v145, v145, v149
v_mul_f32 v146, v146, v150
v_mul_f32 v147, v147, v151
v_cvt_pk_bf16_f32 v116, v144, v145
v_cvt_pk_bf16_f32 v117, v146, v147
v_mul_f32_e64 v140, |v12|, s56
v_mul_f32_e64 v141, |v13|, s56
v_mul_f32_e64 v142, |v14|, s56
v_mul_f32_e64 v143, |v15|, s56
v_exp_f32 v140, v140
v_exp_f32 v141, v141
v_exp_f32 v142, v142
v_exp_f32 v143, v143
v_mul_f32 v152, v140, v140
v_mul_f32 v153, v141, v141
v_mul_f32 v154, v142, v142
v_mul_f32 v155, v143, v143
v_cmp_le_f32 vcc, 0, v12
v_add_f32 v140, v140, v140
s_nop 0
v_cndmask_b32 v144, v152, 1.0, vcc
v_cndmask_b32 v148, 1.0, v152, vcc
v_cmp_le_f32 vcc, 0, v13
v_add_f32 v141, v141, v141
s_nop 0
v_cndmask_b32 v145, v153, 1.0, vcc
v_cndmask_b32 v149, 1.0, v153, vcc
v_cmp_le_f32 vcc, 0, v14
v_add_f32 v142, v142, v142
s_nop 0
v_cndmask_b32 v146, v154, 1.0, vcc
v_cndmask_b32 v150, 1.0, v154, vcc
v_cmp_le_f32 vcc, 0, v15
v_add_f32 v143, v143, v143
s_nop 0
v_cndmask_b32 v147, v155, 1.0, vcc
v_cndmask_b32 v151, 1.0, v155, vcc
v_add_f32 v144, v144, v140
v_add_f32 v145, v145, v141
v_add_f32 v146, v146, v142
v_add_f32 v147, v147, v143
v_fma_f32 v148, v148, 2.0, v144
v_fma_f32 v149, v149, 2.0, v145
v_fma_f32 v150, v150, 2.0, v146
v_fma_f32 v151, v151, 2.0, v147
v_rcp_f32 v148, v148
v_rcp_f32 v149, v149
v_rcp_f32 v150, v150
v_rcp_f32 v151, v151
v_mul_f32 v144, v144, v12
v_mul_f32 v145, v145, v13
v_mul_f32 v146, v146, v14
v_mul_f32 v147, v147, v15
v_mul_f32 v144, v144, v148
v_mul_f32 v145, v145, v149
v_mul_f32 v146, v146, v150
v_mul_f32 v147, v147, v151
v_cvt_pk_bf16_f32 v118, v144, v145
v_cvt_pk_bf16_f32 v119, v146, v147
buffer_store_dwordx4 v[116:119], v126, s[96:99], s37 offen offset:1024 sc1
s_cmp_eq_u64 s[78:79], 0
s_cbranch_scc1 F1_E
s_waitcnt vmcnt(0)
s_add_u32 s42, s36, 1
v_mov_b32 v120, s42
buffer_store_dword v120, v127, s[96:99], 0 offen offset:0 sc1
s_nop 0
F1_F:
buffer_load_dwordx2 v[136:137], v128, s[96:99], 0 offen offset:0 sc1
s_waitcnt vmcnt(0)
v_min_u32 v136, v136, v137
v_cmp_gt_u32 vcc, s59, v136
s_cbranch_vccz F1_FK
s_sleep 3
s_sub_u32 s41, s41, 1
s_cbranch_scc0 F1_F
s_mov_b32 s41, 0
F1_FK:
s_add_u32 s38, s36, 1
s_min_u32 s38, s38, 63
s_mul_i32 s38, s38, 40960
s_add_u32 s38, s38, 241172480
buffer_load_dwordx4 v[72:75], v130, s[96:99], s38 offen offset:0 sc1
buffer_load_dwordx4 v[76:79], v130, s[96:99], s38 offen offset:1024 sc1
buffer_load_dwordx4 v[80:83], v130, s[96:99], s38 offen offset:2048 sc1
buffer_load_dwordx4 v[84:87], v130, s[96:99], s38 offen offset:3072 sc1
s_add_u32 s38, s38, 4096
buffer_load_dwordx4 v[88:91], v130, s[96:99], s38 offen offset:0 sc1
buffer_load_dwordx4 v[92:95], v130, s[96:99], s38 offen offset:1024 sc1
buffer_load_dwordx4 v[96:99], v130, s[96:99], s38 offen offset:2048 sc1
buffer_load_dwordx4 v[100:103], v130, s[96:99], s38 offen offset:3072 sc1
s_add_u32 s38, s38, 4096
buffer_load_dwordx4 v[104:107], v130, s[96:99], s38 offen offset:0 sc1
buffer_load_dwordx4 v[108:111], v130, s[96:99], s38 offen offset:1024 sc1
s_waitcnt vmcnt(0)
F1_E:
s_waitcnt vmcnt(2)
s_add_u32 s36, s36, 1
s_cmp_lt_u32 s36, 64
s_cbranch_scc1 F1_T
s_waitcnt vmcnt(0)
v_mov_b32 v120, s36
buffer_store_dword v120, v127, s[96:99], 0 offen offset:0 sc1
s_endpgm
ROLE_F2:
s_mov_b64 s[48:49], %0
s_mov_b64 s[50:51], %1
s_mov_b64 s[52:53], %2
s_mov_b32 s54, %3
s_mov_b32 s40, %4
s_mov_b32 s39, %5
s_mov_b32 s41, 0x200000
s_mov_b32 s36, 0
s_lshl_b32 s55, s40, 10
v_mbcnt_lo_u32_b32 v31, -1, 0
v_mbcnt_hi_u32_b32 v31, -1, v31
s_lshl_b32 s42, s40, 8
s_add_u32 s42, s42, 147456
v_lshl_add_u32 v31, v31, 2, s42
ds_read_b32 v132, v31 offset:0
ds_read_b32 v133, v31 offset:1024
ds_read_b32 v134, v31 offset:2048
ds_read_b32 v135, v31 offset:3072
ds_read_b32 v136, v31 offset:4096
ds_read_b32 v144, v31 offset:5120
ds_read_b32 v145, v31 offset:6144
s_waitcnt lgkmcnt(0)
s_memrealtime s[94:95]
s_waitcnt lgkmcnt(0)
s_mov_b32 s92, s94
s_add_u32 s20, s48, 0x0
s_addc_u32 s21, s49, 0
s_and_b32 s21, s21, 0xffff
s_mov_b32 s22, 0x400000
s_mov_b32 s23, 0x20000
s_add_u32 s96, s48, 0x0
s_addc_u32 s97, s49, 0
s_and_b32 s97, s97, 0xffff
s_mov_b32 s98, 0xfe00000
s_mov_b32 s99, 0x20000
s_mov_b32 s44, 0x5000
s_mov_b32 s45, 0
s_add_u32 m0, s55, 0
s_nop 0
global_load_lds_dwordx4 v[144:145], off
v_lshl_add_u64 v[144:145], v[144:145], 0, s[44:45]
s_add_u32 m0, s55, 4096
s_nop 0
global_load_lds_dwordx4 v[144:145], off
v_lshl_add_u64 v[144:145], v[144:145], 0, s[44:45]
s_add_u32 m0, s55, 8192
s_nop 0
global_load_lds_dwordx4 v[144:145], off
v_lshl_add_u64 v[144:145], v[144:145], 0, s[44:45]
s_add_u32 m0, s55, 12288
s_nop 0
global_load_lds_dwordx4 v[144:145], off
v_lshl_add_u64 v[144:145], v[144:145], 0, s[44:45]
s_add_u32 m0, s55, 16384
s_nop 0
global_load_lds_dwordx4 v[144:145], off
v_lshl_add_u64 v[144:145], v[144:145], 0, s[44:45]
s_add_u32 m0, s55, 20480
s_nop 0
global_load_lds_dwordx4 v[144:145], off
v_lshl_add_u64 v[144:145], v[144:145], 0, s[44:45]
s_add_u32 m0, s55, 24576
s_nop 0
global_load_lds_dwordx4 v[144:145], off
v_lshl_add_u64 v[144:145], v[144:145], 0, s[44:45]
s_add_u32 m0, s55, 28672
s_nop 0
global_load_lds_dwordx4 v[144:145], off
v_lshl_add_u64 v[144:145], v[144:145], 0, s[44:45]
s_add_u32 m0, s55, 32768
s_nop 0
global_load_lds_dwordx4 v[144:145], off
v_lshl_add_u64 v[144:145], v[144:145], 0, s[44:45]
s_add_u32 m0, s55, 36864
s_nop 0
global_load_lds_dwordx4 v[144:145], off
v_lshl_add_u64 v[144:145], v[144:145], 0, s[44:45]
s_add_u32 m0, s55, 40960
s_nop 0
global_load_lds_dwordx4 v[144:145], off
v_lshl_add_u64 v[144:145], v[144:145], 0, s[44:45]
s_add_u32 m0, s55, 45056
s_nop 0
global_load_lds_dwordx4 v[144:145], off
v_lshl_add_u64 v[144:145], v[144:145], 0, s[44:45]
s_add_u32 m0, s55, 49152
s_nop 0
global_load_lds_dwordx4 v[144:145], off
v_lshl_add_u64 v[144:145], v[144:145], 0, s[44:45]
s_add_u32 m0, s55, 53248
s_nop 0
global_load_lds_dwordx4 v[144:145], off
v_lshl_add_u64 v[144:145], v[144:145], 0, s[44:45]
s_add_u32 m0, s55, 57344
s_nop 0
global_load_lds_dwordx4 v[144:145], off
v_lshl_add_u64 v[144:145], v[144:145], 0, s[44:45]
s_add_u32 m0, s55, 61440
s_nop 0
global_load_lds_dwordx4 v[144:145], off
v_lshl_add_u64 v[144:145], v[144:145], 0, s[44:45]
s_add_u32 m0, s55, 65536
s_nop 0
global_load_lds_dwordx4 v[144:145], off
v_lshl_add_u64 v[144:145], v[144:145], 0, s[44:45]
s_add_u32 m0, s55, 69632
s_nop 0
global_load_lds_dwordx4 v[144:145], off
v_lshl_add_u64 v[144:145], v[144:145], 0, s[44:45]
s_add_u32 m0, s55, 73728
s_nop 0
global_load_lds_dwordx4 v[144:145], off
v_lshl_add_u64 v[144:145], v[144:145], 0, s[44:45]
s_add_u32 m0, s55, 77824
s_nop 0
global_load_lds_dwordx4 v[144:145], off
v_lshl_add_u64 v[144:145], v[144:145], 0, s[44:45]
s_add_u32 m0, s55, 81920
s_nop 0
global_load_lds_dwordx4 v[144:145], off
v_lshl_add_u64 v[144:145], v[144:145], 0, s[44:45]
s_add_u32 m0, s55, 86016
s_nop 0
global_load_lds_dwordx4 v[144:145], off
v_lshl_add_u64 v[144:145], v[144:145], 0, s[44:45]
s_add_u32 m0, s55, 90112
s_nop 0
global_load_lds_dwordx4 v[144:145], off
v_lshl_add_u64 v[144:145], v[144:145], 0, s[44:45]
s_add_u32 m0, s55, 94208
s_nop 0
global_load_lds_dwordx4 v[144:145], off
v_lshl_add_u64 v[144:145], v[144:145], 0, s[44:45]
s_waitcnt vmcnt(20)
s_waitcnt lgkmcnt(0)
s_barrier
v_add_u32 v138, 0, v133
ds_read_b64_tr_b16 a[0:1], v138 offset:0
ds_read_b64_tr_b16 a[2:3], v138 offset:1024
ds_read_b64_tr_b16 a[4:5], v138 offset:4096
ds_read_b64_tr_b16 a[6:7], v138 offset:5120
ds_read_b64_tr_b16 a[8:9], v138 offset:8192
ds_read_b64_tr_b16 a[10:11], v138 offset:9216
ds_read_b64_tr_b16 a[12:13], v138 offset:12288
ds_read_b64_tr_b16 a[14:15], v138 offset:13312
s_waitcnt vmcnt(16)
s_waitcnt lgkmcnt(0)
s_barrier
v_add_u32 v138, 16384, v133
ds_read_b64_tr_b16 a[16:17], v138 offset:0
ds_read_b64_tr_b16 a[18:19], v138 offset:1024
ds_read_b64_tr_b16 a[20:21], v138 offset:4096
ds_read_b64_tr_b16 a[22:23], v138 offset:5120
ds_read_b64_tr_b16 a[24:25], v138 offset:8192
ds_read_b64_tr_b16 a[26:27], v138 offset:9216
ds_read_b64_tr_b16 a[28:29], v138 offset:12288
ds_read_b64_tr_b16 a[30:31], v138 offset:13312
s_waitcnt vmcnt(12)
s_waitcnt lgkmcnt(0)
s_barrier
v_add_u32 v138, 32768, v133
ds_read_b64_tr_b16 a[32:33], v138 offset:0
ds_read_b64_tr_b16 a[34:35], v138 offset:1024
ds_read_b64_tr_b16 a[36:37], v138 offset:4096
ds_read_b64_tr_b16 a[38:39], v138 offset:5120
ds_read_b64_tr_b16 a[40:41], v138 offset:8192
ds_read_b64_tr_b16 a[42:43], v138 offset:9216
ds_read_b64_tr_b16 a[44:45], v138 offset:12288
ds_read_b64_tr_b16 a[46:47], v138 offset:13312
s_waitcnt vmcnt(8)
s_waitcnt lgkmcnt(0)
s_barrier
v_add_u32 v138, 49152, v133
ds_read_b64_tr_b16 a[48:49], v138 offset:0
ds_read_b64_tr_b16 a[50:51], v138 offset:1024
ds_read_b64_tr_b16 a[52:53], v138 offset:4096
ds_read_b64_tr_b16 a[54:55], v138 offset:5120
ds_read_b64_tr_b16 a[56:57], v138 offset:8192
ds_read_b64_tr_b16 a[58:59], v138 offset:9216
ds_read_b64_tr_b16 a[60:61], v138 offset:12288
ds_read_b64_tr_b16 a[62:63], v138 offset:13312
s_waitcnt vmcnt(4)
s_waitcnt lgkmcnt(0)
s_barrier
v_add_u32 v138, 65536, v133
ds_read_b64_tr_b16 a[64:65], v138 offset:0
ds_read_b64_tr_b16 a[66:67], v138 offset:1024
ds_read_b64_tr_b16 a[68:69], v138 offset:4096
ds_read_b64_tr_b16 a[70:71], v138 offset:5120
ds_read_b64_tr_b16 a[72:73], v138 offset:8192
ds_read_b64_tr_b16 a[74:75], v138 offset:9216
ds_read_b64_tr_b16 a[76:77], v138 offset:12288
ds_read_b64_tr_b16 a[78:79], v138 offset:13312
s_waitcnt vmcnt(0)
s_waitcnt lgkmcnt(0)
s_barrier
v_add_u32 v138, 81920, v133
ds_read_b64_tr_b16 a[80:81], v138 offset:0
ds_read_b64_tr_b16 a[82:83], v138 offset:1024
ds_read_b64_tr_b16 a[84:85], v138 offset:4096
ds_read_b64_tr_b16 a[86:87], v138 offset:5120
ds_read_b64_tr_b16 a[88:89], v138 offset:8192
ds_read_b64_tr_b16 a[90:91], v138 offset:9216
ds_read_b64_tr_b16 a[92:93], v138 offset:12288
ds_read_b64_tr_b16 a[94:95], v138 offset:13312
s_waitcnt lgkmcnt(0)
s_barrier
s_mov_b64 s[28:29], s[50:51]
s_and_b32 s29, s29, 0xffff
s_mov_b32 s30, 2621440
s_mov_b32 s31, 0x20000
s_lshl_b32 s42, s40, 12
v_add_u32 v140, s42, v132
v_add_u32 v141, s55, v132
buffer_load_dword v139, v136, s[96:99], 0 offen offset:0 sc1
s_nop 0
F2_T:
s_add_u32 s59, s36, 1
s_waitcnt vmcnt(0)
v_cmp_gt_u32 vcc, s59, v139
s_cbranch_vccz F2_FK
s_nop 3
F2_F:
buffer_load_dword v139, v136, s[96:99], 0 offen offset:0 sc1
s_waitcnt vmcnt(0)
v_cmp_gt_u32 vcc, s59, v139
s_cbranch_vccz F2_FK
s_sleep 3
s_sub_u32 s41, s41, 1
s_cbranch_scc0 F2_F
s_mov_b32 s41, 0
F2_FK:
s_mul_i32 s42, s36, 98304
s_mul_i32 s43, s40, 24576
s_add_u32 s57, s42, s43
s_add_u32 s57, s57, 244318208
buffer_load_dwordx4 v[0:3], v132, s[96:99], s57 offen offset:0 sc1
buffer_load_dwordx4 v[4:7], v132, s[96:99], s57 offen offset:1024 sc1
buffer_load_dwordx4 v[8:11], v132, s[96:99], s57 offen offset:2048 sc1
buffer_load_dwordx4 v[12:15], v132, s[96:99], s57 offen offset:3072 sc1
s_add_u32 s57, s57, 4096
buffer_load_dwordx4 v[16:19], v132, s[96:99], s57 offen offset:0 sc1
buffer_load_dwordx4 v[20:23], v132, s[96:99], s57 offen offset:1024 sc1
buffer_load_dwordx4 v[24:27], v132, s[96:99], s57 offen offset:2048 sc1
buffer_load_dwordx4 v[28:31], v132, s[96:99], s57 offen offset:3072 sc1
s_add_u32 s57, s57, 4096
buffer_load_dwordx4 v[32:35], v132, s[96:99], s57 offen offset:0 sc1
buffer_load_dwordx4 v[36:39], v132, s[96:99], s57 offen offset:1024 sc1
buffer_load_dwordx4 v[40:43], v132, s[96:99], s57 offen offset:2048 sc1
buffer_load_dwordx4 v[44:47], v132, s[96:99], s57 offen offset:3072 sc1
s_add_u32 s57, s57, 4096
buffer_load_dwordx4 v[48:51], v132, s[96:99], s57 offen offset:0 sc1
buffer_load_dwordx4 v[52:55], v132, s[96:99], s57 offen offset:1024 sc1
buffer_load_dwordx4 v[56:59], v132, s[96:99], s57 offen offset:2048 sc1
buffer_load_dwordx4 v[60:63], v132, s[96:99], s57 offen offset:3072 sc1
s_add_u32 s57, s57, 4096
buffer_load_dwordx4 v[64:67], v132, s[96:99], s57 offen offset:0 sc1
buffer_load_dwordx4 v[68:71], v132, s[96:99], s57 offen offset:1024 sc1
buffer_load_dwordx4 v[72:75], v132, s[96:99], s57 offen offset:2048 sc1
buffer_load_dwordx4 v[76:79], v132, s[96:99], s57 offen offset:3072 sc1
s_add_u32 s57, s57, 4096
buffer_load_dwordx4 v[80:83], v132, s[96:99], s57 offen offset:0 sc1
buffer_load_dwordx4 v[84:87], v132, s[96:99], s57 offen offset:1024 sc1
buffer_load_dwordx4 v[88:91], v132, s[96:99], s57 offen offset:2048 sc1
buffer_load_dwordx4 v[92:95], v132, s[96:99], s57 offen offset:3072 sc1
s_and_b32 s42, s36, 1
s_lshl_b32 s42, s42, 14
v_add_u32 v142, s42, v140
v_add_u32 v143, s42, v141
s_waitcnt vmcnt(23)
v_mfma_f32_32x32x16_bf16 v[96:111], a[0:3], v[0:3], 0
s_waitcnt vmcnt(22)
v_mfma_f32_32x32x16_bf16 v[96:111], a[4:7], v[4:7], v[96:111]
s_waitcnt vmcnt(21)
v_mfma_f32_32x32x16_bf16 v[96:111], a[8:11], v[8:11], v[96:111]
s_waitcnt vmcnt(20)
v_mfma_f32_32x32x16_bf16 v[96:111], a[12:15], v[12:15], v[96:111]
s_waitcnt vmcnt(19)
v_mfma_f32_32x32x16_bf16 v[96:111], a[16:19], v[16:19], v[96:111]
s_waitcnt vmcnt(18)
v_mfma_f32_32x32x16_bf16 v[96:111], a[20:23], v[20:23], v[96:111]
s_waitcnt vmcnt(17)
v_mfma_f32_32x32x16_bf16 v[96:111], a[24:27], v[24:27], v[96:111]
s_waitcnt vmcnt(16)
v_mfma_f32_32x32x16_bf16 v[96:111], a[28:31], v[28:31], v[96:111]
s_waitcnt vmcnt(15)
v_mfma_f32_32x32x16_bf16 v[96:111], a[32:35], v[32:35], v[96:111]
s_waitcnt vmcnt(14)
v_mfma_f32_32x32x16_bf16 v[96:111], a[36:39], v[36:39], v[96:111]
s_waitcnt vmcnt(13)
v_mfma_f32_32x32x16_bf16 v[96:111], a[40:43], v[40:43], v[96:111]
s_waitcnt vmcnt(12)
v_mfma_f32_32x32x16_bf16 v[96:111], a[44:47], v[44:47], v[96:111]
s_waitcnt vmcnt(11)
v_mfma_f32_32x32x16_bf16 v[96:111], a[48:51], v[48:51], v[96:111]
s_waitcnt vmcnt(10)
v_mfma_f32_32x32x16_bf16 v[96:111], a[52:55], v[52:55], v[96:111]
s_waitcnt vmcnt(9)
v_mfma_f32_32x32x16_bf16 v[96:111], a[56:59], v[56:59], v[96:111]
s_waitcnt vmcnt(8)
v_mfma_f32_32x32x16_bf16 v[96:111], a[60:63], v[60:63], v[96:111]
s_waitcnt vmcnt(7)
v_mfma_f32_32x32x16_bf16 v[96:111], a[64:67], v[64:67], v[96:111]
s_waitcnt vmcnt(6)
v_mfma_f32_32x32x16_bf16 v[96:111], a[68:71], v[68:71], v[96:111]
s_waitcnt vmcnt(5)
v_mfma_f32_32x32x16_bf16 v[96:111], a[72:75], v[72:75], v[96:111]
s_waitcnt vmcnt(4)
v_mfma_f32_32x32x16_bf16 v[96:111], a[76:79], v[76:79], v[96:111]
s_waitcnt vmcnt(3)
v_mfma_f32_32x32x16_bf16 v[96:111], a[80:83], v[80:83], v[96:111]
s_waitcnt vmcnt(2)
v_mfma_f32_32x32x16_bf16 v[96:111], a[84:87], v[84:87], v[96:111]
s_waitcnt vmcnt(1)
v_mfma_f32_32x32x16_bf16 v[96:111], a[88:91], v[88:91], v[96:111]
s_waitcnt vmcnt(0)
v_mfma_f32_32x32x16_bf16 v[96:111], a[92:95], v[92:95], v[96:111]
s_nop 11
ds_write_b128 v142, v[96:99] offset:0
ds_write_b128 v142, v[100:103] offset:1024
ds_write_b128 v142, v[104:107] offset:2048
ds_write_b128 v142, v[108:111] offset:3072
s_waitcnt lgkmcnt(0)
s_barrier
s_mul_i32 s42, s36, 40960
buffer_load_dwordx2 v[128:129], v134, s[96:99], s42 offen offset:0 sc1
buffer_load_dword v139, v136, s[96:99], 0 offen offset:0 sc1
ds_read_b128 v[112:115], v143 offset:0
ds_read_b128 v[116:119], v143 offset:4096
ds_read_b128 v[120:123], v143 offset:8192
ds_read_b128 v[124:127], v143 offset:12288
s_waitcnt lgkmcnt(2)
v_add_f32 v112, v112, v116
s_waitcnt lgkmcnt(0)
v_add_f32 v120, v120, v124
v_add_f32 v112, v112, v120
v_add_f32 v113, v113, v117
v_add_f32 v121, v121, v125
v_add_f32 v113, v113, v121
v_add_f32 v114, v114, v118
v_add_f32 v122, v122, v126
v_add_f32 v114, v114, v122
v_add_f32 v115, v115, v119
v_add_f32 v123, v123, v127
v_add_f32 v115, v115, v123
s_waitcnt vmcnt(1)
v_lshlrev_b32 v137, 16, v128
v_add_f32 v112, v112, v137
v_and_b32 v137, 0xffff0000, v128
v_add_f32 v113, v113, v137
v_lshlrev_b32 v137, 16, v129
v_add_f32 v114, v114, v137
v_and_b32 v137, 0xffff0000, v129
v_add_f32 v115, v115, v137
v_cvt_pk_bf16_f32 v112, v112, v113
v_cvt_pk_bf16_f32 v113, v114, v115
s_mul_i32 s42, s36, 40960
buffer_store_dwordx2 v[112:113], v135, s[28:31], s42 offen offset:0
s_add_u32 s36, s36, 1
s_cmp_lt_u32 s36, 64
s_cbranch_scc1 F2_T
s_endpgm
)ASM" :: "s"(p.ws), "s"(p0), "s"(p1), "s"(id), "s"(wave), "s"(par), "s"(role), "s"((unsigned)(uintptr_t)(LAS char*)smem) : CLOB);
}

DEV float bf2f(unsigned short v) { return __uint_as_float((unsigned)v << 16); }
__global__ void __launch_bounds__(256) k_pre(Params p) {
    const int b = blockIdx.x, wave = threadIdx.x >> 6, lane = threadIdx.x & 63;
    for (int rr = 0; rr < 2; ++rr) {
        const int row = b * 8 + wave * 2 + rr;
        const v4u z4 = {0, 0, 0, 0};
        const v4u* src = (const v4u*)((const char*)p.x + (size_t)row * 1280);
        const v4u a = src[lane], c = lane < 16 ? src[64 + lane] : z4;
        float v[16];
#pragma unroll
        for (int j = 0; j < 4; ++j) { v[2 * j] = __uint_as_float(a[j] << 16); v[2 * j + 1] = __uint_as_float(a[j] & 0xFFFF0000u); v[8 + 2 * j] = __uint_as_float(c[j] << 16); v[9 + 2 * j] = __uint_as_float(c[j] & 0xFFFF0000u); }
        float sm = 0;
#pragma unroll
        for (int j = 0; j < 16; ++j) sm += v[j];
#pragma unroll
        for (int o = 32; o >= 1; o >>= 1) sm += __shfl_xor(sm, o);
        const float mean = sm * (1.0f / DM);
        float sq = 0;
#pragma unroll
        for (int j = 0; j < 16; ++j) { const float d = (j < 8 || lane < 16) ? v[j] - mean : 0.f; sq += d * d; }
#pragma unroll
        for (int o = 32; o >= 1; o >>= 1) sq += __shfl_xor(sq, o);
        const float rs = 1.0f / sqrtf(sq * (1.0f / DM) + 1e-5f);
        char* dst = p.ws + O_XN + (size_t)(row >> 5) * 40960 + (row & 31) * 16;
        v4u o1, o2;
#pragma unroll
        for (int j = 0; j < 4; ++j) { o1[j] = cvt_pk((v[2 * j] - mean) * rs, (v[2 * j + 1] - mean) * rs); o2[j] = cvt_pk((v[8 + 2 * j] - mean) * rs, (v[9 + 2 * j] - mean) * rs); }
        *(v4u*)(dst + lane * 512) = o1;
        if (lane < 16) *(v4u*)(dst + (64 + lane) * 512) = o2;
    }
    {
        const v4u s4 = {SENT, SENT, SENT, SENT};
        v4u* c = (v4u*)(p.ws + O_CRIT) + (size_t)b * 1024;
        for (int i = threadIdx.x; i < 1024; i += 256) {
            const unsigned phys = (b * 1024 + i) * 16u;
            v4u val = s4;
            if (phys < 32768u) {
                const unsigned lo = ((phys >> 14) << 13) | (phys & 8191u);
                if (lo < 10240u) {
                    const int u = lo >> 8, half = (lo >> 4) & 1, r = (lo >> 5) & 7;
                    const unsigned* m = (const unsigned*)((const char*)p.mem0 + r * 1280 + u * 32);
#pragma unroll
                    for (int g = 0; g < 4; ++g) val[g] = pub(m[2 * g + half]);
                }
            }
            c[i] = val;
        }
    }
    {
        const v4u s4 = {SENT, SENT, SENT, SENT};
        for (int i = b * 256 + threadIdx.x; i < 2 * 40 * 1024; i += 65536) *(v4u*)(p.ws + O_U + (size_t)(i / 40960) * MB + (size_t)(i % 40960) * 16) = s4;
    }
    if (b == 1) for (int i = threadIdx.x; i < 4096; i += 256) ((unsigned*)(p.ws + O_CTL + 4096))[i] = 0;
    if (b == 0 && wave == 0) {
        unsigned par = 0;
        for (int pg = 0; pg < 1; ++pg) {
            unsigned best[2] = {~0u, ~0u};
            for (int rep = 0; rep < 4; ++rep)
                for (int hf = 0; hf < 2; ++hf) {
                    char* q = p.ws + O_CRIT + (size_t)pg * 2 * MB + 32768 * 2 * 31 + 49152 + hf * 8192 + 7168 + rep * 256;
                    unsigned v; unsigned long long t0, t1;
                    asm volatile("global_store_dword %3, %4, off sc1\n\ts_waitcnt vmcnt(0)\n\ts_memtime %0\n\ts_waitcnt lgkmcnt(0)\n\tglobal_load_dword %2, %3, off sc1\n\ts_waitcnt vmcnt(0)\n\ts_memtime %1\n\ts_waitcnt lgkmcnt(0)"
                                 : "=&s"(t0), "=&s"(t1), "=&v"(v) : "v"(q), "v"(SENT) : "memory");
                    best[hf] = min(best[hf], (unsigned)(t1 - t0));
                }
            par |= (best[1] < best[0] ? 1u : 0u) << pg;
        }
        if (lane == 0) { *(unsigned*)(p.ws + O_CTL) = par; *(unsigned*)(p.ws + O_CTL + 64) = 0; }
    }
}

extern "C" void kernel_launch(void* const* d_in, const int* in_sizes, int n_in, void* d_out, int out_size, void* d_ws, size_t ws_size, hipStream_t stream) {
    Params p;
    p.x = d_in[0]; p.mem0 = d_in[1]; p.wq = d_in[2]; p.wk = d_in[3]; p.wv = d_in[4]; p.wo = d_in[5];
    p.w1 = d_in[8]; p.b1 = d_in[9]; p.w2 = d_in[10]; p.out = d_out;
    p.ws = (char*)(((uintptr_t)d_ws + 2 * MB - 1) & ~(uintptr_t)(2 * MB - 1));
    k_pre<<<256, 256, 0, stream>>>(p);
    k_main<<<256, 256, 0, stream>>>(p);
}
```
